```python
import torch

def _softmax(x, axis=-1):
    return torch.softmax(x, dim=axis)
def _gelu(x):
    return torch.nn.functional.gelu(x, approximate="tanh")
def _silu(x):
    return torch.nn.functional.silu(x)
def _sigmoid(x):
    return torch.sigmoid(x)
def _layernorm(x, axis=-1, eps=1e-5):
    m = x.mean(dim=axis, keepdim=True)
    v = x.var(dim=axis, keepdim=True, unbiased=False)
    return (x - m) / torch.sqrt(v + eps)
def _scatter_add(tgt, idx, src, axis=0):
    return tgt.clone().index_add_(axis, idx, src)
def _segment_sum(src, seg, n):
    out = torch.zeros((n,) + src.shape[1:], dtype=src.dtype)
    return out.index_add_(0, seg, src)
def _pad(x, pw):
    flat = tuple(v for pair in reversed(pw) for v in pair)
    return torch.nn.functional.pad(x, flat)

kernel_name = 'sequential_preLN__compressed_kv_cla__dense_activation_relu__prompt'

def setup_inputs():

    _w = (lambda a: (lambda t: t.to(torch.bfloat16) if t.is_floating_point() else t)(torch.as_tensor(a)))
    def _A_setup():
        B, H, Sq, Sk, D, M = 1, 32, 2048, 2048, 128, 576
        Dc = 4 * D
        return {
            "x": _w((lambda *s: torch.randn(*s, dtype=torch.bfloat16) * 0.05)(B, Sq, M)),
            "c_kv": _w((lambda *s: torch.randn(*s, dtype=torch.bfloat16) * 0.4)(B, Sk, Dc)),
            "w_q": _w((lambda *s: torch.randn(*s, dtype=torch.bfloat16) * 3.2)(M, H, D) * (M ** -0.5)),
            "w_uk": _w((lambda *s: torch.randn(*s, dtype=torch.bfloat16) * 3.2)(Dc, H, D) * (Dc ** -0.5)),
            "w_uv": _w((lambda *s: torch.randn(*s, dtype=torch.bfloat16) * 0.4)(Dc, H, D) * (Dc ** -0.5)),
            "w_o": _w((lambda *s: torch.randn(*s, dtype=torch.bfloat16) * 0.4)(H, D, M) * ((H * D) ** -0.5)),
        }
    _A_in = _A_setup()

    def _B_setup():
        B, S, D, Dff = 1, 2048, 576, 4608
        return {
            "x": _w((lambda *s: torch.randn(*s, dtype=torch.bfloat16) * 0.05)(B, S, D)),
            "w1": _w((lambda *s: torch.randn(*s, dtype=torch.bfloat16) * 0.05)(D, Dff)),
            "w2": _w((lambda *s: torch.randn(*s, dtype=torch.bfloat16) * 0.05)(Dff, D)),
        }
    _B_in = _B_setup()

    return {
        "A_x": _A_in["x"],
        "A_c_kv": _A_in["c_kv"],
        "A_w_q": _A_in["w_q"],
        "A_w_uk": _A_in["w_uk"],
        "A_w_uv": _A_in["w_uv"],
        "A_w_o": _A_in["w_o"],
        "B_x": _B_in["x"],
        "B_w1": _B_in["w1"],
        "B_w2": _B_in["w2"],
    }

def reference(A_x, A_c_kv, A_w_q, A_w_uk, A_w_uv, A_w_o, B_x, B_w1, B_w2):
    def _A_ref(x, c_kv, w_q, w_uk, w_uv, w_o):
        B, Sq, M = x.shape[0], x.shape[1], x.shape[2]
        Sk = c_kv.shape[1]
        H, D = w_q.shape[1], w_q.shape[2]
        q = torch.einsum("bqm,mhd->bhqd", x, w_q)
        k = torch.einsum("bkc,chd->bhkd", c_kv, w_uk)
        v = torch.einsum("bkc,chd->bhkd", c_kv, w_uv)
        scores = torch.einsum("bhqd,bhkd->bhqk", q, k) * (D ** -0.5)
        off = Sk - Sq
        qi = torch.arange(Sq)[:, None]
        kj = torch.arange(Sk)[None, :]
        cmask = kj <= (qi + off)
        scores = torch.where(cmask[None, None], scores, -1e9)
        p = _softmax(scores, axis=-1)
        ctx = torch.einsum("bhqk,bhkd->bhqd", p, v)
        return torch.einsum("bhqd,hdm->bqm", ctx, w_o)

    def _B_ref(x, w1, w2):
        h = torch.einsum("bsd,df->bsf", x, w1)
        zero = torch.zeros_like(h)
        h = torch.maximum(h, zero)
        return torch.einsum("bsf,fd->bsd", h, w2)

    x = A_x
    h = x + _A_ref(_layernorm(x, axis=-1), A_c_kv, A_w_q, A_w_uk, A_w_uv, A_w_o)
    h2 = h + _B_ref(_layernorm(h, axis=-1), B_w1, B_w2)
    return h2
```

```cpp
#include <hip/hip_runtime.h>
#include <hip/hip_bf16.h>
typedef unsigned short u16;
__device__ inline float bf2f(u16 b){ return __uint_as_float(((unsigned)b)<<16); }
__device__ inline u16 f2bf(float f){ unsigned u=__float_as_uint(f); u += 0x7FFFu + ((u>>16)&1u); return (u16)(u>>16); }
constexpr int S=2048, M=576, H=32, D=128, DC=512, DFF=4608;

__global__ void ln_k(const u16* x, u16* y){
  int r=blockIdx.x, l=threadIdx.x; float v[9]; float s=0;
  for(int i=0;i<9;i++){ v[i]=bf2f(x[r*M+i*64+l]); s+=v[i]; }
  for(int o=32;o;o>>=1) s+=__shfl_xor(s,o);
  float m=s/M, q=0; for(int i=0;i<9;i++){ float d=v[i]-m; q+=d*d; }
  for(int o=32;o;o>>=1) q+=__shfl_xor(q,o);
  float rs=1.0f/sqrtf(q/M+1e-5f);
  for(int i=0;i<9;i++) y[r*M+i*64+l]=f2bf((v[i]-m)*rs);
}
__global__ void gemm_k(const u16* A,const u16* B,u16* C,const u16* res,int MM,int N,int K,int mode){
  __shared__ float As[16][17], Bs[16][17];
  int tx=threadIdx.x, ty=threadIdx.y; int r=blockIdx.y*16+ty, c=blockIdx.x*16+tx; float acc=0;
  for(int k0=0;k0<K;k0+=16){
    As[ty][tx]=bf2f(A[(size_t)r*K+k0+tx]); Bs[ty][tx]=bf2f(B[(size_t)(k0+ty)*N+c]); __syncthreads();
    for(int k=0;k<16;k++) acc+=As[ty][k]*Bs[k][tx];
    __syncthreads();
  }
  if(mode==1) acc=fmaxf(acc,0.f);
  if(mode==2) acc=bf2f(f2bf(acc))+bf2f(res[(size_t)r*N+c]);
  C[(size_t)r*N+c]=f2bf(acc);
}
__global__ void attn_k(const u16* q,const u16* k,const u16* v,u16* o){
  int r=blockIdx.x, h=blockIdx.y, t=threadIdx.x; __shared__ float qs[128]; __shared__ float p[2048]; __shared__ float red[128];
  qs[t]=bf2f(q[(size_t)r*4096+h*128+t]); __syncthreads();
  float mx=-1e30f;
  for(int j=t;j<=r;j+=128){ float s=0; for(int d=0;d<128;d++) s+=qs[d]*bf2f(k[(size_t)j*4096+h*128+d]); s*=0.08838834764831845f; p[j]=s; mx=fmaxf(mx,s);}
  red[t]=mx; __syncthreads(); for(int o2=64;o2;o2>>=1){ if(t<o2) red[t]=fmaxf(red[t],red[t+o2]); __syncthreads(); } mx=red[0]; __syncthreads();
  float sum=0; for(int j=t;j<=r;j+=128){ float e=expf(p[j]-mx); p[j]=e; sum+=e; }
  red[t]=sum; __syncthreads(); for(int o2=64;o2;o2>>=1){ if(t<o2) red[t]+=red[t+o2]; __syncthreads(); } sum=red[0];
  float acc=0; for(int j=0;j<=r;j++) acc+=p[j]*bf2f(v[(size_t)j*4096+h*128+t]);
  o[(size_t)r*4096+h*128+t]=f2bf(acc/sum);
}
extern "C" void kernel_launch(void* const* d_in, const int* in_sizes, int n_in, void* d_out, int out_size, void* d_ws, size_t ws_size, hipStream_t stream){
  const u16* x=(const u16*)d_in[0]; const u16* ckv=(const u16*)d_in[1]; const u16* wq=(const u16*)d_in[2]; const u16* wuk=(const u16*)d_in[3];
  const u16* wuv=(const u16*)d_in[4]; const u16* wo=(const u16*)d_in[5]; const u16* w1=(const u16*)d_in[7]; const u16* w2=(const u16*)d_in[8];
  u16* ws=(u16*)d_ws; u16* ln1=ws; u16* q=ln1+S*M; u16* k=q+S*4096; u16* v=k+S*4096; u16* ctx=v+S*4096; u16* h=ctx+S*4096; u16* ln2=h+S*M; u16* h1=ln2+S*M;
  ln_k<<<S,64,0,stream>>>(x,ln1);
  gemm_k<<<dim3(4096/16,S/16),dim3(16,16),0,stream>>>(ln1,wq,q,nullptr,S,4096,M,0);
  gemm_k<<<dim3(4096/16,S/16),dim3(16,16),0,stream>>>(ckv,wuk,k,nullptr,S,4096,DC,0);
  gemm_k<<<dim3(4096/16,S/16),dim3(16,16),0,stream>>>(ckv,wuv,v,nullptr,S,4096,DC,0);
  attn_k<<<dim3(S,H),128,0,stream>>>(q,k,v,ctx);
  gemm_k<<<dim3(M/16,S/16),dim3(16,16),0,stream>>>(ctx,wo,h,x,S,M,4096,2);
  ln_k<<<S,64,0,stream>>>(h,ln2);
  gemm_k<<<dim3(DFF/16,S/16),dim3(16,16),0,stream>>>(ln2,w1,h1,nullptr,S,DFF,M,1);
  gemm_k<<<dim3(M/16,S/16),dim3(16,16),0,stream>>>(h1,w2,(u16*)d_out,h,S,M,DFF,2);
}
```

```cpp
#include <hip/hip_runtime.h>
#include <hip/hip_bf16.h>
#include <stdint.h>
#define LDS3 __attribute__((address_space(3)))
typedef unsigned u32x2 __attribute__((ext_vector_type(2)));
typedef unsigned u32x4 __attribute__((ext_vector_type(4)));
typedef float f32x2 __attribute__((ext_vector_type(2)));
typedef float f32x16 __attribute__((ext_vector_type(16)));
typedef short bf16x8 __attribute__((ext_vector_type(8)));
typedef __bf16 bf16x2v __attribute__((ext_vector_type(2)));
__device__ __forceinline__ unsigned cvtpk(float lo, float hi){ f32x2 v={lo,hi}; bf16x2v b=__builtin_convertvector(v,bf16x2v); return __builtin_bit_cast(unsigned,b); }
__device__ __forceinline__ float bflo(unsigned w){ return __uint_as_float(w<<16); }
__device__ __forceinline__ float bfhi(unsigned w){ return __uint_as_float(w&0xffff0000u); }
__device__ __forceinline__ void st16(void* p, u32x4 v){ asm volatile("global_store_dwordx4 %0, %1, off sc1\n\ts_nop 1" :: "v"(p), "v"(v) : "memory"); }
__device__ __forceinline__ void st8(void* p, u32x2 v){ asm volatile("global_store_dwordx2 %0, %1, off sc1" :: "v"(p), "v"(v) : "memory"); }
__device__ __forceinline__ void lds_barrier(){ asm volatile("s_waitcnt lgkmcnt(0)\n\ts_barrier" ::: "memory"); }
__device__ __forceinline__ int lane_id(){ int l; asm volatile("v_mbcnt_lo_u32_b32 %0, -1, 0\n\tv_mbcnt_hi_u32_b32 %0, -1, %0" : "=v"(l)); return l; }
__device__ __forceinline__ unsigned prefetch_line(const char* p){ unsigned t; asm volatile("global_load_dword %0, %1, off" : "=&v"(t) : "v"(p) : "memory"); return t; }
#define CODE_ADDR(sym,out) asm volatile("s_getpc_b64 s[20:21]\n\ts_add_u32 s20, s20, " #sym "@rel32@lo+4\n\ts_addc_u32 s21, s21, " #sym "@rel32@hi+12\n\ts_mov_b64 %0, s[20:21]" : "=s"(out) :: "s20","s21","scc")
constexpr int CODE_QKV=19008, CODE_ATTN=11904, CODE_OPROJ=12160, CODE_FFN1=8512, CODE_FFN2=10816;

struct PtrTab { const char* wuk; const char* wuv; const char* wq; const char* wo; const char* w1; const char* w2; char* ws; char* out; };
__device__ PtrTab g_tab;
typedef unsigned u32x16 __attribute__((ext_vector_type(16)));
__device__ __forceinline__ PtrTab load_tab(){
  uint64_t a; CODE_ADDR(g_tab,a);
  u32x16 t; asm volatile("s_load_dwordx16 %0, %1, 0x0\n\ts_waitcnt lgkmcnt(0)" : "=s"(t) : "s"(a) : "memory");
#define TABP(i) ((char*)(((uint64_t)t[2*(i)+1]<<32)|t[2*(i)]))
  PtrTab r; r.wuk=TABP(0); r.wuv=TABP(1); r.wq=TABP(2); r.wo=TABP(3); r.w1=TABP(4); r.w2=TABP(5); r.ws=TABP(6); r.out=TABP(7);
#undef TABP
  return r;
}

constexpr size_t AFB=(size_t)128*18*1024;
constexpr size_t WS_CNT=0, WS_LN1=8192, WS_XAF=WS_LN1+AFB, WS_CKV=WS_XAF+AFB, WS_Q=WS_CKV+(size_t)128*16*1024, WS_K=WS_Q+((size_t)16<<20), WS_V=WS_K+((size_t)16<<20),
                 WS_CTX=WS_V+((size_t)16<<20), WS_P=WS_CTX+((size_t)16<<20), WS_H=WS_P+8*AFB, WS_LN2=WS_H+AFB, WS_H1=WS_LN2+AFB, WS_P2=WS_H1+(size_t)128*144*1024, WS_END=WS_P2+8*AFB;
constexpr int NCNT=40;

__device__ __forceinline__ void arrive_and_wait(unsigned* cnt, unsigned need, int wid, int lane){
  asm volatile("s_waitcnt vmcnt(0)" ::: "memory");
  __syncthreads();
  if(wid==0){
    if(lane==0) __hip_atomic_fetch_add(cnt,1u,__ATOMIC_RELAXED,__HIP_MEMORY_SCOPE_AGENT);
    const unsigned long long t0=__builtin_amdgcn_s_memrealtime();
    for(;;){
      const unsigned v=__builtin_amdgcn_readfirstlane(__hip_atomic_load(cnt,__ATOMIC_RELAXED,__HIP_MEMORY_SCOPE_AGENT));
      if(v>=need) break;
      if(__builtin_amdgcn_s_memrealtime()-t0>400000ull) break;
      __builtin_amdgcn_s_sleep(1);
    }
  }
  asm volatile("s_barrier" ::: "memory");
}
#define ATTN_ASM_TEXT \
  "s_mov_b64 s[76:77], %[qf]\n\t" \
  "s_mov_b64 s[78:79], %[cf]\n\t" \
  "s_mov_b32 s80, %[head]\n\t" \
  "s_mov_b32 s81, %[pair]\n\t" \
  "s_mov_b32 s82, %[wid]\n\t" \
  "s_mov_b32 s74, %[lds0]\n\t" \
  "s_lshl_b32 s58, s80, 19\n\t" \
  "s_mov_b64 s[60:61], %[kf]\n\t" \
  "s_add_u32 s36, s60, s58\n\t" \
  "s_addc_u32 s37, s61, 0\n\t" \
  "s_and_b32 s37, s37, 0xffff\n\t" \
  "s_mov_b32 s38, 0x80000\n\t" \
  "s_mov_b32 s39, 0x20000\n\t" \
  "s_mov_b64 s[60:61], %[vf]\n\t" \
  "s_add_u32 s40, s60, s58\n\t" \
  "s_addc_u32 s41, s61, 0\n\t" \
  "s_and_b32 s41, s41, 0xffff\n\t" \
  "s_mov_b32 s42, 0x80000\n\t" \
  "s_mov_b32 s43, 0x20000\n\t" \
  "s_and_b32 s57, s82, 3\n\t" \
  "s_lshr_b32 s56, s82, 2\n\t" \
  "s_mov_b32 s72, 0\n\t" \
  "v_mbcnt_lo_u32_b32 v223, -1, 0\n\t" \
  "v_mbcnt_hi_u32_b32 v223, -1, v223\n\t" \
  "v_lshlrev_b32_e32 v226, 4, v223\n\t" \
  "s_lshl_b32 s58, s57, 13\n\t" \
  "v_add_u32_e32 v220, s58, v226\n\t" \
  "s_add_u32 s50, s74, s58\n\t" \
  "s_add_u32 s51, s50, 65536\n\t" \
  "s_sub_u32 s75, 15, s81\n\t" \
  "s_cmp_eq_u32 s56, 0\n\t" \
  "s_cselect_b32 s73, s75, s81\n\t" \
  "s_cmp_eq_u32 s56, 0\n\t" \
  "s_cbranch_scc1 L_p0%=\n\t" \
  "s_lshl_b32 s58, s57, 12\n\t" \
  "v_add_u32_e32 v228, s58, v226\n\t" \
  "s_add_u32 s58, s58, s74\n\t" \
  "s_add_u32 m0, s58, 49152\n\t" \
  "s_nop 0\n\t" \
  "buffer_load_dwordx4 v228, s[36:39], 0 offen offset:0 lds\n\t" \
  "buffer_load_dwordx4 v228, s[36:39], 0 offen offset:1024 lds\n\t" \
  "buffer_load_dwordx4 v228, s[36:39], 0 offen offset:2048 lds\n\t" \
  "buffer_load_dwordx4 v228, s[36:39], 0 offen offset:3072 lds\n\t" \
  "s_mov_b32 s62, 16384\n\t" \
  "s_add_u32 m0, s50, 0\n\t" \
  "s_add_u32 s66, s62, 4096\n\t" \
  "buffer_load_dwordx4 v220, s[36:39], s62 offen offset:0 lds\n\t" \
  "buffer_load_dwordx4 v220, s[36:39], s62 offen offset:1024 lds\n\t" \
  "buffer_load_dwordx4 v220, s[36:39], s62 offen offset:2048 lds\n\t" \
  "buffer_load_dwordx4 v220, s[36:39], s62 offen offset:3072 lds\n\t" \
  "s_add_u32 m0, m0, 4096\n\t" \
  "s_nop 0\n\t" \
  "buffer_load_dwordx4 v220, s[36:39], s66 offen offset:0 lds\n\t" \
  "buffer_load_dwordx4 v220, s[36:39], s66 offen offset:1024 lds\n\t" \
  "buffer_load_dwordx4 v220, s[36:39], s66 offen offset:2048 lds\n\t" \
  "buffer_load_dwordx4 v220, s[36:39], s66 offen offset:3072 lds\n\t" \
  "s_mov_b32 s62, 0\n\t" \
  "s_add_u32 m0, s51, 0\n\t" \
  "s_add_u32 s66, s62, 4096\n\t" \
  "buffer_load_dwordx4 v220, s[40:43], s62 offen offset:0 lds\n\t" \
  "buffer_load_dwordx4 v220, s[40:43], s62 offen offset:1024 lds\n\t" \
  "buffer_load_dwordx4 v220, s[40:43], s62 offen offset:2048 lds\n\t" \
  "buffer_load_dwordx4 v220, s[40:43], s62 offen offset:3072 lds\n\t" \
  "s_add_u32 m0, m0, 4096\n\t" \
  "s_nop 0\n\t" \
  "buffer_load_dwordx4 v220, s[40:43], s66 offen offset:0 lds\n\t" \
  "buffer_load_dwordx4 v220, s[40:43], s66 offen offset:1024 lds\n\t" \
  "buffer_load_dwordx4 v220, s[40:43], s66 offen offset:2048 lds\n\t" \
  "buffer_load_dwordx4 v220, s[40:43], s66 offen offset:3072 lds\n\t" \
  "L_p0%=:\n\t" \
  "s_lshl_b32 s60, s80, 6\n\t" \
  "s_lshl_b32 s61, s73, 2\n\t" \
  "s_add_u32 s60, s60, s61\n\t" \
  "s_add_u32 s60, s60, s57\n\t" \
  "s_lshl_b32 s60, s60, 13\n\t" \
  "s_add_u32 s44, s76, s60\n\t" \
  "s_addc_u32 s45, s77, 0\n\t" \
  "s_add_u32 s46, s44, 4096\n\t" \
  "s_addc_u32 s47, s45, 0\n\t" \
  "s_cmp_eq_u32 s56, 0\n\t" \
  "s_cbranch_scc0 L_q0%=\n\t" \
  "global_load_dwordx4 v[112:115], v226, s[44:45] offset:0\n\t" \
  "global_load_dwordx4 v[116:119], v226, s[44:45] offset:1024\n\t" \
  "global_load_dwordx4 v[120:123], v226, s[44:45] offset:2048\n\t" \
  "global_load_dwordx4 v[124:127], v226, s[44:45] offset:3072\n\t" \
  "global_load_dwordx4 v[128:131], v226, s[46:47] offset:0\n\t" \
  "global_load_dwordx4 v[132:135], v226, s[46:47] offset:1024\n\t" \
  "global_load_dwordx4 v[136:139], v226, s[46:47] offset:2048\n\t" \
  "global_load_dwordx4 v[140:143], v226, s[46:47] offset:3072\n\t" \
  "L_q0%=:\n\t" \
  "v_mov_b32_e32 v224, 0xff800000\n\t" \
  "v_add_u32_e32 v218, s74, v226\n\t" \
  "v_add_u32_e32 v219, 65536, v218\n\t" \
  "v_and_b32_e32 v228, 31, v223\n\t" \
  "v_lshrrev_b32_e32 v229, 5, v223\n\t" \
  "v_lshlrev_b32_e32 v230, 2, v229\n\t" \
  "v_sub_u32_e32 v222, v228, v230\n\t" \
  "v_lshrrev_b32_e32 v230, 4, v228\n\t" \
  "v_lshlrev_b32_e32 v230, 17, v230\n\t" \
  "v_lshlrev_b32_e32 v231, 8, v229\n\t" \
  "v_and_b32_e32 v232, 15, v228\n\t" \
  "v_lshlrev_b32_e32 v232, 4, v232\n\t" \
  "v_add3_u32 v225, v230, v231, v232\n\t" \
  "v_mov_b32_e32 v0, 0\n\t" \
  "v_mov_b32_e32 v1, 0\n\t" \
  "v_mov_b32_e32 v2, 0\n\t" \
  "v_mov_b32_e32 v3, 0\n\t" \
  "v_mov_b32_e32 v4, 0\n\t" \
  "v_mov_b32_e32 v5, 0\n\t" \
  "v_mov_b32_e32 v6, 0\n\t" \
  "v_mov_b32_e32 v7, 0\n\t" \
  "v_mov_b32_e32 v8, 0\n\t" \
  "v_mov_b32_e32 v9, 0\n\t" \
  "v_mov_b32_e32 v10, 0\n\t" \
  "v_mov_b32_e32 v11, 0\n\t" \
  "v_mov_b32_e32 v12, 0\n\t" \
  "v_mov_b32_e32 v13, 0\n\t" \
  "v_mov_b32_e32 v14, 0\n\t" \
  "v_mov_b32_e32 v15, 0\n\t" \
  "v_mov_b32_e32 v16, 0\n\t" \
  "v_mov_b32_e32 v17, 0\n\t" \
  "v_mov_b32_e32 v18, 0\n\t" \
  "v_mov_b32_e32 v19, 0\n\t" \
  "v_mov_b32_e32 v20, 0\n\t" \
  "v_mov_b32_e32 v21, 0\n\t" \
  "v_mov_b32_e32 v22, 0\n\t" \
  "v_mov_b32_e32 v23, 0\n\t" \
  "v_mov_b32_e32 v24, 0\n\t" \
  "v_mov_b32_e32 v25, 0\n\t" \
  "v_mov_b32_e32 v26, 0\n\t" \
  "v_mov_b32_e32 v27, 0\n\t" \
  "v_mov_b32_e32 v28, 0\n\t" \
  "v_mov_b32_e32 v29, 0\n\t" \
  "v_mov_b32_e32 v30, 0\n\t" \
  "v_mov_b32_e32 v31, 0\n\t" \
  "v_mov_b32_e32 v32, 0\n\t" \
  "v_mov_b32_e32 v33, 0\n\t" \
  "v_mov_b32_e32 v34, 0\n\t" \
  "v_mov_b32_e32 v35, 0\n\t" \
  "v_mov_b32_e32 v36, 0\n\t" \
  "v_mov_b32_e32 v37, 0\n\t" \
  "v_mov_b32_e32 v38, 0\n\t" \
  "v_mov_b32_e32 v39, 0\n\t" \
  "v_mov_b32_e32 v40, 0\n\t" \
  "v_mov_b32_e32 v41, 0\n\t" \
  "v_mov_b32_e32 v42, 0\n\t" \
  "v_mov_b32_e32 v43, 0\n\t" \
  "v_mov_b32_e32 v44, 0\n\t" \
  "v_mov_b32_e32 v45, 0\n\t" \
  "v_mov_b32_e32 v46, 0\n\t" \
  "v_mov_b32_e32 v47, 0\n\t" \
  "v_mov_b32_e32 v48, 0\n\t" \
  "v_mov_b32_e32 v49, 0\n\t" \
  "v_mov_b32_e32 v50, 0\n\t" \
  "v_mov_b32_e32 v51, 0\n\t" \
  "v_mov_b32_e32 v52, 0\n\t" \
  "v_mov_b32_e32 v53, 0\n\t" \
  "v_mov_b32_e32 v54, 0\n\t" \
  "v_mov_b32_e32 v55, 0\n\t" \
  "v_mov_b32_e32 v56, 0\n\t" \
  "v_mov_b32_e32 v57, 0\n\t" \
  "v_mov_b32_e32 v58, 0\n\t" \
  "v_mov_b32_e32 v59, 0\n\t" \
  "v_mov_b32_e32 v60, 0\n\t" \
  "v_mov_b32_e32 v61, 0\n\t" \
  "v_mov_b32_e32 v62, 0\n\t" \
  "v_mov_b32_e32 v63, 0\n\t" \
  "s_lshl_b32 s54, s73, 2\n\t" \
  "s_add_u32 s54, s54, s57\n\t" \
  "s_add_u32 s54, s54, 1\n\t" \
  "s_cmp_eq_u32 s54, 1\n\t" \
  "s_cselect_b32 s58, 0, 1000\n\t" \
  "v_add_u32_e32 v228, s58, v222\n\t" \
  "v_cmp_gt_i32_e64 s[84:85], 0, v228\n\t" \
  "v_cmp_gt_i32_e64 s[86:87], 1, v228\n\t" \
  "v_cmp_gt_i32_e64 s[88:89], 2, v228\n\t" \
  "v_cmp_gt_i32_e64 s[90:91], 3, v228\n\t" \
  "s_nop 0\n\t" \
  "v_cndmask_b32_e64 v64, 0, v224, s[84:85]\n\t" \
  "v_cndmask_b32_e64 v65, 0, v224, s[86:87]\n\t" \
  "v_cndmask_b32_e64 v66, 0, v224, s[88:89]\n\t" \
  "v_cndmask_b32_e64 v67, 0, v224, s[90:91]\n\t" \
  "v_cmp_gt_i32_e64 s[84:85], 16, v228\n\t" \
  "v_cmp_gt_i32_e64 s[86:87], 17, v228\n\t" \
  "v_cmp_gt_i32_e64 s[88:89], 18, v228\n\t" \
  "v_cmp_gt_i32_e64 s[90:91], 19, v228\n\t" \
  "s_nop 0\n\t" \
  "v_cndmask_b32_e64 v68, 0, v224, s[84:85]\n\t" \
  "v_cndmask_b32_e64 v69, 0, v224, s[86:87]\n\t" \
  "v_cndmask_b32_e64 v70, 0, v224, s[88:89]\n\t" \
  "v_cndmask_b32_e64 v71, 0, v224, s[90:91]\n\t" \
  "v_cmp_gt_i32_e64 s[84:85], 8, v228\n\t" \
  "v_cmp_gt_i32_e64 s[86:87], 9, v228\n\t" \
  "v_cmp_gt_i32_e64 s[88:89], 10, v228\n\t" \
  "v_cmp_gt_i32_e64 s[90:91], 11, v228\n\t" \
  "s_nop 0\n\t" \
  "v_cndmask_b32_e64 v72, 0, v224, s[84:85]\n\t" \
  "v_cndmask_b32_e64 v73, 0, v224, s[86:87]\n\t" \
  "v_cndmask_b32_e64 v74, 0, v224, s[88:89]\n\t" \
  "v_cndmask_b32_e64 v75, 0, v224, s[90:91]\n\t" \
  "v_cmp_gt_i32_e64 s[84:85], 24, v228\n\t" \
  "v_cmp_gt_i32_e64 s[86:87], 25, v228\n\t" \
  "v_cmp_gt_i32_e64 s[88:89], 26, v228\n\t" \
  "v_cmp_gt_i32_e64 s[90:91], 27, v228\n\t" \
  "s_nop 0\n\t" \
  "v_cndmask_b32_e64 v76, 0, v224, s[84:85]\n\t" \
  "v_cndmask_b32_e64 v77, 0, v224, s[86:87]\n\t" \
  "v_cndmask_b32_e64 v78, 0, v224, s[88:89]\n\t" \
  "v_cndmask_b32_e64 v79, 0, v224, s[90:91]\n\t" \
  "s_mov_b32 s52, 0\n\t" \
  "s_mov_b32 s53, 0\n\t" \
  "s_cmp_eq_u32 s56, 0\n\t" \
  "s_cbranch_scc0 L_np%=\n\t" \
  "s_setprio 1\n\t" \
  "L_np%=:\n\t" \
  "s_cmp_eq_u32 s56, 0\n\t" \
  "s_cbranch_scc1 L_b1%=\n\t" \
  "s_waitcnt vmcnt(16)\n\t" \
  "L_b1%=:\n\t" \
  "s_barrier\n\t" \
  "v_add_u32_e32 v227, 49152, v218\n\t" \
  "ds_read_b128 v[144:147], v227 offset:0\n\t" \
  "ds_read_b128 v[148:151], v227 offset:1024\n\t" \
  "ds_read_b128 v[152:155], v227 offset:2048\n\t" \
  "ds_read_b128 v[156:159], v227 offset:3072\n\t" \
  "ds_read_b128 v[160:163], v227 offset:4096\n\t" \
  "ds_read_b128 v[164:167], v227 offset:5120\n\t" \
  "ds_read_b128 v[168:171], v227 offset:6144\n\t" \
  "ds_read_b128 v[172:175], v227 offset:7168\n\t" \
  "ds_read_b128 v[176:179], v227 offset:8192\n\t" \
  "ds_read_b128 v[180:183], v227 offset:9216\n\t" \
  "ds_read_b128 v[184:187], v227 offset:10240\n\t" \
  "ds_read_b128 v[188:191], v227 offset:11264\n\t" \
  "ds_read_b128 v[192:195], v227 offset:12288\n\t" \
  "ds_read_b128 v[196:199], v227 offset:13312\n\t" \
  "ds_read_b128 v[200:203], v227 offset:14336\n\t" \
  "ds_read_b128 v[204:207], v227 offset:15360\n\t" \
  "s_cmp_eq_u32 s56, 0\n\t" \
  "s_cbranch_scc1 L_p1%=\n\t" \
  "global_load_dwordx4 v[112:115], v226, s[44:45] offset:0\n\t" \
  "global_load_dwordx4 v[116:119], v226, s[44:45] offset:1024\n\t" \
  "global_load_dwordx4 v[120:123], v226, s[44:45] offset:2048\n\t" \
  "global_load_dwordx4 v[124:127], v226, s[44:45] offset:3072\n\t" \
  "global_load_dwordx4 v[128:131], v226, s[46:47] offset:0\n\t" \
  "global_load_dwordx4 v[132:135], v226, s[46:47] offset:1024\n\t" \
  "global_load_dwordx4 v[136:139], v226, s[46:47] offset:2048\n\t" \
  "global_load_dwordx4 v[140:143], v226, s[46:47] offset:3072\n\t" \
  "s_waitcnt vmcnt(8) lgkmcnt(0)\n\t" \
  "s_barrier\n\t" \
  "s_cmp_ge_u32 s53, s75\n\t" \
  "s_cbranch_scc1 L_nodmaP%=\n\t" \
  "s_add_u32 s58, s53, 1\n\t" \
  "s_lshl_b32 s62, s58, 15\n\t" \
  "s_add_u32 s61, s62, 16384\n\t" \
  "s_xor_b32 s63, s52, 0x8000\n\t" \
  "s_add_u32 m0, s50, s63\n\t" \
  "s_add_u32 s66, s61, 4096\n\t" \
  "buffer_load_dwordx4 v220, s[36:39], s61 offen offset:0 lds\n\t" \
  "buffer_load_dwordx4 v220, s[36:39], s61 offen offset:1024 lds\n\t" \
  "buffer_load_dwordx4 v220, s[36:39], s61 offen offset:2048 lds\n\t" \
  "buffer_load_dwordx4 v220, s[36:39], s61 offen offset:3072 lds\n\t" \
  "s_add_u32 m0, m0, 4096\n\t" \
  "s_nop 0\n\t" \
  "buffer_load_dwordx4 v220, s[36:39], s66 offen offset:0 lds\n\t" \
  "buffer_load_dwordx4 v220, s[36:39], s66 offen offset:1024 lds\n\t" \
  "buffer_load_dwordx4 v220, s[36:39], s66 offen offset:2048 lds\n\t" \
  "buffer_load_dwordx4 v220, s[36:39], s66 offen offset:3072 lds\n\t" \
  "s_add_u32 m0, s51, s63\n\t" \
  "s_add_u32 s66, s62, 4096\n\t" \
  "buffer_load_dwordx4 v220, s[40:43], s62 offen offset:0 lds\n\t" \
  "buffer_load_dwordx4 v220, s[40:43], s62 offen offset:1024 lds\n\t" \
  "buffer_load_dwordx4 v220, s[40:43], s62 offen offset:2048 lds\n\t" \
  "buffer_load_dwordx4 v220, s[40:43], s62 offen offset:3072 lds\n\t" \
  "s_add_u32 m0, m0, 4096\n\t" \
  "s_nop 0\n\t" \
  "buffer_load_dwordx4 v220, s[40:43], s66 offen offset:0 lds\n\t" \
  "buffer_load_dwordx4 v220, s[40:43], s66 offen offset:1024 lds\n\t" \
  "buffer_load_dwordx4 v220, s[40:43], s66 offen offset:2048 lds\n\t" \
  "buffer_load_dwordx4 v220, s[40:43], s66 offen offset:3072 lds\n\t" \
  "L_nodmaP%=:\n\t" \
  "s_waitcnt vmcnt(16)\n\t" \
  "s_branch L_p2%=\n\t" \
  "L_p1%=:\n\t" \
  "s_waitcnt vmcnt(0) lgkmcnt(0)\n\t" \
  "L_p2%=:\n\t" \
  "v_mfma_f32_32x32x16_bf16 v[64:79], v[144:147], v[112:115], v[64:79]\n\t" \
  "v_mfma_f32_32x32x16_bf16 v[64:79], v[148:151], v[116:119], v[64:79]\n\t" \
  "v_mfma_f32_32x32x16_bf16 v[64:79], v[152:155], v[120:123], v[64:79]\n\t" \
  "v_mfma_f32_32x32x16_bf16 v[64:79], v[156:159], v[124:127], v[64:79]\n\t" \
  "v_mfma_f32_32x32x16_bf16 v[64:79], v[160:163], v[128:131], v[64:79]\n\t" \
  "v_mfma_f32_32x32x16_bf16 v[64:79], v[164:167], v[132:135], v[64:79]\n\t" \
  "v_mfma_f32_32x32x16_bf16 v[64:79], v[168:171], v[136:139], v[64:79]\n\t" \
  "v_mfma_f32_32x32x16_bf16 v[64:79], v[172:175], v[140:143], v[64:79]\n\t" \
  "v_mov_b32_e32 v144, v176\n\t" \
  "v_mov_b32_e32 v145, v177\n\t" \
  "v_mov_b32_e32 v146, v178\n\t" \
  "v_mov_b32_e32 v147, v179\n\t" \
  "v_mov_b32_e32 v148, v180\n\t" \
  "v_mov_b32_e32 v149, v181\n\t" \
  "v_mov_b32_e32 v150, v182\n\t" \
  "v_mov_b32_e32 v151, v183\n\t" \
  "v_mov_b32_e32 v152, v184\n\t" \
  "v_mov_b32_e32 v153, v185\n\t" \
  "v_mov_b32_e32 v154, v186\n\t" \
  "v_mov_b32_e32 v155, v187\n\t" \
  "v_mov_b32_e32 v156, v188\n\t" \
  "v_mov_b32_e32 v157, v189\n\t" \
  "v_mov_b32_e32 v158, v190\n\t" \
  "v_mov_b32_e32 v159, v191\n\t" \
  "v_mov_b32_e32 v160, v192\n\t" \
  "v_mov_b32_e32 v161, v193\n\t" \
  "v_mov_b32_e32 v162, v194\n\t" \
  "v_mov_b32_e32 v163, v195\n\t" \
  "v_mov_b32_e32 v164, v196\n\t" \
  "v_mov_b32_e32 v165, v197\n\t" \
  "v_mov_b32_e32 v166, v198\n\t" \
  "v_mov_b32_e32 v167, v199\n\t" \
  "v_mov_b32_e32 v168, v200\n\t" \
  "v_mov_b32_e32 v169, v201\n\t" \
  "v_mov_b32_e32 v170, v202\n\t" \
  "v_mov_b32_e32 v171, v203\n\t" \
  "v_mov_b32_e32 v172, v204\n\t" \
  "v_mov_b32_e32 v173, v205\n\t" \
  "v_mov_b32_e32 v174, v206\n\t" \
  "v_mov_b32_e32 v175, v207\n\t" \
  "v_max3_f32 v229, v64, v65, v66\n\t" \
  "v_max3_f32 v230, v67, v68, v69\n\t" \
  "v_max3_f32 v229, v229, v70, v71\n\t" \
  "v_max3_f32 v230, v230, v72, v73\n\t" \
  "v_max3_f32 v229, v229, v74, v75\n\t" \
  "v_max3_f32 v230, v230, v76, v77\n\t" \
  "v_max3_f32 v229, v229, v78, v79\n\t" \
  "v_max_f32_e32 v229, v229, v230\n\t" \
  "v_mov_b32_e32 v230, v229\n\t" \
  "s_nop 1\n\t" \
  "v_permlane32_swap_b32_e32 v229, v230\n\t" \
  "s_nop 1\n\t" \
  "v_max_f32_e32 v229, v229, v230\n\t" \
  "v_sub_f32_e32 v221, 0, v229\n\t" \
  "v_add_f32_e32 v64, v64, v221\n\t" \
  "v_add_f32_e32 v65, v65, v221\n\t" \
  "v_add_f32_e32 v66, v66, v221\n\t" \
  "v_add_f32_e32 v67, v67, v221\n\t" \
  "v_add_f32_e32 v68, v68, v221\n\t" \
  "v_add_f32_e32 v69, v69, v221\n\t" \
  "v_add_f32_e32 v70, v70, v221\n\t" \
  "v_add_f32_e32 v71, v71, v221\n\t" \
  "v_add_f32_e32 v72, v72, v221\n\t" \
  "v_add_f32_e32 v73, v73, v221\n\t" \
  "v_add_f32_e32 v74, v74, v221\n\t" \
  "v_add_f32_e32 v75, v75, v221\n\t" \
  "v_add_f32_e32 v76, v76, v221\n\t" \
  "v_add_f32_e32 v77, v77, v221\n\t" \
  "v_add_f32_e32 v78, v78, v221\n\t" \
  "v_add_f32_e32 v79, v79, v221\n\t" \
  "v_mov_b32_e32 v96, v221\n\t" \
  "v_mov_b32_e32 v97, v221\n\t" \
  "v_mov_b32_e32 v98, v221\n\t" \
  "v_mov_b32_e32 v99, v221\n\t" \
  "v_mov_b32_e32 v100, v221\n\t" \
  "v_mov_b32_e32 v101, v221\n\t" \
  "v_mov_b32_e32 v102, v221\n\t" \
  "v_mov_b32_e32 v103, v221\n\t" \
  "v_mov_b32_e32 v104, v221\n\t" \
  "v_mov_b32_e32 v105, v221\n\t" \
  "v_mov_b32_e32 v106, v221\n\t" \
  "v_mov_b32_e32 v107, v221\n\t" \
  "v_mov_b32_e32 v108, v221\n\t" \
  "v_mov_b32_e32 v109, v221\n\t" \
  "v_mov_b32_e32 v110, v221\n\t" \
  "v_mov_b32_e32 v111, v221\n\t" \
  "v_exp_f32_e32 v64, v64\n\t" \
  "v_exp_f32_e32 v65, v65\n\t" \
  "v_exp_f32_e32 v66, v66\n\t" \
  "v_exp_f32_e32 v67, v67\n\t" \
  "v_exp_f32_e32 v68, v68\n\t" \
  "v_exp_f32_e32 v69, v69\n\t" \
  "s_nop 0\n\t" \
  "v_add_f32_e32 v216, v64, v66\n\t" \
  "v_add_f32_e32 v217, v65, v67\n\t" \
  "v_add_f32_e32 v216, v216, v68\n\t" \
  "v_cvt_pk_bf16_f32 v208, v64, v65\n\t" \
  "v_cvt_pk_bf16_f32 v209, v66, v67\n\t" \
  "s_cmp_eq_u32 s56, 0\n\t" \
  "s_cbranch_scc0 L_p3%=\n\t" \
  "s_barrier\n\t" \
  "L_p3%=:\n\t" \
  "s_branch L_step2%=\n\t" \
  "L_step%=:\n\t" \
  "s_cmp_eq_u32 s56, 0\n\t" \
  "s_cbranch_scc1 L_step2%=\n\t" \
  "s_cmp_ge_u32 s53, s75\n\t" \
  "s_cbranch_scc1 L_nodmaS%=\n\t" \
  "s_add_u32 s58, s53, 1\n\t" \
  "s_lshl_b32 s62, s58, 15\n\t" \
  "s_add_u32 s61, s62, 16384\n\t" \
  "s_xor_b32 s63, s52, 0x8000\n\t" \
  "s_add_u32 m0, s50, s63\n\t" \
  "s_add_u32 s66, s61, 4096\n\t" \
  "buffer_load_dwordx4 v220, s[36:39], s61 offen offset:0 lds\n\t" \
  "buffer_load_dwordx4 v220, s[36:39], s61 offen offset:1024 lds\n\t" \
  "buffer_load_dwordx4 v220, s[36:39], s61 offen offset:2048 lds\n\t" \
  "buffer_load_dwordx4 v220, s[36:39], s61 offen offset:3072 lds\n\t" \
  "s_add_u32 m0, m0, 4096\n\t" \
  "s_nop 0\n\t" \
  "buffer_load_dwordx4 v220, s[36:39], s66 offen offset:0 lds\n\t" \
  "buffer_load_dwordx4 v220, s[36:39], s66 offen offset:1024 lds\n\t" \
  "buffer_load_dwordx4 v220, s[36:39], s66 offen offset:2048 lds\n\t" \
  "buffer_load_dwordx4 v220, s[36:39], s66 offen offset:3072 lds\n\t" \
  "s_add_u32 m0, s51, s63\n\t" \
  "s_add_u32 s66, s62, 4096\n\t" \
  "buffer_load_dwordx4 v220, s[40:43], s62 offen offset:0 lds\n\t" \
  "buffer_load_dwordx4 v220, s[40:43], s62 offen offset:1024 lds\n\t" \
  "buffer_load_dwordx4 v220, s[40:43], s62 offen offset:2048 lds\n\t" \
  "buffer_load_dwordx4 v220, s[40:43], s62 offen offset:3072 lds\n\t" \
  "s_add_u32 m0, m0, 4096\n\t" \
  "s_nop 0\n\t" \
  "buffer_load_dwordx4 v220, s[40:43], s66 offen offset:0 lds\n\t" \
  "buffer_load_dwordx4 v220, s[40:43], s66 offen offset:1024 lds\n\t" \
  "buffer_load_dwordx4 v220, s[40:43], s66 offen offset:2048 lds\n\t" \
  "buffer_load_dwordx4 v220, s[40:43], s66 offen offset:3072 lds\n\t" \
  "L_nodmaS%=:\n\t" \
  "L_step2%=:\n\t" \
  "s_cmp_eq_u32 s54, 1\n\t" \
  "s_cbranch_scc1 L_exit0%=\n\t" \
  "s_cmp_lg_u32 s54, 2\n\t" \
  "s_cbranch_scc1 L_nomask0%=\n\t" \
  "v_cmp_gt_i32_e64 s[84:85], 0, v222\n\t" \
  "v_cmp_gt_i32_e64 s[86:87], 1, v222\n\t" \
  "v_cmp_gt_i32_e64 s[88:89], 2, v222\n\t" \
  "v_cmp_gt_i32_e64 s[90:91], 3, v222\n\t" \
  "s_nop 0\n\t" \
  "v_cndmask_b32_e64 v96, v221, v224, s[84:85]\n\t" \
  "v_cndmask_b32_e64 v97, v221, v224, s[86:87]\n\t" \
  "v_cndmask_b32_e64 v98, v221, v224, s[88:89]\n\t" \
  "v_cndmask_b32_e64 v99, v221, v224, s[90:91]\n\t" \
  "v_cmp_gt_i32_e64 s[84:85], 16, v222\n\t" \
  "v_cmp_gt_i32_e64 s[86:87], 17, v222\n\t" \
  "v_cmp_gt_i32_e64 s[88:89], 18, v222\n\t" \
  "v_cmp_gt_i32_e64 s[90:91], 19, v222\n\t" \
  "s_nop 0\n\t" \
  "v_cndmask_b32_e64 v100, v221, v224, s[84:85]\n\t" \
  "v_cndmask_b32_e64 v101, v221, v224, s[86:87]\n\t" \
  "v_cndmask_b32_e64 v102, v221, v224, s[88:89]\n\t" \
  "v_cndmask_b32_e64 v103, v221, v224, s[90:91]\n\t" \
  "v_cmp_gt_i32_e64 s[84:85], 8, v222\n\t" \
  "v_cmp_gt_i32_e64 s[86:87], 9, v222\n\t" \
  "v_cmp_gt_i32_e64 s[88:89], 10, v222\n\t" \
  "v_cmp_gt_i32_e64 s[90:91], 11, v222\n\t" \
  "s_nop 0\n\t" \
  "v_cndmask_b32_e64 v104, v221, v224, s[84:85]\n\t" \
  "v_cndmask_b32_e64 v105, v221, v224, s[86:87]\n\t" \
  "v_cndmask_b32_e64 v106, v221, v224, s[88:89]\n\t" \
  "v_cndmask_b32_e64 v107, v221, v224, s[90:91]\n\t" \
  "v_cmp_gt_i32_e64 s[84:85], 24, v222\n\t" \
  "v_cmp_gt_i32_e64 s[86:87], 25, v222\n\t" \
  "v_cmp_gt_i32_e64 s[88:89], 26, v222\n\t" \
  "v_cmp_gt_i32_e64 s[90:91], 27, v222\n\t" \
  "s_nop 0\n\t" \
  "v_cndmask_b32_e64 v108, v221, v224, s[84:85]\n\t" \
  "v_cndmask_b32_e64 v109, v221, v224, s[86:87]\n\t" \
  "v_cndmask_b32_e64 v110, v221, v224, s[88:89]\n\t" \
  "v_cndmask_b32_e64 v111, v221, v224, s[90:91]\n\t" \
  "s_nop 1\n\t" \
  "L_nomask0%=:\n\t" \
  "s_sub_u32 s54, s54, 1\n\t" \
  "v_mfma_f32_32x32x16_bf16 v[80:95], v[144:147], v[112:115], v[96:111]\n\t" \
  "ds_read_b128 v[176:179], v219 offset:0\n\t" \
  "v_exp_f32_e32 v70, v70\n\t" \
  "v_add_f32_e32 v216, v216, v69\n\t" \
  "v_cvt_pk_bf16_f32 v210, v68, v69\n\t" \
  "v_mfma_f32_32x32x16_bf16 v[80:95], v[148:151], v[116:119], v[80:95]\n\t" \
  "ds_read_b128 v[180:183], v219 offset:1024\n\t" \
  "ds_read_b128 v[144:147], v218 offset:0\n\t" \
  "v_exp_f32_e32 v71, v71\n\t" \
  "v_add_f32_e32 v217, v217, v70\n\t" \
  "v_mfma_f32_32x32x16_bf16 v[80:95], v[152:155], v[120:123], v[80:95]\n\t" \
  "ds_read_b128 v[184:187], v219 offset:2048\n\t" \
  "ds_read_b128 v[148:151], v218 offset:1024\n\t" \
  "v_exp_f32_e32 v72, v72\n\t" \
  "v_add_f32_e32 v216, v216, v71\n\t" \
  "v_cvt_pk_bf16_f32 v211, v70, v71\n\t" \
  "v_mfma_f32_32x32x16_bf16 v[80:95], v[156:159], v[124:127], v[80:95]\n\t" \
  "ds_read_b128 v[188:191], v219 offset:3072\n\t" \
  "ds_read_b128 v[152:155], v218 offset:2048\n\t" \
  "v_exp_f32_e32 v73, v73\n\t" \
  "v_add_f32_e32 v217, v217, v72\n\t" \
  "v_mfma_f32_32x32x16_bf16 v[80:95], v[160:163], v[128:131], v[80:95]\n\t" \
  "ds_read_b128 v[192:195], v219 offset:4096\n\t" \
  "ds_read_b128 v[156:159], v218 offset:3072\n\t" \
  "v_exp_f32_e32 v74, v74\n\t" \
  "v_add_f32_e32 v216, v216, v73\n\t" \
  "v_cvt_pk_bf16_f32 v212, v72, v73\n\t" \
  "v_mfma_f32_32x32x16_bf16 v[80:95], v[164:167], v[132:135], v[80:95]\n\t" \
  "ds_read_b128 v[196:199], v219 offset:5120\n\t" \
  "ds_read_b128 v[160:163], v218 offset:4096\n\t" \
  "v_exp_f32_e32 v75, v75\n\t" \
  "v_add_f32_e32 v217, v217, v74\n\t" \
  "v_mfma_f32_32x32x16_bf16 v[80:95], v[168:171], v[136:139], v[80:95]\n\t" \
  "ds_read_b128 v[200:203], v219 offset:6144\n\t" \
  "ds_read_b128 v[164:167], v218 offset:5120\n\t" \
  "v_exp_f32_e32 v76, v76\n\t" \
  "v_add_f32_e32 v216, v216, v75\n\t" \
  "v_cvt_pk_bf16_f32 v213, v74, v75\n\t" \
  "v_mfma_f32_32x32x16_bf16 v[80:95], v[172:175], v[140:143], v[80:95]\n\t" \
  "ds_read_b128 v[204:207], v219 offset:7168\n\t" \
  "ds_read_b128 v[168:171], v218 offset:6144\n\t" \
  "v_exp_f32_e32 v77, v77\n\t" \
  "v_add_f32_e32 v217, v217, v76\n\t" \
  "s_waitcnt lgkmcnt(14)\n\t" \
  "v_mfma_f32_32x32x16_bf16 v[0:15], v[176:179], v[208:211], v[0:15]\n\t" \
  "ds_read_b128 v[172:175], v218 offset:7168\n\t" \
  "v_exp_f32_e32 v78, v78\n\t" \
  "v_add_f32_e32 v216, v216, v77\n\t" \
  "v_cvt_pk_bf16_f32 v214, v76, v77\n\t" \
  "s_waitcnt lgkmcnt(14)\n\t" \
  "v_mfma_f32_32x32x16_bf16 v[16:31], v[180:183], v[208:211], v[16:31]\n\t" \
  "ds_read_b128 v[176:179], v219 offset:8192\n\t" \
  "v_exp_f32_e32 v79, v79\n\t" \
  "v_add_f32_e32 v217, v217, v78\n\t" \
  "s_waitcnt lgkmcnt(13)\n\t" \
  "v_mfma_f32_32x32x16_bf16 v[32:47], v[184:187], v[208:211], v[32:47]\n\t" \
  "ds_read_b128 v[180:183], v219 offset:9216\n\t" \
  "v_exp_f32_e32 v80, v80\n\t" \
  "v_add_f32_e32 v216, v216, v79\n\t" \
  "v_cvt_pk_bf16_f32 v215, v78, v79\n\t" \
  "s_waitcnt lgkmcnt(12)\n\t" \
  "v_mfma_f32_32x32x16_bf16 v[48:63], v[188:191], v[208:211], v[48:63]\n\t" \
  "ds_read_b128 v[184:187], v219 offset:10240\n\t" \
  "v_exp_f32_e32 v81, v81\n\t" \
  "v_add_f32_e32 v217, v217, v80\n\t" \
  "s_waitcnt lgkmcnt(11)\n\t" \
  "v_mfma_f32_32x32x16_bf16 v[0:15], v[192:195], v[212:215], v[0:15]\n\t" \
  "ds_read_b128 v[188:191], v219 offset:11264\n\t" \
  "v_exp_f32_e32 v82, v82\n\t" \
  "v_add_f32_e32 v216, v216, v81\n\t" \
  "v_cvt_pk_bf16_f32 v208, v80, v81\n\t" \
  "s_waitcnt lgkmcnt(10)\n\t" \
  "v_mfma_f32_32x32x16_bf16 v[16:31], v[196:199], v[212:215], v[16:31]\n\t" \
  "ds_read_b128 v[192:195], v219 offset:12288\n\t" \
  "v_exp_f32_e32 v83, v83\n\t" \
  "v_add_f32_e32 v217, v217, v82\n\t" \
  "s_waitcnt lgkmcnt(9)\n\t" \
  "v_mfma_f32_32x32x16_bf16 v[32:47], v[200:203], v[212:215], v[32:47]\n\t" \
  "ds_read_b128 v[196:199], v219 offset:13312\n\t" \
  "v_exp_f32_e32 v84, v84\n\t" \
  "v_add_f32_e32 v216, v216, v83\n\t" \
  "v_cvt_pk_bf16_f32 v209, v82, v83\n\t" \
  "s_waitcnt lgkmcnt(8)\n\t" \
  "v_mfma_f32_32x32x16_bf16 v[48:63], v[204:207], v[212:215], v[48:63]\n\t" \
  "ds_read_b128 v[200:203], v219 offset:14336\n\t" \
  "v_exp_f32_e32 v85, v85\n\t" \
  "v_add_f32_e32 v217, v217, v84\n\t" \
  "s_cmp_eq_u32 s54, 1\n\t" \
  "s_cbranch_scc1 L_exit1%=\n\t" \
  "s_cmp_lg_u32 s54, 2\n\t" \
  "s_cbranch_scc1 L_nomask1%=\n\t" \
  "v_cmp_gt_i32_e64 s[84:85], 0, v222\n\t" \
  "v_cmp_gt_i32_e64 s[86:87], 1, v222\n\t" \
  "v_cmp_gt_i32_e64 s[88:89], 2, v222\n\t" \
  "v_cmp_gt_i32_e64 s[90:91], 3, v222\n\t" \
  "s_nop 0\n\t" \
  "v_cndmask_b32_e64 v96, v221, v224, s[84:85]\n\t" \
  "v_cndmask_b32_e64 v97, v221, v224, s[86:87]\n\t" \
  "v_cndmask_b32_e64 v98, v221, v224, s[88:89]\n\t" \
  "v_cndmask_b32_e64 v99, v221, v224, s[90:91]\n\t" \
  "v_cmp_gt_i32_e64 s[84:85], 16, v222\n\t" \
  "v_cmp_gt_i32_e64 s[86:87], 17, v222\n\t" \
  "v_cmp_gt_i32_e64 s[88:89], 18, v222\n\t" \
  "v_cmp_gt_i32_e64 s[90:91], 19, v222\n\t" \
  "s_nop 0\n\t" \
  "v_cndmask_b32_e64 v100, v221, v224, s[84:85]\n\t" \
  "v_cndmask_b32_e64 v101, v221, v224, s[86:87]\n\t" \
  "v_cndmask_b32_e64 v102, v221, v224, s[88:89]\n\t" \
  "v_cndmask_b32_e64 v103, v221, v224, s[90:91]\n\t" \
  "v_cmp_gt_i32_e64 s[84:85], 8, v222\n\t" \
  "v_cmp_gt_i32_e64 s[86:87], 9, v222\n\t" \
  "v_cmp_gt_i32_e64 s[88:89], 10, v222\n\t" \
  "v_cmp_gt_i32_e64 s[90:91], 11, v222\n\t" \
  "s_nop 0\n\t" \
  "v_cndmask_b32_e64 v104, v221, v224, s[84:85]\n\t" \
  "v_cndmask_b32_e64 v105, v221, v224, s[86:87]\n\t" \
  "v_cndmask_b32_e64 v106, v221, v224, s[88:89]\n\t" \
  "v_cndmask_b32_e64 v107, v221, v224, s[90:91]\n\t" \
  "v_cmp_gt_i32_e64 s[84:85], 24, v222\n\t" \
  "v_cmp_gt_i32_e64 s[86:87], 25, v222\n\t" \
  "v_cmp_gt_i32_e64 s[88:89], 26, v222\n\t" \
  "v_cmp_gt_i32_e64 s[90:91], 27, v222\n\t" \
  "s_nop 0\n\t" \
  "v_cndmask_b32_e64 v108, v221, v224, s[84:85]\n\t" \
  "v_cndmask_b32_e64 v109, v221, v224, s[86:87]\n\t" \
  "v_cndmask_b32_e64 v110, v221, v224, s[88:89]\n\t" \
  "v_cndmask_b32_e64 v111, v221, v224, s[90:91]\n\t" \
  "s_nop 1\n\t" \
  "L_nomask1%=:\n\t" \
  "s_sub_u32 s54, s54, 1\n\t" \
  "v_mfma_f32_32x32x16_bf16 v[64:79], v[144:147], v[112:115], v[96:111]\n\t" \
  "ds_read_b128 v[204:207], v219 offset:15360\n\t" \
  "v_exp_f32_e32 v86, v86\n\t" \
  "v_add_f32_e32 v216, v216, v85\n\t" \
  "v_cvt_pk_bf16_f32 v210, v84, v85\n\t" \
  "v_mfma_f32_32x32x16_bf16 v[64:79], v[148:151], v[116:119], v[64:79]\n\t" \
  "ds_read_b128 v[144:147], v218 offset:8192\n\t" \
  "v_exp_f32_e32 v87, v87\n\t" \
  "v_add_f32_e32 v217, v217, v86\n\t" \
  "v_mfma_f32_32x32x16_bf16 v[64:79], v[152:155], v[120:123], v[64:79]\n\t" \
  "ds_read_b128 v[148:151], v218 offset:9216\n\t" \
  "v_exp_f32_e32 v88, v88\n\t" \
  "v_add_f32_e32 v216, v216, v87\n\t" \
  "v_cvt_pk_bf16_f32 v211, v86, v87\n\t" \
  "v_mfma_f32_32x32x16_bf16 v[64:79], v[156:159], v[124:127], v[64:79]\n\t" \
  "ds_read_b128 v[152:155], v218 offset:10240\n\t" \
  "v_exp_f32_e32 v89, v89\n\t" \
  "v_add_f32_e32 v217, v217, v88\n\t" \
  "v_mfma_f32_32x32x16_bf16 v[64:79], v[160:163], v[128:131], v[64:79]\n\t" \
  "ds_read_b128 v[156:159], v218 offset:11264\n\t" \
  "v_exp_f32_e32 v90, v90\n\t" \
  "v_add_f32_e32 v216, v216, v89\n\t" \
  "v_cvt_pk_bf16_f32 v212, v88, v89\n\t" \
  "v_mfma_f32_32x32x16_bf16 v[64:79], v[164:167], v[132:135], v[64:79]\n\t" \
  "ds_read_b128 v[160:163], v218 offset:12288\n\t" \
  "v_exp_f32_e32 v91, v91\n\t" \
  "v_add_f32_e32 v217, v217, v90\n\t" \
  "s_waitcnt lgkmcnt(14)\n\t" \
  "v_mfma_f32_32x32x16_bf16 v[64:79], v[168:171], v[136:139], v[64:79]\n\t" \
  "ds_read_b128 v[164:167], v218 offset:13312\n\t" \
  "v_exp_f32_e32 v92, v92\n\t" \
  "v_add_f32_e32 v216, v216, v91\n\t" \
  "v_cvt_pk_bf16_f32 v213, v90, v91\n\t" \
  "s_waitcnt lgkmcnt(14)\n\t" \
  "v_mfma_f32_32x32x16_bf16 v[64:79], v[172:175], v[140:143], v[64:79]\n\t" \
  "ds_read_b128 v[168:171], v218 offset:14336\n\t" \
  "v_exp_f32_e32 v93, v93\n\t" \
  "v_add_f32_e32 v217, v217, v92\n\t" \
  "s_waitcnt lgkmcnt(14)\n\t" \
  "v_mfma_f32_32x32x16_bf16 v[0:15], v[176:179], v[208:211], v[0:15]\n\t" \
  "ds_read_b128 v[172:175], v218 offset:15360\n\t" \
  "v_exp_f32_e32 v94, v94\n\t" \
  "v_add_f32_e32 v216, v216, v93\n\t" \
  "v_cvt_pk_bf16_f32 v214, v92, v93\n\t" \
  "s_waitcnt lgkmcnt(14)\n\t" \
  "v_mfma_f32_32x32x16_bf16 v[16:31], v[180:183], v[208:211], v[16:31]\n\t" \
  "ds_read_b128 v[176:179], v219 offset:16384\n\t" \
  "v_exp_f32_e32 v95, v95\n\t" \
  "v_add_f32_e32 v217, v217, v94\n\t" \
  "s_waitcnt lgkmcnt(14)\n\t" \
  "v_mfma_f32_32x32x16_bf16 v[32:47], v[184:187], v[208:211], v[32:47]\n\t" \
  "ds_read_b128 v[180:183], v219 offset:17408\n\t" \
  "v_exp_f32_e32 v64, v64\n\t" \
  "v_add_f32_e32 v216, v216, v95\n\t" \
  "v_cvt_pk_bf16_f32 v215, v94, v95\n\t" \
  "s_waitcnt lgkmcnt(14)\n\t" \
  "v_mfma_f32_32x32x16_bf16 v[48:63], v[188:191], v[208:211], v[48:63]\n\t" \
  "ds_read_b128 v[184:187], v219 offset:18432\n\t" \
  "v_exp_f32_e32 v65, v65\n\t" \
  "v_add_f32_e32 v217, v217, v64\n\t" \
  "s_waitcnt lgkmcnt(14)\n\t" \
  "v_mfma_f32_32x32x16_bf16 v[0:15], v[192:195], v[212:215], v[0:15]\n\t" \
  "ds_read_b128 v[188:191], v219 offset:19456\n\t" \
  "v_exp_f32_e32 v66, v66\n\t" \
  "v_add_f32_e32 v216, v216, v65\n\t" \
  "v_cvt_pk_bf16_f32 v208, v64, v65\n\t" \
  "s_waitcnt lgkmcnt(14)\n\t" \
  "v_mfma_f32_32x32x16_bf16 v[16:31], v[196:199], v[212:215], v[16:31]\n\t" \
  "ds_read_b128 v[192:195], v219 offset:20480\n\t" \
  "v_exp_f32_e32 v67, v67\n\t" \
  "v_add_f32_e32 v217, v217, v66\n\t" \
  "s_waitcnt lgkmcnt(14)\n\t" \
  "v_mfma_f32_32x32x16_bf16 v[32:47], v[200:203], v[212:215], v[32:47]\n\t" \
  "ds_read_b128 v[196:199], v219 offset:21504\n\t" \
  "v_exp_f32_e32 v68, v68\n\t" \
  "v_add_f32_e32 v216, v216, v67\n\t" \
  "v_cvt_pk_bf16_f32 v209, v66, v67\n\t" \
  "s_waitcnt lgkmcnt(14)\n\t" \
  "v_mfma_f32_32x32x16_bf16 v[48:63], v[204:207], v[212:215], v[48:63]\n\t" \
  "ds_read_b128 v[200:203], v219 offset:22528\n\t" \
  "v_exp_f32_e32 v69, v69\n\t" \
  "v_add_f32_e32 v217, v217, v68\n\t" \
  "s_cmp_eq_u32 s54, 1\n\t" \
  "s_cbranch_scc1 L_exit2%=\n\t" \
  "s_cmp_lg_u32 s54, 2\n\t" \
  "s_cbranch_scc1 L_nomask2%=\n\t" \
  "v_cmp_gt_i32_e64 s[84:85], 0, v222\n\t" \
  "v_cmp_gt_i32_e64 s[86:87], 1, v222\n\t" \
  "v_cmp_gt_i32_e64 s[88:89], 2, v222\n\t" \
  "v_cmp_gt_i32_e64 s[90:91], 3, v222\n\t" \
  "s_nop 0\n\t" \
  "v_cndmask_b32_e64 v96, v221, v224, s[84:85]\n\t" \
  "v_cndmask_b32_e64 v97, v221, v224, s[86:87]\n\t" \
  "v_cndmask_b32_e64 v98, v221, v224, s[88:89]\n\t" \
  "v_cndmask_b32_e64 v99, v221, v224, s[90:91]\n\t" \
  "v_cmp_gt_i32_e64 s[84:85], 16, v222\n\t" \
  "v_cmp_gt_i32_e64 s[86:87], 17, v222\n\t" \
  "v_cmp_gt_i32_e64 s[88:89], 18, v222\n\t" \
  "v_cmp_gt_i32_e64 s[90:91], 19, v222\n\t" \
  "s_nop 0\n\t" \
  "v_cndmask_b32_e64 v100, v221, v224, s[84:85]\n\t" \
  "v_cndmask_b32_e64 v101, v221, v224, s[86:87]\n\t" \
  "v_cndmask_b32_e64 v102, v221, v224, s[88:89]\n\t" \
  "v_cndmask_b32_e64 v103, v221, v224, s[90:91]\n\t" \
  "v_cmp_gt_i32_e64 s[84:85], 8, v222\n\t" \
  "v_cmp_gt_i32_e64 s[86:87], 9, v222\n\t" \
  "v_cmp_gt_i32_e64 s[88:89], 10, v222\n\t" \
  "v_cmp_gt_i32_e64 s[90:91], 11, v222\n\t" \
  "s_nop 0\n\t" \
  "v_cndmask_b32_e64 v104, v221, v224, s[84:85]\n\t" \
  "v_cndmask_b32_e64 v105, v221, v224, s[86:87]\n\t" \
  "v_cndmask_b32_e64 v106, v221, v224, s[88:89]\n\t" \
  "v_cndmask_b32_e64 v107, v221, v224, s[90:91]\n\t" \
  "v_cmp_gt_i32_e64 s[84:85], 24, v222\n\t" \
  "v_cmp_gt_i32_e64 s[86:87], 25, v222\n\t" \
  "v_cmp_gt_i32_e64 s[88:89], 26, v222\n\t" \
  "v_cmp_gt_i32_e64 s[90:91], 27, v222\n\t" \
  "s_nop 0\n\t" \
  "v_cndmask_b32_e64 v108, v221, v224, s[84:85]\n\t" \
  "v_cndmask_b32_e64 v109, v221, v224, s[86:87]\n\t" \
  "v_cndmask_b32_e64 v110, v221, v224, s[88:89]\n\t" \
  "v_cndmask_b32_e64 v111, v221, v224, s[90:91]\n\t" \
  "s_nop 1\n\t" \
  "L_nomask2%=:\n\t" \
  "s_sub_u32 s54, s54, 1\n\t" \
  "s_waitcnt lgkmcnt(14)\n\t" \
  "v_mfma_f32_32x32x16_bf16 v[80:95], v[144:147], v[112:115], v[96:111]\n\t" \
  "ds_read_b128 v[204:207], v219 offset:23552\n\t" \
  "v_exp_f32_e32 v70, v70\n\t" \
  "v_add_f32_e32 v216, v216, v69\n\t" \
  "v_cvt_pk_bf16_f32 v210, v68, v69\n\t" \
  "s_waitcnt lgkmcnt(14)\n\t" \
  "v_mfma_f32_32x32x16_bf16 v[80:95], v[148:151], v[116:119], v[80:95]\n\t" \
  "ds_read_b128 v[144:147], v218 offset:16384\n\t" \
  "v_exp_f32_e32 v71, v71\n\t" \
  "v_add_f32_e32 v217, v217, v70\n\t" \
  "s_waitcnt lgkmcnt(14)\n\t" \
  "v_mfma_f32_32x32x16_bf16 v[80:95], v[152:155], v[120:123], v[80:95]\n\t" \
  "ds_read_b128 v[148:151], v218 offset:17408\n\t" \
  "v_exp_f32_e32 v72, v72\n\t" \
  "v_add_f32_e32 v216, v216, v71\n\t" \
  "v_cvt_pk_bf16_f32 v211, v70, v71\n\t" \
  "s_waitcnt lgkmcnt(14)\n\t" \
  "v_mfma_f32_32x32x16_bf16 v[80:95], v[156:159], v[124:127], v[80:95]\n\t" \
  "ds_read_b128 v[152:155], v218 offset:18432\n\t" \
  "v_exp_f32_e32 v73, v73\n\t" \
  "v_add_f32_e32 v217, v217, v72\n\t" \
  "s_waitcnt lgkmcnt(14)\n\t" \
  "v_mfma_f32_32x32x16_bf16 v[80:95], v[160:163], v[128:131], v[80:95]\n\t" \
  "ds_read_b128 v[156:159], v218 offset:19456\n\t" \
  "v_exp_f32_e32 v74, v74\n\t" \
  "v_add_f32_e32 v216, v216, v73\n\t" \
  "v_cvt_pk_bf16_f32 v212, v72, v73\n\t" \
  "s_waitcnt lgkmcnt(14)\n\t" \
  "v_mfma_f32_32x32x16_bf16 v[80:95], v[164:167], v[132:135], v[80:95]\n\t" \
  "ds_read_b128 v[160:163], v218 offset:20480\n\t" \
  "v_exp_f32_e32 v75, v75\n\t" \
  "v_add_f32_e32 v217, v217, v74\n\t" \
  "s_waitcnt lgkmcnt(14)\n\t" \
  "v_mfma_f32_32x32x16_bf16 v[80:95], v[168:171], v[136:139], v[80:95]\n\t" \
  "ds_read_b128 v[164:167], v218 offset:21504\n\t" \
  "v_exp_f32_e32 v76, v76\n\t" \
  "v_add_f32_e32 v216, v216, v75\n\t" \
  "v_cvt_pk_bf16_f32 v213, v74, v75\n\t" \
  "s_waitcnt lgkmcnt(14)\n\t" \
  "v_mfma_f32_32x32x16_bf16 v[80:95], v[172:175], v[140:143], v[80:95]\n\t" \
  "ds_read_b128 v[168:171], v218 offset:22528\n\t" \
  "v_exp_f32_e32 v77, v77\n\t" \
  "v_add_f32_e32 v217, v217, v76\n\t" \
  "s_waitcnt lgkmcnt(14)\n\t" \
  "v_mfma_f32_32x32x16_bf16 v[0:15], v[176:179], v[208:211], v[0:15]\n\t" \
  "ds_read_b128 v[172:175], v218 offset:23552\n\t" \
  "v_exp_f32_e32 v78, v78\n\t" \
  "v_add_f32_e32 v216, v216, v77\n\t" \
  "v_cvt_pk_bf16_f32 v214, v76, v77\n\t" \
  "s_waitcnt lgkmcnt(14)\n\t" \
  "v_mfma_f32_32x32x16_bf16 v[16:31], v[180:183], v[208:211], v[16:31]\n\t" \
  "ds_read_b128 v[176:179], v219 offset:24576\n\t" \
  "v_exp_f32_e32 v79, v79\n\t" \
  "v_add_f32_e32 v217, v217, v78\n\t" \
  "s_waitcnt lgkmcnt(14)\n\t" \
  "v_mfma_f32_32x32x16_bf16 v[32:47], v[184:187], v[208:211], v[32:47]\n\t" \
  "ds_read_b128 v[180:183], v219 offset:25600\n\t" \
  "v_exp_f32_e32 v80, v80\n\t" \
  "v_add_f32_e32 v216, v216, v79\n\t" \
  "v_cvt_pk_bf16_f32 v215, v78, v79\n\t" \
  "s_waitcnt lgkmcnt(14)\n\t" \
  "v_mfma_f32_32x32x16_bf16 v[48:63], v[188:191], v[208:211], v[48:63]\n\t" \
  "ds_read_b128 v[184:187], v219 offset:26624\n\t" \
  "v_exp_f32_e32 v81, v81\n\t" \
  "v_add_f32_e32 v217, v217, v80\n\t" \
  "s_waitcnt lgkmcnt(14)\n\t" \
  "v_mfma_f32_32x32x16_bf16 v[0:15], v[192:195], v[212:215], v[0:15]\n\t" \
  "ds_read_b128 v[188:191], v219 offset:27648\n\t" \
  "v_exp_f32_e32 v82, v82\n\t" \
  "v_add_f32_e32 v216, v216, v81\n\t" \
  "v_cvt_pk_bf16_f32 v208, v80, v81\n\t" \
  "s_waitcnt lgkmcnt(14)\n\t" \
  "v_mfma_f32_32x32x16_bf16 v[16:31], v[196:199], v[212:215], v[16:31]\n\t" \
  "ds_read_b128 v[192:195], v219 offset:28672\n\t" \
  "v_exp_f32_e32 v83, v83\n\t" \
  "v_add_f32_e32 v217, v217, v82\n\t" \
  "s_waitcnt lgkmcnt(14)\n\t" \
  "v_mfma_f32_32x32x16_bf16 v[32:47], v[200:203], v[212:215], v[32:47]\n\t" \
  "ds_read_b128 v[196:199], v219 offset:29696\n\t" \
  "v_exp_f32_e32 v84, v84\n\t" \
  "v_add_f32_e32 v216, v216, v83\n\t" \
  "v_cvt_pk_bf16_f32 v209, v82, v83\n\t" \
  "s_waitcnt lgkmcnt(14)\n\t" \
  "v_mfma_f32_32x32x16_bf16 v[48:63], v[204:207], v[212:215], v[48:63]\n\t" \
  "ds_read_b128 v[200:203], v219 offset:30720\n\t" \
  "v_exp_f32_e32 v85, v85\n\t" \
  "v_add_f32_e32 v217, v217, v84\n\t" \
  "s_cmp_eq_u32 s54, 1\n\t" \
  "s_cbranch_scc1 L_exit3%=\n\t" \
  "s_cmp_lg_u32 s54, 2\n\t" \
  "s_cbranch_scc1 L_nomask3%=\n\t" \
  "v_cmp_gt_i32_e64 s[84:85], 0, v222\n\t" \
  "v_cmp_gt_i32_e64 s[86:87], 1, v222\n\t" \
  "v_cmp_gt_i32_e64 s[88:89], 2, v222\n\t" \
  "v_cmp_gt_i32_e64 s[90:91], 3, v222\n\t" \
  "s_nop 0\n\t" \
  "v_cndmask_b32_e64 v96, v221, v224, s[84:85]\n\t" \
  "v_cndmask_b32_e64 v97, v221, v224, s[86:87]\n\t" \
  "v_cndmask_b32_e64 v98, v221, v224, s[88:89]\n\t" \
  "v_cndmask_b32_e64 v99, v221, v224, s[90:91]\n\t" \
  "v_cmp_gt_i32_e64 s[84:85], 16, v222\n\t" \
  "v_cmp_gt_i32_e64 s[86:87], 17, v222\n\t" \
  "v_cmp_gt_i32_e64 s[88:89], 18, v222\n\t" \
  "v_cmp_gt_i32_e64 s[90:91], 19, v222\n\t" \
  "s_nop 0\n\t" \
  "v_cndmask_b32_e64 v100, v221, v224, s[84:85]\n\t" \
  "v_cndmask_b32_e64 v101, v221, v224, s[86:87]\n\t" \
  "v_cndmask_b32_e64 v102, v221, v224, s[88:89]\n\t" \
  "v_cndmask_b32_e64 v103, v221, v224, s[90:91]\n\t" \
  "v_cmp_gt_i32_e64 s[84:85], 8, v222\n\t" \
  "v_cmp_gt_i32_e64 s[86:87], 9, v222\n\t" \
  "v_cmp_gt_i32_e64 s[88:89], 10, v222\n\t" \
  "v_cmp_gt_i32_e64 s[90:91], 11, v222\n\t" \
  "s_nop 0\n\t" \
  "v_cndmask_b32_e64 v104, v221, v224, s[84:85]\n\t" \
  "v_cndmask_b32_e64 v105, v221, v224, s[86:87]\n\t" \
  "v_cndmask_b32_e64 v106, v221, v224, s[88:89]\n\t" \
  "v_cndmask_b32_e64 v107, v221, v224, s[90:91]\n\t" \
  "v_cmp_gt_i32_e64 s[84:85], 24, v222\n\t" \
  "v_cmp_gt_i32_e64 s[86:87], 25, v222\n\t" \
  "v_cmp_gt_i32_e64 s[88:89], 26, v222\n\t" \
  "v_cmp_gt_i32_e64 s[90:91], 27, v222\n\t" \
  "s_nop 0\n\t" \
  "v_cndmask_b32_e64 v108, v221, v224, s[84:85]\n\t" \
  "v_cndmask_b32_e64 v109, v221, v224, s[86:87]\n\t" \
  "v_cndmask_b32_e64 v110, v221, v224, s[88:89]\n\t" \
  "v_cndmask_b32_e64 v111, v221, v224, s[90:91]\n\t" \
  "s_nop 1\n\t" \
  "L_nomask3%=:\n\t" \
  "s_sub_u32 s54, s54, 1\n\t" \
  "s_waitcnt lgkmcnt(14)\n\t" \
  "v_mfma_f32_32x32x16_bf16 v[64:79], v[144:147], v[112:115], v[96:111]\n\t" \
  "ds_read_b128 v[204:207], v219 offset:31744\n\t" \
  "v_exp_f32_e32 v86, v86\n\t" \
  "v_add_f32_e32 v216, v216, v85\n\t" \
  "v_cvt_pk_bf16_f32 v210, v84, v85\n\t" \
  "s_waitcnt lgkmcnt(14)\n\t" \
  "v_mfma_f32_32x32x16_bf16 v[64:79], v[148:151], v[116:119], v[64:79]\n\t" \
  "ds_read_b128 v[144:147], v218 offset:24576\n\t" \
  "v_exp_f32_e32 v87, v87\n\t" \
  "v_add_f32_e32 v217, v217, v86\n\t" \
  "s_waitcnt lgkmcnt(14)\n\t" \
  "v_mfma_f32_32x32x16_bf16 v[64:79], v[152:155], v[120:123], v[64:79]\n\t" \
  "ds_read_b128 v[148:151], v218 offset:25600\n\t" \
  "v_exp_f32_e32 v88, v88\n\t" \
  "v_add_f32_e32 v216, v216, v87\n\t" \
  "v_cvt_pk_bf16_f32 v211, v86, v87\n\t" \
  "s_waitcnt lgkmcnt(14)\n\t" \
  "v_mfma_f32_32x32x16_bf16 v[64:79], v[156:159], v[124:127], v[64:79]\n\t" \
  "ds_read_b128 v[152:155], v218 offset:26624\n\t" \
  "v_exp_f32_e32 v89, v89\n\t" \
  "v_add_f32_e32 v217, v217, v88\n\t" \
  "s_waitcnt lgkmcnt(14)\n\t" \
  "v_mfma_f32_32x32x16_bf16 v[64:79], v[160:163], v[128:131], v[64:79]\n\t" \
  "ds_read_b128 v[156:159], v218 offset:27648\n\t" \
  "v_exp_f32_e32 v90, v90\n\t" \
  "v_add_f32_e32 v216, v216, v89\n\t" \
  "v_cvt_pk_bf16_f32 v212, v88, v89\n\t" \
  "s_waitcnt lgkmcnt(14)\n\t" \
  "v_mfma_f32_32x32x16_bf16 v[64:79], v[164:167], v[132:135], v[64:79]\n\t" \
  "ds_read_b128 v[160:163], v218 offset:28672\n\t" \
  "v_exp_f32_e32 v91, v91\n\t" \
  "v_add_f32_e32 v217, v217, v90\n\t" \
  "s_waitcnt lgkmcnt(14)\n\t" \
  "v_mfma_f32_32x32x16_bf16 v[64:79], v[168:171], v[136:139], v[64:79]\n\t" \
  "ds_read_b128 v[164:167], v218 offset:29696\n\t" \
  "v_exp_f32_e32 v92, v92\n\t" \
  "v_add_f32_e32 v216, v216, v91\n\t" \
  "v_cvt_pk_bf16_f32 v213, v90, v91\n\t" \
  "s_waitcnt lgkmcnt(14)\n\t" \
  "v_mfma_f32_32x32x16_bf16 v[64:79], v[172:175], v[140:143], v[64:79]\n\t" \
  "ds_read_b128 v[168:171], v218 offset:30720\n\t" \
  "v_exp_f32_e32 v93, v93\n\t" \
  "v_add_f32_e32 v217, v217, v92\n\t" \
  "s_waitcnt lgkmcnt(14)\n\t" \
  "v_mfma_f32_32x32x16_bf16 v[0:15], v[176:179], v[208:211], v[0:15]\n\t" \
  "ds_read_b128 v[172:175], v218 offset:31744\n\t" \
  "v_exp_f32_e32 v94, v94\n\t" \
  "v_add_f32_e32 v216, v216, v93\n\t" \
  "v_cvt_pk_bf16_f32 v214, v92, v93\n\t" \
  "s_waitcnt lgkmcnt(14)\n\t" \
  "v_mfma_f32_32x32x16_bf16 v[16:31], v[180:183], v[208:211], v[16:31]\n\t" \
  "v_exp_f32_e32 v95, v95\n\t" \
  "v_add_f32_e32 v217, v217, v94\n\t" \
  "s_waitcnt lgkmcnt(13)\n\t" \
  "v_mfma_f32_32x32x16_bf16 v[32:47], v[184:187], v[208:211], v[32:47]\n\t" \
  "v_exp_f32_e32 v64, v64\n\t" \
  "v_add_f32_e32 v216, v216, v95\n\t" \
  "v_cvt_pk_bf16_f32 v215, v94, v95\n\t" \
  "s_waitcnt lgkmcnt(12)\n\t" \
  "v_mfma_f32_32x32x16_bf16 v[48:63], v[188:191], v[208:211], v[48:63]\n\t" \
  "v_exp_f32_e32 v65, v65\n\t" \
  "v_add_f32_e32 v217, v217, v64\n\t" \
  "s_waitcnt lgkmcnt(11)\n\t" \
  "v_mfma_f32_32x32x16_bf16 v[0:15], v[192:195], v[212:215], v[0:15]\n\t" \
  "v_exp_f32_e32 v66, v66\n\t" \
  "v_add_f32_e32 v216, v216, v65\n\t" \
  "v_cvt_pk_bf16_f32 v208, v64, v65\n\t" \
  "s_waitcnt lgkmcnt(10)\n\t" \
  "v_mfma_f32_32x32x16_bf16 v[16:31], v[196:199], v[212:215], v[16:31]\n\t" \
  "v_exp_f32_e32 v67, v67\n\t" \
  "v_add_f32_e32 v217, v217, v66\n\t" \
  "s_waitcnt lgkmcnt(9)\n\t" \
  "v_mfma_f32_32x32x16_bf16 v[32:47], v[200:203], v[212:215], v[32:47]\n\t" \
  "v_exp_f32_e32 v68, v68\n\t" \
  "v_add_f32_e32 v216, v216, v67\n\t" \
  "v_cvt_pk_bf16_f32 v209, v66, v67\n\t" \
  "s_waitcnt lgkmcnt(8)\n\t" \
  "v_mfma_f32_32x32x16_bf16 v[48:63], v[204:207], v[212:215], v[48:63]\n\t" \
  "v_exp_f32_e32 v69, v69\n\t" \
  "v_add_f32_e32 v217, v217, v68\n\t" \
  "s_waitcnt vmcnt(0) lgkmcnt(0)\n\t" \
  "s_barrier\n\t" \
  "s_xor_b32 s52, s52, 0x8000\n\t" \
  "v_xor_b32_e32 v218, 0x8000, v218\n\t" \
  "v_xor_b32_e32 v219, 0x8000, v219\n\t" \
  "s_add_u32 s53, s53, 1\n\t" \
  "s_branch L_step%=\n\t" \
  "L_exit0%=:\n\t" \
  "v_add_u32_e32 v227, 0, v219\n\t" \
  "s_branch L_tail0%=\n\t" \
  "L_exit1%=:\n\t" \
  "v_add_u32_e32 v227, 8192, v219\n\t" \
  "s_branch L_tail1%=\n\t" \
  "L_exit2%=:\n\t" \
  "v_add_u32_e32 v227, 16384, v219\n\t" \
  "s_branch L_tail0%=\n\t" \
  "L_exit3%=:\n\t" \
  "v_add_u32_e32 v227, 24576, v219\n\t" \
  "s_branch L_tail1%=\n\t" \
  "L_tail0%=:\n\t" \
  "s_waitcnt lgkmcnt(0)\n\t" \
  "ds_read_b128 v[176:179], v227 offset:0\n\t" \
  "ds_read_b128 v[180:183], v227 offset:1024\n\t" \
  "ds_read_b128 v[184:187], v227 offset:2048\n\t" \
  "ds_read_b128 v[188:191], v227 offset:3072\n\t" \
  "ds_read_b128 v[192:195], v227 offset:4096\n\t" \
  "ds_read_b128 v[196:199], v227 offset:5120\n\t" \
  "ds_read_b128 v[200:203], v227 offset:6144\n\t" \
  "ds_read_b128 v[204:207], v227 offset:7168\n\t" \
  "v_exp_f32_e32 v70, v70\n\t" \
  "v_exp_f32_e32 v71, v71\n\t" \
  "v_exp_f32_e32 v72, v72\n\t" \
  "v_exp_f32_e32 v73, v73\n\t" \
  "v_exp_f32_e32 v74, v74\n\t" \
  "v_exp_f32_e32 v75, v75\n\t" \
  "v_exp_f32_e32 v76, v76\n\t" \
  "v_exp_f32_e32 v77, v77\n\t" \
  "v_exp_f32_e32 v78, v78\n\t" \
  "v_exp_f32_e32 v79, v79\n\t" \
  "s_nop 0\n\t" \
  "v_add_f32_e32 v217, v217, v69\n\t" \
  "v_add_f32_e32 v216, v216, v70\n\t" \
  "v_add_f32_e32 v217, v217, v71\n\t" \
  "v_add_f32_e32 v216, v216, v72\n\t" \
  "v_add_f32_e32 v217, v217, v73\n\t" \
  "v_add_f32_e32 v216, v216, v74\n\t" \
  "v_add_f32_e32 v217, v217, v75\n\t" \
  "v_add_f32_e32 v216, v216, v76\n\t" \
  "v_add_f32_e32 v217, v217, v77\n\t" \
  "v_add_f32_e32 v216, v216, v78\n\t" \
  "v_add_f32_e32 v217, v217, v79\n\t" \
  "v_cvt_pk_bf16_f32 v210, v68, v69\n\t" \
  "v_cvt_pk_bf16_f32 v211, v70, v71\n\t" \
  "v_cvt_pk_bf16_f32 v212, v72, v73\n\t" \
  "v_cvt_pk_bf16_f32 v213, v74, v75\n\t" \
  "v_cvt_pk_bf16_f32 v214, v76, v77\n\t" \
  "v_cvt_pk_bf16_f32 v215, v78, v79\n\t" \
  "s_nop 1\n\t" \
  "s_waitcnt lgkmcnt(7)\n\t" \
  "v_mfma_f32_32x32x16_bf16 v[0:15], v[176:179], v[208:211], v[0:15]\n\t" \
  "s_waitcnt lgkmcnt(6)\n\t" \
  "v_mfma_f32_32x32x16_bf16 v[16:31], v[180:183], v[208:211], v[16:31]\n\t" \
  "s_waitcnt lgkmcnt(5)\n\t" \
  "v_mfma_f32_32x32x16_bf16 v[32:47], v[184:187], v[208:211], v[32:47]\n\t" \
  "s_waitcnt lgkmcnt(4)\n\t" \
  "v_mfma_f32_32x32x16_bf16 v[48:63], v[188:191], v[208:211], v[48:63]\n\t" \
  "s_waitcnt lgkmcnt(3)\n\t" \
  "v_mfma_f32_32x32x16_bf16 v[0:15], v[192:195], v[212:215], v[0:15]\n\t" \
  "s_waitcnt lgkmcnt(2)\n\t" \
  "v_mfma_f32_32x32x16_bf16 v[16:31], v[196:199], v[212:215], v[16:31]\n\t" \
  "s_waitcnt lgkmcnt(1)\n\t" \
  "v_mfma_f32_32x32x16_bf16 v[32:47], v[200:203], v[212:215], v[32:47]\n\t" \
  "s_waitcnt lgkmcnt(0)\n\t" \
  "v_mfma_f32_32x32x16_bf16 v[48:63], v[204:207], v[212:215], v[48:63]\n\t" \
  "s_branch L_fin%=\n\t" \
  "L_tail1%=:\n\t" \
  "s_waitcnt lgkmcnt(0)\n\t" \
  "ds_read_b128 v[176:179], v227 offset:0\n\t" \
  "ds_read_b128 v[180:183], v227 offset:1024\n\t" \
  "ds_read_b128 v[184:187], v227 offset:2048\n\t" \
  "ds_read_b128 v[188:191], v227 offset:3072\n\t" \
  "ds_read_b128 v[192:195], v227 offset:4096\n\t" \
  "ds_read_b128 v[196:199], v227 offset:5120\n\t" \
  "ds_read_b128 v[200:203], v227 offset:6144\n\t" \
  "ds_read_b128 v[204:207], v227 offset:7168\n\t" \
  "v_exp_f32_e32 v86, v86\n\t" \
  "v_exp_f32_e32 v87, v87\n\t" \
  "v_exp_f32_e32 v88, v88\n\t" \
  "v_exp_f32_e32 v89, v89\n\t" \
  "v_exp_f32_e32 v90, v90\n\t" \
  "v_exp_f32_e32 v91, v91\n\t" \
  "v_exp_f32_e32 v92, v92\n\t" \
  "v_exp_f32_e32 v93, v93\n\t" \
  "v_exp_f32_e32 v94, v94\n\t" \
  "v_exp_f32_e32 v95, v95\n\t" \
  "s_nop 0\n\t" \
  "v_add_f32_e32 v217, v217, v85\n\t" \
  "v_add_f32_e32 v216, v216, v86\n\t" \
  "v_add_f32_e32 v217, v217, v87\n\t" \
  "v_add_f32_e32 v216, v216, v88\n\t" \
  "v_add_f32_e32 v217, v217, v89\n\t" \
  "v_add_f32_e32 v216, v216, v90\n\t" \
  "v_add_f32_e32 v217, v217, v91\n\t" \
  "v_add_f32_e32 v216, v216, v92\n\t" \
  "v_add_f32_e32 v217, v217, v93\n\t" \
  "v_add_f32_e32 v216, v216, v94\n\t" \
  "v_add_f32_e32 v217, v217, v95\n\t" \
  "v_cvt_pk_bf16_f32 v210, v84, v85\n\t" \
  "v_cvt_pk_bf16_f32 v211, v86, v87\n\t" \
  "v_cvt_pk_bf16_f32 v212, v88, v89\n\t" \
  "v_cvt_pk_bf16_f32 v213, v90, v91\n\t" \
  "v_cvt_pk_bf16_f32 v214, v92, v93\n\t" \
  "v_cvt_pk_bf16_f32 v215, v94, v95\n\t" \
  "s_nop 1\n\t" \
  "s_waitcnt lgkmcnt(7)\n\t" \
  "v_mfma_f32_32x32x16_bf16 v[0:15], v[176:179], v[208:211], v[0:15]\n\t" \
  "s_waitcnt lgkmcnt(6)\n\t" \
  "v_mfma_f32_32x32x16_bf16 v[16:31], v[180:183], v[208:211], v[16:31]\n\t" \
  "s_waitcnt lgkmcnt(5)\n\t" \
  "v_mfma_f32_32x32x16_bf16 v[32:47], v[184:187], v[208:211], v[32:47]\n\t" \
  "s_waitcnt lgkmcnt(4)\n\t" \
  "v_mfma_f32_32x32x16_bf16 v[48:63], v[188:191], v[208:211], v[48:63]\n\t" \
  "s_waitcnt lgkmcnt(3)\n\t" \
  "v_mfma_f32_32x32x16_bf16 v[0:15], v[192:195], v[212:215], v[0:15]\n\t" \
  "s_waitcnt lgkmcnt(2)\n\t" \
  "v_mfma_f32_32x32x16_bf16 v[16:31], v[196:199], v[212:215], v[16:31]\n\t" \
  "s_waitcnt lgkmcnt(1)\n\t" \
  "v_mfma_f32_32x32x16_bf16 v[32:47], v[200:203], v[212:215], v[32:47]\n\t" \
  "s_waitcnt lgkmcnt(0)\n\t" \
  "v_mfma_f32_32x32x16_bf16 v[48:63], v[204:207], v[212:215], v[48:63]\n\t" \
  "s_branch L_fin%=\n\t" \
  "L_fin%=:\n\t" \
  "s_cmp_eq_u32 s56, 0\n\t" \
  "s_cbranch_scc1 L_fin2%=\n\t" \
  "s_waitcnt vmcnt(0) lgkmcnt(0)\n\t" \
  "s_barrier\n\t" \
  "s_xor_b32 s52, s52, 0x8000\n\t" \
  "s_add_u32 s53, s53, 1\n\t" \
  "s_cmp_ge_u32 s53, s75\n\t" \
  "s_cbranch_scc1 L_nodmaF%=\n\t" \
  "s_add_u32 s58, s53, 1\n\t" \
  "s_lshl_b32 s62, s58, 15\n\t" \
  "s_add_u32 s61, s62, 16384\n\t" \
  "s_xor_b32 s63, s52, 0x8000\n\t" \
  "s_add_u32 m0, s50, s63\n\t" \
  "s_add_u32 s66, s61, 4096\n\t" \
  "buffer_load_dwordx4 v220, s[36:39], s61 offen offset:0 lds\n\t" \
  "buffer_load_dwordx4 v220, s[36:39], s61 offen offset:1024 lds\n\t" \
  "buffer_load_dwordx4 v220, s[36:39], s61 offen offset:2048 lds\n\t" \
  "buffer_load_dwordx4 v220, s[36:39], s61 offen offset:3072 lds\n\t" \
  "s_add_u32 m0, m0, 4096\n\t" \
  "s_nop 0\n\t" \
  "buffer_load_dwordx4 v220, s[36:39], s66 offen offset:0 lds\n\t" \
  "buffer_load_dwordx4 v220, s[36:39], s66 offen offset:1024 lds\n\t" \
  "buffer_load_dwordx4 v220, s[36:39], s66 offen offset:2048 lds\n\t" \
  "buffer_load_dwordx4 v220, s[36:39], s66 offen offset:3072 lds\n\t" \
  "s_add_u32 m0, s51, s63\n\t" \
  "s_add_u32 s66, s62, 4096\n\t" \
  "buffer_load_dwordx4 v220, s[40:43], s62 offen offset:0 lds\n\t" \
  "buffer_load_dwordx4 v220, s[40:43], s62 offen offset:1024 lds\n\t" \
  "buffer_load_dwordx4 v220, s[40:43], s62 offen offset:2048 lds\n\t" \
  "buffer_load_dwordx4 v220, s[40:43], s62 offen offset:3072 lds\n\t" \
  "s_add_u32 m0, m0, 4096\n\t" \
  "s_nop 0\n\t" \
  "buffer_load_dwordx4 v220, s[40:43], s66 offen offset:0 lds\n\t" \
  "buffer_load_dwordx4 v220, s[40:43], s66 offen offset:1024 lds\n\t" \
  "buffer_load_dwordx4 v220, s[40:43], s66 offen offset:2048 lds\n\t" \
  "buffer_load_dwordx4 v220, s[40:43], s66 offen offset:3072 lds\n\t" \
  "L_nodmaF%=:\n\t" \
  "L_fin2%=:\n\t" \
  "v_add_f32_e32 v216, v216, v217\n\t" \
  "v_mov_b32_e32 v217, v216\n\t" \
  "s_nop 1\n\t" \
  "v_permlane32_swap_b32_e32 v216, v217\n\t" \
  "s_nop 1\n\t" \
  "v_add_f32_e32 v216, v216, v217\n\t" \
  "v_rcp_f32_e32 v229, v216\n\t" \
  "v_mov_b32_e32 v230, 0x5f800000\n\t" \
  "v_cmp_nlt_f32_e64 s[88:89], v216, v230\n\t" \
  "v_mov_b32_e32 v231, 0\n\t" \
  "s_nop 7\n\t" \
  "v_mov_b32_e32 v232, v229\n\t" \
  "v_mov_b32_e32 v233, v229\n\t" \
  "v_mov_b32_e32 v234, 0\n\t" \
  "v_mov_b32_e32 v235, 0\n\t" \
  "v_mov_b32_e32 v236, 0\n\t" \
  "v_mov_b32_e32 v237, 0\n\t" \
  "v_pk_mul_f32 v[0:1], v[0:1], v[232:233]\n\t" \
  "v_pk_mul_f32 v[2:3], v[2:3], v[232:233]\n\t" \
  "v_pk_mul_f32 v[4:5], v[4:5], v[232:233]\n\t" \
  "v_pk_mul_f32 v[6:7], v[6:7], v[232:233]\n\t" \
  "v_pk_mul_f32 v[8:9], v[8:9], v[232:233]\n\t" \
  "v_pk_mul_f32 v[10:11], v[10:11], v[232:233]\n\t" \
  "v_pk_mul_f32 v[12:13], v[12:13], v[232:233]\n\t" \
  "v_pk_mul_f32 v[14:15], v[14:15], v[232:233]\n\t" \
  "v_pk_mul_f32 v[16:17], v[16:17], v[232:233]\n\t" \
  "v_pk_mul_f32 v[18:19], v[18:19], v[232:233]\n\t" \
  "v_pk_mul_f32 v[20:21], v[20:21], v[232:233]\n\t" \
  "v_pk_mul_f32 v[22:23], v[22:23], v[232:233]\n\t" \
  "v_pk_mul_f32 v[24:25], v[24:25], v[232:233]\n\t" \
  "v_pk_mul_f32 v[26:27], v[26:27], v[232:233]\n\t" \
  "v_pk_mul_f32 v[28:29], v[28:29], v[232:233]\n\t" \
  "v_pk_mul_f32 v[30:31], v[30:31], v[232:233]\n\t" \
  "v_pk_mul_f32 v[32:33], v[32:33], v[232:233]\n\t" \
  "v_pk_mul_f32 v[34:35], v[34:35], v[232:233]\n\t" \
  "v_pk_mul_f32 v[36:37], v[36:37], v[232:233]\n\t" \
  "v_pk_mul_f32 v[38:39], v[38:39], v[232:233]\n\t" \
  "v_pk_mul_f32 v[40:41], v[40:41], v[232:233]\n\t" \
  "v_pk_mul_f32 v[42:43], v[42:43], v[232:233]\n\t" \
  "v_pk_mul_f32 v[44:45], v[44:45], v[232:233]\n\t" \
  "v_pk_mul_f32 v[46:47], v[46:47], v[232:233]\n\t" \
  "v_pk_mul_f32 v[48:49], v[48:49], v[232:233]\n\t" \
  "v_pk_mul_f32 v[50:51], v[50:51], v[232:233]\n\t" \
  "v_pk_mul_f32 v[52:53], v[52:53], v[232:233]\n\t" \
  "v_pk_mul_f32 v[54:55], v[54:55], v[232:233]\n\t" \
  "v_pk_mul_f32 v[56:57], v[56:57], v[232:233]\n\t" \
  "v_pk_mul_f32 v[58:59], v[58:59], v[232:233]\n\t" \
  "v_pk_mul_f32 v[60:61], v[60:61], v[232:233]\n\t" \
  "v_pk_mul_f32 v[62:63], v[62:63], v[232:233]\n\t" \
  "v_pk_fma_f32 v[234:235], v[0:1], v[236:237], v[234:235]\n\t" \
  "v_pk_fma_f32 v[234:235], v[2:3], v[236:237], v[234:235]\n\t" \
  "v_pk_fma_f32 v[234:235], v[4:5], v[236:237], v[234:235]\n\t" \
  "v_pk_fma_f32 v[234:235], v[6:7], v[236:237], v[234:235]\n\t" \
  "v_pk_fma_f32 v[234:235], v[8:9], v[236:237], v[234:235]\n\t" \
  "v_pk_fma_f32 v[234:235], v[10:11], v[236:237], v[234:235]\n\t" \
  "v_pk_fma_f32 v[234:235], v[12:13], v[236:237], v[234:235]\n\t" \
  "v_pk_fma_f32 v[234:235], v[14:15], v[236:237], v[234:235]\n\t" \
  "v_pk_fma_f32 v[234:235], v[16:17], v[236:237], v[234:235]\n\t" \
  "v_pk_fma_f32 v[234:235], v[18:19], v[236:237], v[234:235]\n\t" \
  "v_pk_fma_f32 v[234:235], v[20:21], v[236:237], v[234:235]\n\t" \
  "v_pk_fma_f32 v[234:235], v[22:23], v[236:237], v[234:235]\n\t" \
  "v_pk_fma_f32 v[234:235], v[24:25], v[236:237], v[234:235]\n\t" \
  "v_pk_fma_f32 v[234:235], v[26:27], v[236:237], v[234:235]\n\t" \
  "v_pk_fma_f32 v[234:235], v[28:29], v[236:237], v[234:235]\n\t" \
  "v_pk_fma_f32 v[234:235], v[30:31], v[236:237], v[234:235]\n\t" \
  "v_pk_fma_f32 v[234:235], v[32:33], v[236:237], v[234:235]\n\t" \
  "v_pk_fma_f32 v[234:235], v[34:35], v[236:237], v[234:235]\n\t" \
  "v_pk_fma_f32 v[234:235], v[36:37], v[236:237], v[234:235]\n\t" \
  "v_pk_fma_f32 v[234:235], v[38:39], v[236:237], v[234:235]\n\t" \
  "v_pk_fma_f32 v[234:235], v[40:41], v[236:237], v[234:235]\n\t" \
  "v_pk_fma_f32 v[234:235], v[42:43], v[236:237], v[234:235]\n\t" \
  "v_pk_fma_f32 v[234:235], v[44:45], v[236:237], v[234:235]\n\t" \
  "v_pk_fma_f32 v[234:235], v[46:47], v[236:237], v[234:235]\n\t" \
  "v_pk_fma_f32 v[234:235], v[48:49], v[236:237], v[234:235]\n\t" \
  "v_pk_fma_f32 v[234:235], v[50:51], v[236:237], v[234:235]\n\t" \
  "v_pk_fma_f32 v[234:235], v[52:53], v[236:237], v[234:235]\n\t" \
  "v_pk_fma_f32 v[234:235], v[54:55], v[236:237], v[234:235]\n\t" \
  "v_pk_fma_f32 v[234:235], v[56:57], v[236:237], v[234:235]\n\t" \
  "v_pk_fma_f32 v[234:235], v[58:59], v[236:237], v[234:235]\n\t" \
  "v_pk_fma_f32 v[234:235], v[60:61], v[236:237], v[234:235]\n\t" \
  "v_pk_fma_f32 v[234:235], v[62:63], v[236:237], v[234:235]\n\t" \
  "v_add_f32_e32 v231, v234, v235\n\t" \
  "v_cmp_u_f32_e64 s[84:85], v231, v231\n\t" \
  "s_or_b64 s[88:89], s[88:89], s[84:85]\n\t" \
  "s_cmp_lg_u64 s[88:89], 0\n\t" \
  "s_cselect_b32 s72, 1, 0\n\t" \
  "v_cvt_pk_bf16_f32 v144, v0, v1\n\t" \
  "v_cvt_pk_bf16_f32 v145, v2, v3\n\t" \
  "v_cvt_pk_bf16_f32 v146, v8, v9\n\t" \
  "v_cvt_pk_bf16_f32 v147, v10, v11\n\t" \
  "v_cvt_pk_bf16_f32 v148, v4, v5\n\t" \
  "v_cvt_pk_bf16_f32 v149, v6, v7\n\t" \
  "v_cvt_pk_bf16_f32 v150, v12, v13\n\t" \
  "v_cvt_pk_bf16_f32 v151, v14, v15\n\t" \
  "v_cvt_pk_bf16_f32 v152, v16, v17\n\t" \
  "v_cvt_pk_bf16_f32 v153, v18, v19\n\t" \
  "v_cvt_pk_bf16_f32 v154, v24, v25\n\t" \
  "v_cvt_pk_bf16_f32 v155, v26, v27\n\t" \
  "v_cvt_pk_bf16_f32 v156, v20, v21\n\t" \
  "v_cvt_pk_bf16_f32 v157, v22, v23\n\t" \
  "v_cvt_pk_bf16_f32 v158, v28, v29\n\t" \
  "v_cvt_pk_bf16_f32 v159, v30, v31\n\t" \
  "v_cvt_pk_bf16_f32 v160, v32, v33\n\t" \
  "v_cvt_pk_bf16_f32 v161, v34, v35\n\t" \
  "v_cvt_pk_bf16_f32 v162, v40, v41\n\t" \
  "v_cvt_pk_bf16_f32 v163, v42, v43\n\t" \
  "v_cvt_pk_bf16_f32 v164, v36, v37\n\t" \
  "v_cvt_pk_bf16_f32 v165, v38, v39\n\t" \
  "v_cvt_pk_bf16_f32 v166, v44, v45\n\t" \
  "v_cvt_pk_bf16_f32 v167, v46, v47\n\t" \
  "v_cvt_pk_bf16_f32 v168, v48, v49\n\t" \
  "v_cvt_pk_bf16_f32 v169, v50, v51\n\t" \
  "v_cvt_pk_bf16_f32 v170, v56, v57\n\t" \
  "v_cvt_pk_bf16_f32 v171, v58, v59\n\t" \
  "v_cvt_pk_bf16_f32 v172, v52, v53\n\t" \
  "v_cvt_pk_bf16_f32 v173, v54, v55\n\t" \
  "v_cvt_pk_bf16_f32 v174, v60, v61\n\t" \
  "v_cvt_pk_bf16_f32 v175, v62, v63\n\t" \
  "s_lshl_b32 s58, s73, 3\n\t" \
  "s_lshl_b32 s59, s57, 1\n\t" \
  "s_add_u32 s58, s58, s59\n\t" \
  "s_lshl_b32 s58, s58, 7\n\t" \
  "s_lshl_b32 s59, s80, 2\n\t" \
  "s_add_u32 s58, s58, s59\n\t" \
  "s_lshl_b32 s58, s58, 10\n\t" \
  "s_add_u32 s48, s78, s58\n\t" \
  "s_addc_u32 s49, s79, 0\n\t" \
  "s_cmp_eq_u32 s56, 0\n\t" \
  "s_cbranch_scc0 L_g1st%=\n\t" \
  "global_store_dwordx4 v225, v[144:147], s[48:49] offset:0 sc1\n\t" \
  "global_store_dwordx4 v225, v[148:151], s[48:49] offset:512 sc1\n\t" \
  "global_store_dwordx4 v225, v[152:155], s[48:49] offset:1024 sc1\n\t" \
  "global_store_dwordx4 v225, v[156:159], s[48:49] offset:1536 sc1\n\t" \
  "global_store_dwordx4 v225, v[160:163], s[48:49] offset:2048 sc1\n\t" \
  "global_store_dwordx4 v225, v[164:167], s[48:49] offset:2560 sc1\n\t" \
  "global_store_dwordx4 v225, v[168:171], s[48:49] offset:3072 sc1\n\t" \
  "global_store_dwordx4 v225, v[172:175], s[48:49] offset:3584 sc1\n\t" \
  "s_branch L_done%=\n\t" \
  "L_g1st%=:\n\t" \
  "global_store_dwordx4 v225, v[144:147], s[48:49] offset:0 sc1\n\t" \
  "global_store_dwordx4 v225, v[148:151], s[48:49] offset:512 sc1\n\t" \
  "global_store_dwordx4 v225, v[152:155], s[48:49] offset:1024 sc1\n\t" \
  "s_cmp_ge_u32 s53, s75\n\t" \
  "s_cbranch_scc1 L_rest0%=\n\t" \
  "s_waitcnt vmcnt(3) lgkmcnt(0)\n\t" \
  "s_barrier\n\t" \
  "s_xor_b32 s52, s52, 0x8000\n\t" \
  "s_add_u32 s53, s53, 1\n\t" \
  "s_cmp_ge_u32 s53, s75\n\t" \
  "s_cbranch_scc1 L_nodmaI0%=\n\t" \
  "s_add_u32 s58, s53, 1\n\t" \
  "s_lshl_b32 s62, s58, 15\n\t" \
  "s_add_u32 s61, s62, 16384\n\t" \
  "s_xor_b32 s63, s52, 0x8000\n\t" \
  "s_add_u32 m0, s50, s63\n\t" \
  "s_add_u32 s66, s61, 4096\n\t" \
  "buffer_load_dwordx4 v220, s[36:39], s61 offen offset:0 lds\n\t" \
  "buffer_load_dwordx4 v220, s[36:39], s61 offen offset:1024 lds\n\t" \
  "buffer_load_dwordx4 v220, s[36:39], s61 offen offset:2048 lds\n\t" \
  "buffer_load_dwordx4 v220, s[36:39], s61 offen offset:3072 lds\n\t" \
  "s_add_u32 m0, m0, 4096\n\t" \
  "s_nop 0\n\t" \
  "buffer_load_dwordx4 v220, s[36:39], s66 offen offset:0 lds\n\t" \
  "buffer_load_dwordx4 v220, s[36:39], s66 offen offset:1024 lds\n\t" \
  "buffer_load_dwordx4 v220, s[36:39], s66 offen offset:2048 lds\n\t" \
  "buffer_load_dwordx4 v220, s[36:39], s66 offen offset:3072 lds\n\t" \
  "s_add_u32 m0, s51, s63\n\t" \
  "s_add_u32 s66, s62, 4096\n\t" \
  "buffer_load_dwordx4 v220, s[40:43], s62 offen offset:0 lds\n\t" \
  "buffer_load_dwordx4 v220, s[40:43], s62 offen offset:1024 lds\n\t" \
  "buffer_load_dwordx4 v220, s[40:43], s62 offen offset:2048 lds\n\t" \
  "buffer_load_dwordx4 v220, s[40:43], s62 offen offset:3072 lds\n\t" \
  "s_add_u32 m0, m0, 4096\n\t" \
  "s_nop 0\n\t" \
  "buffer_load_dwordx4 v220, s[40:43], s66 offen offset:0 lds\n\t" \
  "buffer_load_dwordx4 v220, s[40:43], s66 offen offset:1024 lds\n\t" \
  "buffer_load_dwordx4 v220, s[40:43], s66 offen offset:2048 lds\n\t" \
  "buffer_load_dwordx4 v220, s[40:43], s66 offen offset:3072 lds\n\t" \
  "L_nodmaI0%=:\n\t" \
  "global_store_dwordx4 v225, v[156:159], s[48:49] offset:1536 sc1\n\t" \
  "global_store_dwordx4 v225, v[160:163], s[48:49] offset:2048 sc1\n\t" \
  "global_store_dwordx4 v225, v[164:167], s[48:49] offset:2560 sc1\n\t" \
  "s_cmp_ge_u32 s53, s75\n\t" \
  "s_cbranch_scc1 L_rest1%=\n\t" \
  "s_waitcnt vmcnt(3) lgkmcnt(0)\n\t" \
  "s_barrier\n\t" \
  "s_xor_b32 s52, s52, 0x8000\n\t" \
  "s_add_u32 s53, s53, 1\n\t" \
  "s_cmp_ge_u32 s53, s75\n\t" \
  "s_cbranch_scc1 L_nodmaI1%=\n\t" \
  "s_add_u32 s58, s53, 1\n\t" \
  "s_lshl_b32 s62, s58, 15\n\t" \
  "s_add_u32 s61, s62, 16384\n\t" \
  "s_xor_b32 s63, s52, 0x8000\n\t" \
  "s_add_u32 m0, s50, s63\n\t" \
  "s_add_u32 s66, s61, 4096\n\t" \
  "buffer_load_dwordx4 v220, s[36:39], s61 offen offset:0 lds\n\t" \
  "buffer_load_dwordx4 v220, s[36:39], s61 offen offset:1024 lds\n\t" \
  "buffer_load_dwordx4 v220, s[36:39], s61 offen offset:2048 lds\n\t" \
  "buffer_load_dwordx4 v220, s[36:39], s61 offen offset:3072 lds\n\t" \
  "s_add_u32 m0, m0, 4096\n\t" \
  "s_nop 0\n\t" \
  "buffer_load_dwordx4 v220, s[36:39], s66 offen offset:0 lds\n\t" \
  "buffer_load_dwordx4 v220, s[36:39], s66 offen offset:1024 lds\n\t" \
  "buffer_load_dwordx4 v220, s[36:39], s66 offen offset:2048 lds\n\t" \
  "buffer_load_dwordx4 v220, s[36:39], s66 offen offset:3072 lds\n\t" \
  "s_add_u32 m0, s51, s63\n\t" \
  "s_add_u32 s66, s62, 4096\n\t" \
  "buffer_load_dwordx4 v220, s[40:43], s62 offen offset:0 lds\n\t" \
  "buffer_load_dwordx4 v220, s[40:43], s62 offen offset:1024 lds\n\t" \
  "buffer_load_dwordx4 v220, s[40:43], s62 offen offset:2048 lds\n\t" \
  "buffer_load_dwordx4 v220, s[40:43], s62 offen offset:3072 lds\n\t" \
  "s_add_u32 m0, m0, 4096\n\t" \
  "s_nop 0\n\t" \
  "buffer_load_dwordx4 v220, s[40:43], s66 offen offset:0 lds\n\t" \
  "buffer_load_dwordx4 v220, s[40:43], s66 offen offset:1024 lds\n\t" \
  "buffer_load_dwordx4 v220, s[40:43], s66 offen offset:2048 lds\n\t" \
  "buffer_load_dwordx4 v220, s[40:43], s66 offen offset:3072 lds\n\t" \
  "L_nodmaI1%=:\n\t" \
  "global_store_dwordx4 v225, v[168:171], s[48:49] offset:3072 sc1\n\t" \
  "global_store_dwordx4 v225, v[172:175], s[48:49] offset:3584 sc1\n\t" \
  "global_load_dword v242, %[pf0], off\n\t" \
  "global_load_dword v243, %[pf1], off\n\t" \
  "global_load_dword v244, %[pf2], off\n\t" \
  "s_cmp_ge_u32 s53, s75\n\t" \
  "s_cbranch_scc1 L_rest2%=\n\t" \
  "s_waitcnt vmcnt(5) lgkmcnt(0)\n\t" \
  "s_barrier\n\t" \
  "s_xor_b32 s52, s52, 0x8000\n\t" \
  "s_add_u32 s53, s53, 1\n\t" \
  "s_cmp_ge_u32 s53, s75\n\t" \
  "s_cbranch_scc1 L_nodmaI2%=\n\t" \
  "s_add_u32 s58, s53, 1\n\t" \
  "s_lshl_b32 s62, s58, 15\n\t" \
  "s_add_u32 s61, s62, 16384\n\t" \
  "s_xor_b32 s63, s52, 0x8000\n\t" \
  "s_add_u32 m0, s50, s63\n\t" \
  "s_add_u32 s66, s61, 4096\n\t" \
  "buffer_load_dwordx4 v220, s[36:39], s61 offen offset:0 lds\n\t" \
  "buffer_load_dwordx4 v220, s[36:39], s61 offen offset:1024 lds\n\t" \
  "buffer_load_dwordx4 v220, s[36:39], s61 offen offset:2048 lds\n\t" \
  "buffer_load_dwordx4 v220, s[36:39], s61 offen offset:3072 lds\n\t" \
  "s_add_u32 m0, m0, 4096\n\t" \
  "s_nop 0\n\t" \
  "buffer_load_dwordx4 v220, s[36:39], s66 offen offset:0 lds\n\t" \
  "buffer_load_dwordx4 v220, s[36:39], s66 offen offset:1024 lds\n\t" \
  "buffer_load_dwordx4 v220, s[36:39], s66 offen offset:2048 lds\n\t" \
  "buffer_load_dwordx4 v220, s[36:39], s66 offen offset:3072 lds\n\t" \
  "s_add_u32 m0, s51, s63\n\t" \
  "s_add_u32 s66, s62, 4096\n\t" \
  "buffer_load_dwordx4 v220, s[40:43], s62 offen offset:0 lds\n\t" \
  "buffer_load_dwordx4 v220, s[40:43], s62 offen offset:1024 lds\n\t" \
  "buffer_load_dwordx4 v220, s[40:43], s62 offen offset:2048 lds\n\t" \
  "buffer_load_dwordx4 v220, s[40:43], s62 offen offset:3072 lds\n\t" \
  "s_add_u32 m0, m0, 4096\n\t" \
  "s_nop 0\n\t" \
  "buffer_load_dwordx4 v220, s[40:43], s66 offen offset:0 lds\n\t" \
  "buffer_load_dwordx4 v220, s[40:43], s66 offen offset:1024 lds\n\t" \
  "buffer_load_dwordx4 v220, s[40:43], s66 offen offset:2048 lds\n\t" \
  "buffer_load_dwordx4 v220, s[40:43], s66 offen offset:3072 lds\n\t" \
  "L_nodmaI2%=:\n\t" \
  "L_idle%=:\n\t" \
  "s_cmp_ge_u32 s53, s75\n\t" \
  "s_cbranch_scc1 L_done%=\n\t" \
  "s_waitcnt vmcnt(0) lgkmcnt(0)\n\t" \
  "s_barrier\n\t" \
  "s_xor_b32 s52, s52, 0x8000\n\t" \
  "s_add_u32 s53, s53, 1\n\t" \
  "s_cmp_ge_u32 s53, s75\n\t" \
  "s_cbranch_scc1 L_nodmaI%=\n\t" \
  "s_add_u32 s58, s53, 1\n\t" \
  "s_lshl_b32 s62, s58, 15\n\t" \
  "s_add_u32 s61, s62, 16384\n\t" \
  "s_xor_b32 s63, s52, 0x8000\n\t" \
  "s_add_u32 m0, s50, s63\n\t" \
  "s_add_u32 s66, s61, 4096\n\t" \
  "buffer_load_dwordx4 v220, s[36:39], s61 offen offset:0 lds\n\t" \
  "buffer_load_dwordx4 v220, s[36:39], s61 offen offset:1024 lds\n\t" \
  "buffer_load_dwordx4 v220, s[36:39], s61 offen offset:2048 lds\n\t" \
  "buffer_load_dwordx4 v220, s[36:39], s61 offen offset:3072 lds\n\t" \
  "s_add_u32 m0, m0, 4096\n\t" \
  "s_nop 0\n\t" \
  "buffer_load_dwordx4 v220, s[36:39], s66 offen offset:0 lds\n\t" \
  "buffer_load_dwordx4 v220, s[36:39], s66 offen offset:1024 lds\n\t" \
  "buffer_load_dwordx4 v220, s[36:39], s66 offen offset:2048 lds\n\t" \
  "buffer_load_dwordx4 v220, s[36:39], s66 offen offset:3072 lds\n\t" \
  "s_add_u32 m0, s51, s63\n\t" \
  "s_add_u32 s66, s62, 4096\n\t" \
  "buffer_load_dwordx4 v220, s[40:43], s62 offen offset:0 lds\n\t" \
  "buffer_load_dwordx4 v220, s[40:43], s62 offen offset:1024 lds\n\t" \
  "buffer_load_dwordx4 v220, s[40:43], s62 offen offset:2048 lds\n\t" \
  "buffer_load_dwordx4 v220, s[40:43], s62 offen offset:3072 lds\n\t" \
  "s_add_u32 m0, m0, 4096\n\t" \
  "s_nop 0\n\t" \
  "buffer_load_dwordx4 v220, s[40:43], s66 offen offset:0 lds\n\t" \
  "buffer_load_dwordx4 v220, s[40:43], s66 offen offset:1024 lds\n\t" \
  "buffer_load_dwordx4 v220, s[40:43], s66 offen offset:2048 lds\n\t" \
  "buffer_load_dwordx4 v220, s[40:43], s66 offen offset:3072 lds\n\t" \
  "L_nodmaI%=:\n\t" \
  "s_branch L_idle%=\n\t" \
  "L_rest0%=:\n\t" \
  "global_store_dwordx4 v225, v[156:159], s[48:49] offset:1536 sc1\n\t" \
  "global_store_dwordx4 v225, v[160:163], s[48:49] offset:2048 sc1\n\t" \
  "global_store_dwordx4 v225, v[164:167], s[48:49] offset:2560 sc1\n\t" \
  "L_rest1%=:\n\t" \
  "global_store_dwordx4 v225, v[168:171], s[48:49] offset:3072 sc1\n\t" \
  "global_store_dwordx4 v225, v[172:175], s[48:49] offset:3584 sc1\n\t" \
  "global_load_dword v242, %[pf0], off\n\t" \
  "global_load_dword v243, %[pf1], off\n\t" \
  "global_load_dword v244, %[pf2], off\n\t" \
  "L_rest2%=:\n\t" \
  "L_done%=:\n\t" \
  "s_waitcnt vmcnt(0)\n\t" \
  "s_mov_b32 %[flag], s72\n\t" \
  ""
#define ATTN_ASM_CLOBBERS "memory","vcc","scc","v0","v1","v2","v3","v4","v5","v6","v7","v8","v9","v10","v11","v12","v13","v14","v15","v16","v17","v18","v19","v20","v21","v22","v23","v24","v25","v26","v27","v28","v29","v30","v31","v32","v33","v34","v35","v36","v37","v38","v39","v40","v41","v42","v43","v44","v45","v46","v47","v48","v49","v50","v51","v52","v53","v54","v55","v56","v57","v58","v59","v60","v61","v62","v63","v64","v65","v66","v67","v68","v69","v70","v71","v72","v73","v74","v75","v76","v77","v78","v79","v80","v81","v82","v83","v84","v85","v86","v87","v88","v89","v90","v91","v92","v93","v94","v95","v96","v97","v98","v99","v100","v101","v102","v103","v104","v105","v106","v107","v108","v109","v110","v111","v112","v113","v114","v115","v116","v117","v118","v119","v120","v121","v122","v123","v124","v125","v126","v127","v128","v129","v130","v131","v132","v133","v134","v135","v136","v137","v138","v139","v140","v141","v142","v143","v144","v145","v146","v147","v148","v149","v150","v151","v152","v153","v154","v155","v156","v157","v158","v159","v160","v161","v162","v163","v164","v165","v166","v167","v168","v169","v170","v171","v172","v173","v174","v175","v176","v177","v178","v179","v180","v181","v182","v183","v184","v185","v186","v187","v188","v189","v190","v191","v192","v193","v194","v195","v196","v197","v198","v199","v200","v201","v202","v203","v204","v205","v206","v207","v208","v209","v210","v211","v212","v213","v214","v215","v216","v217","v218","v219","v220","v221","v222","v223","v224","v225","v226","v227","v228","v229","v230","v231","v232","v233","v234","v235","v236","v237","v238","v239","v240","v241","v242","v243","v244","s36","s37","s38","s39","s40","s41","s42","s43","s44","s45","s46","s47","s48","s49","s50","s51","s52","s53","s54","s55","s56","s57","s58","s59","s60","s61","s62","s63","s64","s65","s66","s67","s68","s69","s70","s71","s72","s73","s74","s75","s76","s77","s78","s79","s80","s81","s82","s83","s84","s85","s86","s87","s88","s89","s90","s91","s92","s93","s94","s95"
#define GEMM_FFN1_TEXT \
  "s_mov_b32 s60, %[wid]\n\t" \
  "s_mov_b32 s61, %[lds0]\n\t" \
  "s_mov_b32 s62, %[wpitch]\n\t" \
  "s_mov_b32 s63, %[ars]\n\t" \
  "s_mov_b32 s64, %[ors]\n\t" \
  "s_mov_b32 s65, %[par]\n\t" \
  "s_mov_b64 s[36:37], %[wptr]\n\t" \
  "s_and_b32 s37, s37, 0xffff\n\t" \
  "s_mov_b32 s38, 0x7fffffff\n\t" \
  "s_mov_b32 s39, 0x20000\n\t" \
  "s_lshl_b32 s48, s60, 1\n\t" \
  "s_mul_i32 s49, s48, s63\n\t" \
  "s_mov_b64 s[40:41], %[aptr]\n\t" \
  "s_add_u32 s40, s40, s49\n\t" \
  "s_addc_u32 s41, s41, 0\n\t" \
  "s_and_b32 s41, s41, 0xffff\n\t" \
  "s_mov_b32 s42, 0x7fffffff\n\t" \
  "s_mov_b32 s43, 0x20000\n\t" \
  "s_lshl_b32 s48, s60, 10\n\t" \
  "s_add_u32 s66, s61, s48\n\t" \
  "s_add_u32 s67, s61, 8192\n\t" \
  "s_lshl_b32 s68, s62, 5\n\t" \
  "s_mul_i32 s51, s68, 0\n\t" \
  "s_cmp_lg_u32 s60, 0\n\t" \
  "s_cbranch_scc1 L_p8_0%=\n\t" \
  "s_add_u32 m0, s67, 0\n\t" \
  "s_nop 0\n\t" \
  "buffer_load_dwordx4 %[wvo8], s[36:39], s51 offen lds\n\t" \
  "L_p8_0%=:\n\t" \
  "s_add_u32 m0, s66, 0\n\t" \
  "s_nop 0\n\t" \
  "buffer_load_dwordx4 %[wvo0], s[36:39], s51 offen lds\n\t" \
  "s_mov_b32 s52, 0\n\t" \
  "buffer_load_dwordx4 v[0:3], %[avo], s[40:43], s52 offen offset:0\n\t" \
  "s_add_u32 s52, s63, 0\n\t" \
  "buffer_load_dwordx4 v[72:75], %[avo], s[40:43], s52 offen offset:0\n\t" \
  "s_mul_i32 s51, s68, 1\n\t" \
  "s_cmp_lg_u32 s60, 1\n\t" \
  "s_cbranch_scc1 L_p8_1%=\n\t" \
  "s_add_u32 m0, s67, 9216\n\t" \
  "s_nop 0\n\t" \
  "buffer_load_dwordx4 %[wvo8], s[36:39], s51 offen lds\n\t" \
  "L_p8_1%=:\n\t" \
  "s_add_u32 m0, s66, 9216\n\t" \
  "s_nop 0\n\t" \
  "buffer_load_dwordx4 %[wvo0], s[36:39], s51 offen lds\n\t" \
  "s_mov_b32 s52, 0\n\t" \
  "buffer_load_dwordx4 v[4:7], %[avo], s[40:43], s52 offen offset:1024\n\t" \
  "s_add_u32 s52, s63, 0\n\t" \
  "buffer_load_dwordx4 v[76:79], %[avo], s[40:43], s52 offen offset:1024\n\t" \
  "s_mul_i32 s51, s68, 2\n\t" \
  "s_cmp_lg_u32 s60, 2\n\t" \
  "s_cbranch_scc1 L_p8_2%=\n\t" \
  "s_add_u32 m0, s67, 18432\n\t" \
  "s_nop 0\n\t" \
  "buffer_load_dwordx4 %[wvo8], s[36:39], s51 offen lds\n\t" \
  "L_p8_2%=:\n\t" \
  "s_add_u32 m0, s66, 18432\n\t" \
  "s_nop 0\n\t" \
  "buffer_load_dwordx4 %[wvo0], s[36:39], s51 offen lds\n\t" \
  "s_mov_b32 s52, 0\n\t" \
  "buffer_load_dwordx4 v[8:11], %[avo], s[40:43], s52 offen offset:2048\n\t" \
  "s_add_u32 s52, s63, 0\n\t" \
  "buffer_load_dwordx4 v[80:83], %[avo], s[40:43], s52 offen offset:2048\n\t" \
  "s_mul_i32 s51, s68, 3\n\t" \
  "s_cmp_lg_u32 s60, 3\n\t" \
  "s_cbranch_scc1 L_p8_3%=\n\t" \
  "s_add_u32 m0, s67, 27648\n\t" \
  "s_nop 0\n\t" \
  "buffer_load_dwordx4 %[wvo8], s[36:39], s51 offen lds\n\t" \
  "L_p8_3%=:\n\t" \
  "s_add_u32 m0, s66, 27648\n\t" \
  "s_nop 0\n\t" \
  "buffer_load_dwordx4 %[wvo0], s[36:39], s51 offen lds\n\t" \
  "s_mov_b32 s52, 0\n\t" \
  "buffer_load_dwordx4 v[12:15], %[avo], s[40:43], s52 offen offset:3072\n\t" \
  "s_add_u32 s52, s63, 0\n\t" \
  "buffer_load_dwordx4 v[84:87], %[avo], s[40:43], s52 offen offset:3072\n\t" \
  "s_mul_i32 s51, s68, 4\n\t" \
  "s_cmp_lg_u32 s60, 4\n\t" \
  "s_cbranch_scc1 L_p8_4%=\n\t" \
  "s_add_u32 m0, s67, 36864\n\t" \
  "s_nop 0\n\t" \
  "buffer_load_dwordx4 %[wvo8], s[36:39], s51 offen lds\n\t" \
  "L_p8_4%=:\n\t" \
  "s_add_u32 m0, s66, 36864\n\t" \
  "s_nop 0\n\t" \
  "buffer_load_dwordx4 %[wvo0], s[36:39], s51 offen lds\n\t" \
  "s_lshl_b32 s48, s60, 1\n\t" \
  "s_mul_i32 s49, s48, s64\n\t" \
  "s_mov_b64 s[44:45], %[optr]\n\t" \
  "s_add_u32 s44, s44, s49\n\t" \
  "s_addc_u32 s45, s45, 0\n\t" \
  "s_and_b32 s45, s45, 0xffff\n\t" \
  "s_mov_b32 s46, 0x7fffffff\n\t" \
  "s_mov_b32 s47, 0x20000\n\t" \
  "s_lshl_b32 s69, s65, 10\n\t" \
  "s_add_u32 s71, s69, s64\n\t" \
  "s_cmp_lg_u32 s65, 0\n\t" \
  "s_cselect_b32 s70, 8, 4096\n\t" \
  "s_add_u32 s72, s70, s64\n\t" \
  "s_lshl_b32 s48, s65, 5\n\t" \
  "v_add_u32_e32 v240, s48, %[tro]\n\t" \
  "s_cmp_lg_u32 s65, 0\n\t" \
  "s_cselect_b32 s48, 0, 256\n\t" \
  "v_add_u32_e32 v241, s48, %[tro]\n\t" \
  "s_waitcnt vmcnt(12)\n\t" \
  "s_barrier\n\t" \
  "ds_read_b64_tr_b16 v[216:217], v240 offset:0\n\t" \
  "ds_read_b64_tr_b16 v[218:219], v240 offset:4608\n\t" \
  "ds_read_b64_tr_b16 v[220:221], v240 offset:32\n\t" \
  "ds_read_b64_tr_b16 v[222:223], v240 offset:4640\n\t" \
  "ds_read_b64_tr_b16 v[224:225], v240 offset:64\n\t" \
  "ds_read_b64_tr_b16 v[226:227], v240 offset:4672\n\t" \
  "ds_read_b64_tr_b16 v[228:229], v240 offset:96\n\t" \
  "ds_read_b64_tr_b16 v[230:231], v240 offset:4704\n\t" \
  "ds_read_b64_tr_b16 v[232:233], v240 offset:128\n\t" \
  "ds_read_b64_tr_b16 v[234:235], v240 offset:4736\n\t" \
  "ds_read_b64_tr_b16 v[236:237], v240 offset:160\n\t" \
  "ds_read_b64_tr_b16 v[238:239], v240 offset:4768\n\t" \
  "s_waitcnt vmcnt(10)\n\t" \
  "s_waitcnt lgkmcnt(10)\n\t" \
  "v_mfma_f32_16x16x32_bf16 v[144:147], v[216:219], v[0:3], 0\n\t" \
  "v_mfma_f32_16x16x32_bf16 v[148:151], v[216:219], v[72:75], 0\n\t" \
  "ds_read_b64_tr_b16 v[216:217], v240 offset:192\n\t" \
  "ds_read_b64_tr_b16 v[218:219], v240 offset:4800\n\t" \
  "s_waitcnt lgkmcnt(10)\n\t" \
  "v_mfma_f32_16x16x32_bf16 v[152:155], v[220:223], v[0:3], 0\n\t" \
  "v_mfma_f32_16x16x32_bf16 v[156:159], v[220:223], v[72:75], 0\n\t" \
  "s_mul_i32 s51, s68, 5\n\t" \
  "s_cmp_lg_u32 s60, 5\n\t" \
  "s_cbranch_scc1 L_p8_5%=\n\t" \
  "s_add_u32 m0, s67, 46080\n\t" \
  "s_nop 0\n\t" \
  "buffer_load_dwordx4 %[wvo8], s[36:39], s51 offen lds\n\t" \
  "L_p8_5%=:\n\t" \
  "s_add_u32 m0, s66, 46080\n\t" \
  "s_nop 0\n\t" \
  "buffer_load_dwordx4 %[wvo0], s[36:39], s51 offen lds\n\t" \
  "ds_read_b64_tr_b16 v[220:221], v240 offset:224\n\t" \
  "ds_read_b64_tr_b16 v[222:223], v240 offset:4832\n\t" \
  "s_waitcnt lgkmcnt(10)\n\t" \
  "v_mfma_f32_16x16x32_bf16 v[160:163], v[224:227], v[0:3], 0\n\t" \
  "v_mfma_f32_16x16x32_bf16 v[164:167], v[224:227], v[72:75], 0\n\t" \
  "ds_read_b64_tr_b16 v[224:225], v241 offset:0\n\t" \
  "ds_read_b64_tr_b16 v[226:227], v241 offset:4608\n\t" \
  "s_waitcnt lgkmcnt(10)\n\t" \
  "v_mfma_f32_16x16x32_bf16 v[168:171], v[228:231], v[0:3], 0\n\t" \
  "v_mfma_f32_16x16x32_bf16 v[172:175], v[228:231], v[72:75], 0\n\t" \
  "s_mov_b32 s52, 4096\n\t" \
  "buffer_load_dwordx4 v[16:19], %[avo], s[40:43], s52 offen offset:0\n\t" \
  "s_waitcnt vmcnt(8)\n\t" \
  "s_barrier\n\t" \
  "ds_read_b64_tr_b16 v[228:229], v240 offset:9216\n\t" \
  "ds_read_b64_tr_b16 v[230:231], v240 offset:13824\n\t" \
  "s_waitcnt lgkmcnt(10)\n\t" \
  "v_mfma_f32_16x16x32_bf16 v[176:179], v[232:235], v[0:3], 0\n\t" \
  "v_mfma_f32_16x16x32_bf16 v[180:183], v[232:235], v[72:75], 0\n\t" \
  "ds_read_b64_tr_b16 v[232:233], v240 offset:9248\n\t" \
  "ds_read_b64_tr_b16 v[234:235], v240 offset:13856\n\t" \
  "s_waitcnt lgkmcnt(10)\n\t" \
  "v_mfma_f32_16x16x32_bf16 v[184:187], v[236:239], v[0:3], 0\n\t" \
  "v_mfma_f32_16x16x32_bf16 v[188:191], v[236:239], v[72:75], 0\n\t" \
  "s_add_u32 s52, s63, 4096\n\t" \
  "buffer_load_dwordx4 v[88:91], %[avo], s[40:43], s52 offen offset:0\n\t" \
  "ds_read_b64_tr_b16 v[236:237], v240 offset:9280\n\t" \
  "ds_read_b64_tr_b16 v[238:239], v240 offset:13888\n\t" \
  "s_waitcnt lgkmcnt(10)\n\t" \
  "v_mfma_f32_16x16x32_bf16 v[192:195], v[216:219], v[0:3], 0\n\t" \
  "v_mfma_f32_16x16x32_bf16 v[196:199], v[216:219], v[72:75], 0\n\t" \
  "ds_read_b64_tr_b16 v[216:217], v240 offset:9312\n\t" \
  "ds_read_b64_tr_b16 v[218:219], v240 offset:13920\n\t" \
  "s_waitcnt lgkmcnt(10)\n\t" \
  "v_mfma_f32_16x16x32_bf16 v[200:203], v[220:223], v[0:3], 0\n\t" \
  "v_mfma_f32_16x16x32_bf16 v[204:207], v[220:223], v[72:75], 0\n\t" \
  "ds_read_b64_tr_b16 v[220:221], v240 offset:9344\n\t" \
  "ds_read_b64_tr_b16 v[222:223], v240 offset:13952\n\t" \
  "s_waitcnt lgkmcnt(10)\n\t" \
  "v_mfma_f32_16x16x32_bf16 v[208:211], v[224:227], v[0:3], 0\n\t" \
  "v_mfma_f32_16x16x32_bf16 v[212:215], v[224:227], v[72:75], 0\n\t" \
  "ds_read_b64_tr_b16 v[224:225], v240 offset:9376\n\t" \
  "ds_read_b64_tr_b16 v[226:227], v240 offset:13984\n\t" \
  "s_waitcnt lgkmcnt(10)\n\t" \
  "v_mfma_f32_16x16x32_bf16 v[144:147], v[228:231], v[4:7], v[144:147]\n\t" \
  "v_mfma_f32_16x16x32_bf16 v[148:151], v[228:231], v[76:79], v[148:151]\n\t" \
  "s_mul_i32 s51, s68, 6\n\t" \
  "s_cmp_lg_u32 s60, 6\n\t" \
  "s_cbranch_scc1 L_p8_6%=\n\t" \
  "s_add_u32 m0, s67, 55296\n\t" \
  "s_nop 0\n\t" \
  "buffer_load_dwordx4 %[wvo8], s[36:39], s51 offen lds\n\t" \
  "L_p8_6%=:\n\t" \
  "s_add_u32 m0, s66, 55296\n\t" \
  "s_nop 0\n\t" \
  "buffer_load_dwordx4 %[wvo0], s[36:39], s51 offen lds\n\t" \
  "ds_read_b64_tr_b16 v[228:229], v240 offset:18432\n\t" \
  "ds_read_b64_tr_b16 v[230:231], v240 offset:23040\n\t" \
  "s_waitcnt lgkmcnt(10)\n\t" \
  "v_mfma_f32_16x16x32_bf16 v[152:155], v[232:235], v[4:7], v[152:155]\n\t" \
  "v_mfma_f32_16x16x32_bf16 v[156:159], v[232:235], v[76:79], v[156:159]\n\t" \
  "ds_read_b64_tr_b16 v[232:233], v240 offset:18464\n\t" \
  "ds_read_b64_tr_b16 v[234:235], v240 offset:23072\n\t" \
  "s_waitcnt lgkmcnt(10)\n\t" \
  "v_mfma_f32_16x16x32_bf16 v[160:163], v[236:239], v[4:7], v[160:163]\n\t" \
  "v_mfma_f32_16x16x32_bf16 v[164:167], v[236:239], v[76:79], v[164:167]\n\t" \
  "s_mov_b32 s52, 4096\n\t" \
  "buffer_load_dwordx4 v[20:23], %[avo], s[40:43], s52 offen offset:1024\n\t" \
  "ds_read_b64_tr_b16 v[236:237], v240 offset:18496\n\t" \
  "ds_read_b64_tr_b16 v[238:239], v240 offset:23104\n\t" \
  "s_waitcnt lgkmcnt(10)\n\t" \
  "v_mfma_f32_16x16x32_bf16 v[168:171], v[216:219], v[4:7], v[168:171]\n\t" \
  "v_mfma_f32_16x16x32_bf16 v[172:175], v[216:219], v[76:79], v[172:175]\n\t" \
  "ds_read_b64_tr_b16 v[216:217], v240 offset:18528\n\t" \
  "ds_read_b64_tr_b16 v[218:219], v240 offset:23136\n\t" \
  "s_waitcnt lgkmcnt(10)\n\t" \
  "v_mfma_f32_16x16x32_bf16 v[176:179], v[220:223], v[4:7], v[176:179]\n\t" \
  "v_mfma_f32_16x16x32_bf16 v[180:183], v[220:223], v[76:79], v[180:183]\n\t" \
  "s_add_u32 s52, s63, 4096\n\t" \
  "buffer_load_dwordx4 v[92:95], %[avo], s[40:43], s52 offen offset:1024\n\t" \
  "ds_read_b64_tr_b16 v[220:221], v240 offset:18560\n\t" \
  "ds_read_b64_tr_b16 v[222:223], v240 offset:23168\n\t" \
  "s_waitcnt lgkmcnt(10)\n\t" \
  "v_mfma_f32_16x16x32_bf16 v[184:187], v[224:227], v[4:7], v[184:187]\n\t" \
  "v_mfma_f32_16x16x32_bf16 v[188:191], v[224:227], v[76:79], v[188:191]\n\t" \
  "ds_read_b64_tr_b16 v[224:225], v240 offset:18592\n\t" \
  "ds_read_b64_tr_b16 v[226:227], v240 offset:23200\n\t" \
  "s_waitcnt vmcnt(10)\n\t" \
  "s_waitcnt lgkmcnt(10)\n\t" \
  "v_mfma_f32_16x16x32_bf16 v[144:147], v[228:231], v[8:11], v[144:147]\n\t" \
  "v_mfma_f32_16x16x32_bf16 v[148:151], v[228:231], v[80:83], v[148:151]\n\t" \
  "s_mul_i32 s51, s68, 7\n\t" \
  "s_cmp_lg_u32 s60, 7\n\t" \
  "s_cbranch_scc1 L_p8_7%=\n\t" \
  "s_add_u32 m0, s67, 64512\n\t" \
  "s_nop 0\n\t" \
  "buffer_load_dwordx4 %[wvo8], s[36:39], s51 offen lds\n\t" \
  "L_p8_7%=:\n\t" \
  "s_add_u32 m0, s66, 64512\n\t" \
  "s_nop 0\n\t" \
  "buffer_load_dwordx4 %[wvo0], s[36:39], s51 offen lds\n\t" \
  "s_waitcnt vmcnt(6)\n\t" \
  "s_barrier\n\t" \
  "ds_read_b64_tr_b16 v[228:229], v240 offset:27648\n\t" \
  "ds_read_b64_tr_b16 v[230:231], v240 offset:32256\n\t" \
  "s_waitcnt lgkmcnt(10)\n\t" \
  "v_mfma_f32_16x16x32_bf16 v[152:155], v[232:235], v[8:11], v[152:155]\n\t" \
  "v_mfma_f32_16x16x32_bf16 v[156:159], v[232:235], v[80:83], v[156:159]\n\t" \
  "ds_read_b64_tr_b16 v[232:233], v240 offset:27680\n\t" \
  "ds_read_b64_tr_b16 v[234:235], v240 offset:32288\n\t" \
  "s_waitcnt lgkmcnt(10)\n\t" \
  "v_mfma_f32_16x16x32_bf16 v[160:163], v[236:239], v[8:11], v[160:163]\n\t" \
  "v_mfma_f32_16x16x32_bf16 v[164:167], v[236:239], v[80:83], v[164:167]\n\t" \
  "s_mov_b32 s52, 4096\n\t" \
  "buffer_load_dwordx4 v[24:27], %[avo], s[40:43], s52 offen offset:2048\n\t" \
  "ds_read_b64_tr_b16 v[236:237], v240 offset:27712\n\t" \
  "ds_read_b64_tr_b16 v[238:239], v240 offset:32320\n\t" \
  "s_waitcnt lgkmcnt(10)\n\t" \
  "v_mfma_f32_16x16x32_bf16 v[168:171], v[216:219], v[8:11], v[168:171]\n\t" \
  "v_mfma_f32_16x16x32_bf16 v[172:175], v[216:219], v[80:83], v[172:175]\n\t" \
  "ds_read_b64_tr_b16 v[216:217], v240 offset:27744\n\t" \
  "ds_read_b64_tr_b16 v[218:219], v240 offset:32352\n\t" \
  "s_waitcnt lgkmcnt(10)\n\t" \
  "v_mfma_f32_16x16x32_bf16 v[176:179], v[220:223], v[8:11], v[176:179]\n\t" \
  "v_mfma_f32_16x16x32_bf16 v[180:183], v[220:223], v[80:83], v[180:183]\n\t" \
  "s_add_u32 s52, s63, 4096\n\t" \
  "buffer_load_dwordx4 v[96:99], %[avo], s[40:43], s52 offen offset:2048\n\t" \
  "ds_read_b64_tr_b16 v[220:221], v240 offset:27776\n\t" \
  "ds_read_b64_tr_b16 v[222:223], v240 offset:32384\n\t" \
  "s_waitcnt lgkmcnt(10)\n\t" \
  "v_mfma_f32_16x16x32_bf16 v[184:187], v[224:227], v[8:11], v[184:187]\n\t" \
  "v_mfma_f32_16x16x32_bf16 v[188:191], v[224:227], v[80:83], v[188:191]\n\t" \
  "ds_read_b64_tr_b16 v[224:225], v240 offset:27808\n\t" \
  "ds_read_b64_tr_b16 v[226:227], v240 offset:32416\n\t" \
  "s_waitcnt lgkmcnt(10)\n\t" \
  "v_mfma_f32_16x16x32_bf16 v[144:147], v[228:231], v[12:15], v[144:147]\n\t" \
  "v_mfma_f32_16x16x32_bf16 v[148:151], v[228:231], v[84:87], v[148:151]\n\t" \
  "s_mul_i32 s51, s68, 8\n\t" \
  "s_cmp_lg_u32 s60, 0\n\t" \
  "s_cbranch_scc1 L_p8_8%=\n\t" \
  "s_add_u32 m0, s67, 73728\n\t" \
  "s_nop 0\n\t" \
  "buffer_load_dwordx4 %[wvo8], s[36:39], s51 offen lds\n\t" \
  "L_p8_8%=:\n\t" \
  "s_add_u32 m0, s66, 73728\n\t" \
  "s_nop 0\n\t" \
  "buffer_load_dwordx4 %[wvo0], s[36:39], s51 offen lds\n\t" \
  "ds_read_b64_tr_b16 v[228:229], v240 offset:36864\n\t" \
  "ds_read_b64_tr_b16 v[230:231], v240 offset:41472\n\t" \
  "s_waitcnt lgkmcnt(10)\n\t" \
  "v_mfma_f32_16x16x32_bf16 v[152:155], v[232:235], v[12:15], v[152:155]\n\t" \
  "v_mfma_f32_16x16x32_bf16 v[156:159], v[232:235], v[84:87], v[156:159]\n\t" \
  "ds_read_b64_tr_b16 v[232:233], v240 offset:36896\n\t" \
  "ds_read_b64_tr_b16 v[234:235], v240 offset:41504\n\t" \
  "s_waitcnt lgkmcnt(10)\n\t" \
  "v_mfma_f32_16x16x32_bf16 v[160:163], v[236:239], v[12:15], v[160:163]\n\t" \
  "v_mfma_f32_16x16x32_bf16 v[164:167], v[236:239], v[84:87], v[164:167]\n\t" \
  "s_mov_b32 s52, 4096\n\t" \
  "buffer_load_dwordx4 v[28:31], %[avo], s[40:43], s52 offen offset:3072\n\t" \
  "ds_read_b64_tr_b16 v[236:237], v240 offset:36928\n\t" \
  "ds_read_b64_tr_b16 v[238:239], v240 offset:41536\n\t" \
  "s_waitcnt lgkmcnt(10)\n\t" \
  "v_mfma_f32_16x16x32_bf16 v[168:171], v[216:219], v[12:15], v[168:171]\n\t" \
  "v_mfma_f32_16x16x32_bf16 v[172:175], v[216:219], v[84:87], v[172:175]\n\t" \
  "ds_read_b64_tr_b16 v[216:217], v240 offset:36960\n\t" \
  "ds_read_b64_tr_b16 v[218:219], v240 offset:41568\n\t" \
  "s_waitcnt lgkmcnt(10)\n\t" \
  "v_mfma_f32_16x16x32_bf16 v[176:179], v[220:223], v[12:15], v[176:179]\n\t" \
  "v_mfma_f32_16x16x32_bf16 v[180:183], v[220:223], v[84:87], v[180:183]\n\t" \
  "s_add_u32 s52, s63, 4096\n\t" \
  "buffer_load_dwordx4 v[100:103], %[avo], s[40:43], s52 offen offset:3072\n\t" \
  "ds_read_b64_tr_b16 v[220:221], v240 offset:36992\n\t" \
  "ds_read_b64_tr_b16 v[222:223], v240 offset:41600\n\t" \
  "s_waitcnt lgkmcnt(10)\n\t" \
  "v_mfma_f32_16x16x32_bf16 v[184:187], v[224:227], v[12:15], v[184:187]\n\t" \
  "v_mfma_f32_16x16x32_bf16 v[188:191], v[224:227], v[84:87], v[188:191]\n\t" \
  "ds_read_b64_tr_b16 v[224:225], v240 offset:37024\n\t" \
  "ds_read_b64_tr_b16 v[226:227], v240 offset:41632\n\t" \
  "s_waitcnt vmcnt(9)\n\t" \
  "s_waitcnt lgkmcnt(10)\n\t" \
  "v_mfma_f32_16x16x32_bf16 v[144:147], v[228:231], v[16:19], v[144:147]\n\t" \
  "v_mfma_f32_16x16x32_bf16 v[148:151], v[228:231], v[88:91], v[148:151]\n\t" \
  "s_mul_i32 s51, s68, 9\n\t" \
  "s_cmp_lg_u32 s60, 1\n\t" \
  "s_cbranch_scc1 L_p8_9%=\n\t" \
  "s_add_u32 m0, s67, 82944\n\t" \
  "s_nop 0\n\t" \
  "buffer_load_dwordx4 %[wvo8], s[36:39], s51 offen lds\n\t" \
  "L_p8_9%=:\n\t" \
  "s_add_u32 m0, s66, 82944\n\t" \
  "s_nop 0\n\t" \
  "buffer_load_dwordx4 %[wvo0], s[36:39], s51 offen lds\n\t" \
  "ds_read_b64_tr_b16 v[228:229], v240 offset:46080\n\t" \
  "ds_read_b64_tr_b16 v[230:231], v240 offset:50688\n\t" \
  "s_waitcnt lgkmcnt(10)\n\t" \
  "v_mfma_f32_16x16x32_bf16 v[152:155], v[232:235], v[16:19], v[152:155]\n\t" \
  "v_mfma_f32_16x16x32_bf16 v[156:159], v[232:235], v[88:91], v[156:159]\n\t" \
  "ds_read_b64_tr_b16 v[232:233], v240 offset:46112\n\t" \
  "ds_read_b64_tr_b16 v[234:235], v240 offset:50720\n\t" \
  "s_waitcnt lgkmcnt(10)\n\t" \
  "v_mfma_f32_16x16x32_bf16 v[160:163], v[236:239], v[16:19], v[160:163]\n\t" \
  "v_mfma_f32_16x16x32_bf16 v[164:167], v[236:239], v[88:91], v[164:167]\n\t" \
  "s_mov_b32 s52, 8192\n\t" \
  "buffer_load_dwordx4 v[32:35], %[avo], s[40:43], s52 offen offset:0\n\t" \
  "ds_read_b64_tr_b16 v[236:237], v240 offset:46144\n\t" \
  "ds_read_b64_tr_b16 v[238:239], v240 offset:50752\n\t" \
  "s_waitcnt lgkmcnt(10)\n\t" \
  "v_mfma_f32_16x16x32_bf16 v[168:171], v[216:219], v[16:19], v[168:171]\n\t" \
  "v_mfma_f32_16x16x32_bf16 v[172:175], v[216:219], v[88:91], v[172:175]\n\t" \
  "ds_read_b64_tr_b16 v[216:217], v240 offset:46176\n\t" \
  "ds_read_b64_tr_b16 v[218:219], v240 offset:50784\n\t" \
  "s_waitcnt lgkmcnt(10)\n\t" \
  "v_mfma_f32_16x16x32_bf16 v[176:179], v[220:223], v[16:19], v[176:179]\n\t" \
  "v_mfma_f32_16x16x32_bf16 v[180:183], v[220:223], v[88:91], v[180:183]\n\t" \
  "s_add_u32 s52, s63, 8192\n\t" \
  "buffer_load_dwordx4 v[104:107], %[avo], s[40:43], s52 offen offset:0\n\t" \
  "ds_read_b64_tr_b16 v[220:221], v240 offset:46208\n\t" \
  "ds_read_b64_tr_b16 v[222:223], v240 offset:50816\n\t" \
  "s_waitcnt lgkmcnt(10)\n\t" \
  "v_mfma_f32_16x16x32_bf16 v[184:187], v[224:227], v[16:19], v[184:187]\n\t" \
  "v_mfma_f32_16x16x32_bf16 v[188:191], v[224:227], v[88:91], v[188:191]\n\t" \
  "ds_read_b64_tr_b16 v[224:225], v240 offset:46240\n\t" \
  "ds_read_b64_tr_b16 v[226:227], v240 offset:50848\n\t" \
  "s_waitcnt vmcnt(9)\n\t" \
  "s_waitcnt lgkmcnt(10)\n\t" \
  "v_mfma_f32_16x16x32_bf16 v[144:147], v[228:231], v[20:23], v[144:147]\n\t" \
  "v_mfma_f32_16x16x32_bf16 v[148:151], v[228:231], v[92:95], v[148:151]\n\t" \
  "s_mul_i32 s51, s68, 10\n\t" \
  "s_cmp_lg_u32 s60, 2\n\t" \
  "s_cbranch_scc1 L_p8_10%=\n\t" \
  "s_add_u32 m0, s67, 92160\n\t" \
  "s_nop 0\n\t" \
  "buffer_load_dwordx4 %[wvo8], s[36:39], s51 offen lds\n\t" \
  "L_p8_10%=:\n\t" \
  "s_add_u32 m0, s66, 92160\n\t" \
  "s_nop 0\n\t" \
  "buffer_load_dwordx4 %[wvo0], s[36:39], s51 offen lds\n\t" \
  "s_waitcnt vmcnt(6)\n\t" \
  "s_barrier\n\t" \
  "ds_read_b64_tr_b16 v[228:229], v240 offset:55296\n\t" \
  "ds_read_b64_tr_b16 v[230:231], v240 offset:59904\n\t" \
  "s_waitcnt lgkmcnt(10)\n\t" \
  "v_mfma_f32_16x16x32_bf16 v[152:155], v[232:235], v[20:23], v[152:155]\n\t" \
  "v_mfma_f32_16x16x32_bf16 v[156:159], v[232:235], v[92:95], v[156:159]\n\t" \
  "ds_read_b64_tr_b16 v[232:233], v240 offset:55328\n\t" \
  "ds_read_b64_tr_b16 v[234:235], v240 offset:59936\n\t" \
  "s_waitcnt lgkmcnt(10)\n\t" \
  "v_mfma_f32_16x16x32_bf16 v[160:163], v[236:239], v[20:23], v[160:163]\n\t" \
  "v_mfma_f32_16x16x32_bf16 v[164:167], v[236:239], v[92:95], v[164:167]\n\t" \
  "s_mov_b32 s52, 8192\n\t" \
  "buffer_load_dwordx4 v[36:39], %[avo], s[40:43], s52 offen offset:1024\n\t" \
  "ds_read_b64_tr_b16 v[236:237], v240 offset:55360\n\t" \
  "ds_read_b64_tr_b16 v[238:239], v240 offset:59968\n\t" \
  "s_waitcnt lgkmcnt(10)\n\t" \
  "v_mfma_f32_16x16x32_bf16 v[168:171], v[216:219], v[20:23], v[168:171]\n\t" \
  "v_mfma_f32_16x16x32_bf16 v[172:175], v[216:219], v[92:95], v[172:175]\n\t" \
  "ds_read_b64_tr_b16 v[216:217], v240 offset:55392\n\t" \
  "ds_read_b64_tr_b16 v[218:219], v240 offset:60000\n\t" \
  "s_waitcnt lgkmcnt(10)\n\t" \
  "v_mfma_f32_16x16x32_bf16 v[176:179], v[220:223], v[20:23], v[176:179]\n\t" \
  "v_mfma_f32_16x16x32_bf16 v[180:183], v[220:223], v[92:95], v[180:183]\n\t" \
  "s_add_u32 s52, s63, 8192\n\t" \
  "buffer_load_dwordx4 v[108:111], %[avo], s[40:43], s52 offen offset:1024\n\t" \
  "ds_read_b64_tr_b16 v[220:221], v240 offset:55424\n\t" \
  "ds_read_b64_tr_b16 v[222:223], v240 offset:60032\n\t" \
  "s_waitcnt lgkmcnt(10)\n\t" \
  "v_mfma_f32_16x16x32_bf16 v[184:187], v[224:227], v[20:23], v[184:187]\n\t" \
  "v_mfma_f32_16x16x32_bf16 v[188:191], v[224:227], v[92:95], v[188:191]\n\t" \
  "ds_read_b64_tr_b16 v[224:225], v240 offset:55456\n\t" \
  "ds_read_b64_tr_b16 v[226:227], v240 offset:60064\n\t" \
  "s_waitcnt lgkmcnt(10)\n\t" \
  "v_mfma_f32_16x16x32_bf16 v[144:147], v[228:231], v[24:27], v[144:147]\n\t" \
  "v_mfma_f32_16x16x32_bf16 v[148:151], v[228:231], v[96:99], v[148:151]\n\t" \
  "s_mul_i32 s51, s68, 11\n\t" \
  "s_cmp_lg_u32 s60, 3\n\t" \
  "s_cbranch_scc1 L_p8_11%=\n\t" \
  "s_add_u32 m0, s67, 101376\n\t" \
  "s_nop 0\n\t" \
  "buffer_load_dwordx4 %[wvo8], s[36:39], s51 offen lds\n\t" \
  "L_p8_11%=:\n\t" \
  "s_add_u32 m0, s66, 101376\n\t" \
  "s_nop 0\n\t" \
  "buffer_load_dwordx4 %[wvo0], s[36:39], s51 offen lds\n\t" \
  "v_add_u32_e32 v240, 64512, v240\n\t" \
  "ds_read_b64_tr_b16 v[228:229], v240 offset:0\n\t" \
  "ds_read_b64_tr_b16 v[230:231], v240 offset:4608\n\t" \
  "s_waitcnt lgkmcnt(10)\n\t" \
  "v_mfma_f32_16x16x32_bf16 v[152:155], v[232:235], v[24:27], v[152:155]\n\t" \
  "v_mfma_f32_16x16x32_bf16 v[156:159], v[232:235], v[96:99], v[156:159]\n\t" \
  "ds_read_b64_tr_b16 v[232:233], v240 offset:32\n\t" \
  "ds_read_b64_tr_b16 v[234:235], v240 offset:4640\n\t" \
  "s_waitcnt lgkmcnt(10)\n\t" \
  "v_mfma_f32_16x16x32_bf16 v[160:163], v[236:239], v[24:27], v[160:163]\n\t" \
  "v_mfma_f32_16x16x32_bf16 v[164:167], v[236:239], v[96:99], v[164:167]\n\t" \
  "s_mov_b32 s52, 8192\n\t" \
  "buffer_load_dwordx4 v[40:43], %[avo], s[40:43], s52 offen offset:2048\n\t" \
  "ds_read_b64_tr_b16 v[236:237], v240 offset:64\n\t" \
  "ds_read_b64_tr_b16 v[238:239], v240 offset:4672\n\t" \
  "s_waitcnt lgkmcnt(10)\n\t" \
  "v_mfma_f32_16x16x32_bf16 v[168:171], v[216:219], v[24:27], v[168:171]\n\t" \
  "v_mfma_f32_16x16x32_bf16 v[172:175], v[216:219], v[96:99], v[172:175]\n\t" \
  "ds_read_b64_tr_b16 v[216:217], v240 offset:96\n\t" \
  "ds_read_b64_tr_b16 v[218:219], v240 offset:4704\n\t" \
  "s_waitcnt lgkmcnt(10)\n\t" \
  "v_mfma_f32_16x16x32_bf16 v[176:179], v[220:223], v[24:27], v[176:179]\n\t" \
  "v_mfma_f32_16x16x32_bf16 v[180:183], v[220:223], v[96:99], v[180:183]\n\t" \
  "s_add_u32 s52, s63, 8192\n\t" \
  "buffer_load_dwordx4 v[112:115], %[avo], s[40:43], s52 offen offset:2048\n\t" \
  "ds_read_b64_tr_b16 v[220:221], v240 offset:128\n\t" \
  "ds_read_b64_tr_b16 v[222:223], v240 offset:4736\n\t" \
  "s_waitcnt lgkmcnt(10)\n\t" \
  "v_mfma_f32_16x16x32_bf16 v[184:187], v[224:227], v[24:27], v[184:187]\n\t" \
  "v_mfma_f32_16x16x32_bf16 v[188:191], v[224:227], v[96:99], v[188:191]\n\t" \
  "ds_read_b64_tr_b16 v[224:225], v240 offset:160\n\t" \
  "ds_read_b64_tr_b16 v[226:227], v240 offset:4768\n\t" \
  "s_waitcnt vmcnt(9)\n\t" \
  "s_waitcnt lgkmcnt(10)\n\t" \
  "v_mfma_f32_16x16x32_bf16 v[144:147], v[228:231], v[28:31], v[144:147]\n\t" \
  "v_mfma_f32_16x16x32_bf16 v[148:151], v[228:231], v[100:103], v[148:151]\n\t" \
  "s_mul_i32 s51, s68, 12\n\t" \
  "s_cmp_lg_u32 s60, 4\n\t" \
  "s_cbranch_scc1 L_p8_12%=\n\t" \
  "s_add_u32 m0, s67, 110592\n\t" \
  "s_nop 0\n\t" \
  "buffer_load_dwordx4 %[wvo8], s[36:39], s51 offen lds\n\t" \
  "L_p8_12%=:\n\t" \
  "s_add_u32 m0, s66, 110592\n\t" \
  "s_nop 0\n\t" \
  "buffer_load_dwordx4 %[wvo0], s[36:39], s51 offen lds\n\t" \
  "ds_read_b64_tr_b16 v[228:229], v240 offset:9216\n\t" \
  "ds_read_b64_tr_b16 v[230:231], v240 offset:13824\n\t" \
  "s_waitcnt lgkmcnt(10)\n\t" \
  "v_mfma_f32_16x16x32_bf16 v[152:155], v[232:235], v[28:31], v[152:155]\n\t" \
  "v_mfma_f32_16x16x32_bf16 v[156:159], v[232:235], v[100:103], v[156:159]\n\t" \
  "ds_read_b64_tr_b16 v[232:233], v240 offset:9248\n\t" \
  "ds_read_b64_tr_b16 v[234:235], v240 offset:13856\n\t" \
  "s_waitcnt lgkmcnt(10)\n\t" \
  "v_mfma_f32_16x16x32_bf16 v[160:163], v[236:239], v[28:31], v[160:163]\n\t" \
  "v_mfma_f32_16x16x32_bf16 v[164:167], v[236:239], v[100:103], v[164:167]\n\t" \
  "s_mov_b32 s52, 8192\n\t" \
  "buffer_load_dwordx4 v[44:47], %[avo], s[40:43], s52 offen offset:3072\n\t" \
  "ds_read_b64_tr_b16 v[236:237], v240 offset:9280\n\t" \
  "ds_read_b64_tr_b16 v[238:239], v240 offset:13888\n\t" \
  "s_waitcnt lgkmcnt(10)\n\t" \
  "v_mfma_f32_16x16x32_bf16 v[168:171], v[216:219], v[28:31], v[168:171]\n\t" \
  "v_mfma_f32_16x16x32_bf16 v[172:175], v[216:219], v[100:103], v[172:175]\n\t" \
  "ds_read_b64_tr_b16 v[216:217], v240 offset:9312\n\t" \
  "ds_read_b64_tr_b16 v[218:219], v240 offset:13920\n\t" \
  "s_waitcnt lgkmcnt(10)\n\t" \
  "v_mfma_f32_16x16x32_bf16 v[176:179], v[220:223], v[28:31], v[176:179]\n\t" \
  "v_mfma_f32_16x16x32_bf16 v[180:183], v[220:223], v[100:103], v[180:183]\n\t" \
  "s_add_u32 s52, s63, 8192\n\t" \
  "buffer_load_dwordx4 v[116:119], %[avo], s[40:43], s52 offen offset:3072\n\t" \
  "ds_read_b64_tr_b16 v[220:221], v240 offset:9344\n\t" \
  "ds_read_b64_tr_b16 v[222:223], v240 offset:13952\n\t" \
  "s_waitcnt lgkmcnt(10)\n\t" \
  "v_mfma_f32_16x16x32_bf16 v[184:187], v[224:227], v[28:31], v[184:187]\n\t" \
  "v_mfma_f32_16x16x32_bf16 v[188:191], v[224:227], v[100:103], v[188:191]\n\t" \
  "ds_read_b64_tr_b16 v[224:225], v240 offset:9376\n\t" \
  "ds_read_b64_tr_b16 v[226:227], v240 offset:13984\n\t" \
  "s_waitcnt vmcnt(9)\n\t" \
  "s_waitcnt lgkmcnt(10)\n\t" \
  "v_mfma_f32_16x16x32_bf16 v[144:147], v[228:231], v[32:35], v[144:147]\n\t" \
  "v_mfma_f32_16x16x32_bf16 v[148:151], v[228:231], v[104:107], v[148:151]\n\t" \
  "s_mul_i32 s51, s68, 13\n\t" \
  "s_cmp_lg_u32 s60, 5\n\t" \
  "s_cbranch_scc1 L_p8_13%=\n\t" \
  "s_add_u32 m0, s67, 119808\n\t" \
  "s_nop 0\n\t" \
  "buffer_load_dwordx4 %[wvo8], s[36:39], s51 offen lds\n\t" \
  "L_p8_13%=:\n\t" \
  "s_add_u32 m0, s66, 119808\n\t" \
  "s_nop 0\n\t" \
  "buffer_load_dwordx4 %[wvo0], s[36:39], s51 offen lds\n\t" \
  "s_waitcnt vmcnt(6)\n\t" \
  "s_barrier\n\t" \
  "ds_read_b64_tr_b16 v[228:229], v240 offset:18432\n\t" \
  "ds_read_b64_tr_b16 v[230:231], v240 offset:23040\n\t" \
  "s_waitcnt lgkmcnt(10)\n\t" \
  "v_mfma_f32_16x16x32_bf16 v[152:155], v[232:235], v[32:35], v[152:155]\n\t" \
  "v_mfma_f32_16x16x32_bf16 v[156:159], v[232:235], v[104:107], v[156:159]\n\t" \
  "ds_read_b64_tr_b16 v[232:233], v240 offset:18464\n\t" \
  "ds_read_b64_tr_b16 v[234:235], v240 offset:23072\n\t" \
  "s_waitcnt lgkmcnt(10)\n\t" \
  "v_mfma_f32_16x16x32_bf16 v[160:163], v[236:239], v[32:35], v[160:163]\n\t" \
  "v_mfma_f32_16x16x32_bf16 v[164:167], v[236:239], v[104:107], v[164:167]\n\t" \
  "s_mov_b32 s52, 12288\n\t" \
  "buffer_load_dwordx4 v[48:51], %[avo], s[40:43], s52 offen offset:0\n\t" \
  "ds_read_b64_tr_b16 v[236:237], v240 offset:18496\n\t" \
  "ds_read_b64_tr_b16 v[238:239], v240 offset:23104\n\t" \
  "s_waitcnt lgkmcnt(10)\n\t" \
  "v_mfma_f32_16x16x32_bf16 v[168:171], v[216:219], v[32:35], v[168:171]\n\t" \
  "v_mfma_f32_16x16x32_bf16 v[172:175], v[216:219], v[104:107], v[172:175]\n\t" \
  "ds_read_b64_tr_b16 v[216:217], v240 offset:18528\n\t" \
  "ds_read_b64_tr_b16 v[218:219], v240 offset:23136\n\t" \
  "s_waitcnt lgkmcnt(10)\n\t" \
  "v_mfma_f32_16x16x32_bf16 v[176:179], v[220:223], v[32:35], v[176:179]\n\t" \
  "v_mfma_f32_16x16x32_bf16 v[180:183], v[220:223], v[104:107], v[180:183]\n\t" \
  "s_add_u32 s52, s63, 12288\n\t" \
  "buffer_load_dwordx4 v[120:123], %[avo], s[40:43], s52 offen offset:0\n\t" \
  "ds_read_b64_tr_b16 v[220:221], v240 offset:18560\n\t" \
  "ds_read_b64_tr_b16 v[222:223], v240 offset:23168\n\t" \
  "s_waitcnt lgkmcnt(10)\n\t" \
  "v_mfma_f32_16x16x32_bf16 v[184:187], v[224:227], v[32:35], v[184:187]\n\t" \
  "v_mfma_f32_16x16x32_bf16 v[188:191], v[224:227], v[104:107], v[188:191]\n\t" \
  "ds_read_b64_tr_b16 v[224:225], v240 offset:18592\n\t" \
  "ds_read_b64_tr_b16 v[226:227], v240 offset:23200\n\t" \
  "s_waitcnt lgkmcnt(10)\n\t" \
  "v_mfma_f32_16x16x32_bf16 v[144:147], v[228:231], v[36:39], v[144:147]\n\t" \
  "v_mfma_f32_16x16x32_bf16 v[148:151], v[228:231], v[108:111], v[148:151]\n\t" \
  "s_mul_i32 s51, s68, 14\n\t" \
  "s_cmp_lg_u32 s60, 6\n\t" \
  "s_cbranch_scc1 L_p8_14%=\n\t" \
  "s_add_u32 m0, s67, 129024\n\t" \
  "s_nop 0\n\t" \
  "buffer_load_dwordx4 %[wvo8], s[36:39], s51 offen lds\n\t" \
  "L_p8_14%=:\n\t" \
  "s_add_u32 m0, s66, 129024\n\t" \
  "s_nop 0\n\t" \
  "buffer_load_dwordx4 %[wvo0], s[36:39], s51 offen lds\n\t" \
  "ds_read_b64_tr_b16 v[228:229], v240 offset:27648\n\t" \
  "ds_read_b64_tr_b16 v[230:231], v240 offset:32256\n\t" \
  "s_waitcnt lgkmcnt(10)\n\t" \
  "v_mfma_f32_16x16x32_bf16 v[152:155], v[232:235], v[36:39], v[152:155]\n\t" \
  "v_mfma_f32_16x16x32_bf16 v[156:159], v[232:235], v[108:111], v[156:159]\n\t" \
  "ds_read_b64_tr_b16 v[232:233], v240 offset:27680\n\t" \
  "ds_read_b64_tr_b16 v[234:235], v240 offset:32288\n\t" \
  "s_waitcnt lgkmcnt(10)\n\t" \
  "v_mfma_f32_16x16x32_bf16 v[160:163], v[236:239], v[36:39], v[160:163]\n\t" \
  "v_mfma_f32_16x16x32_bf16 v[164:167], v[236:239], v[108:111], v[164:167]\n\t" \
  "s_mov_b32 s52, 12288\n\t" \
  "buffer_load_dwordx4 v[52:55], %[avo], s[40:43], s52 offen offset:1024\n\t" \
  "ds_read_b64_tr_b16 v[236:237], v240 offset:27712\n\t" \
  "ds_read_b64_tr_b16 v[238:239], v240 offset:32320\n\t" \
  "s_waitcnt lgkmcnt(10)\n\t" \
  "v_mfma_f32_16x16x32_bf16 v[168:171], v[216:219], v[36:39], v[168:171]\n\t" \
  "v_mfma_f32_16x16x32_bf16 v[172:175], v[216:219], v[108:111], v[172:175]\n\t" \
  "ds_read_b64_tr_b16 v[216:217], v240 offset:27744\n\t" \
  "ds_read_b64_tr_b16 v[218:219], v240 offset:32352\n\t" \
  "s_waitcnt lgkmcnt(10)\n\t" \
  "v_mfma_f32_16x16x32_bf16 v[176:179], v[220:223], v[36:39], v[176:179]\n\t" \
  "v_mfma_f32_16x16x32_bf16 v[180:183], v[220:223], v[108:111], v[180:183]\n\t" \
  "s_add_u32 s52, s63, 12288\n\t" \
  "buffer_load_dwordx4 v[124:127], %[avo], s[40:43], s52 offen offset:1024\n\t" \
  "ds_read_b64_tr_b16 v[220:221], v240 offset:27776\n\t" \
  "ds_read_b64_tr_b16 v[222:223], v240 offset:32384\n\t" \
  "s_waitcnt lgkmcnt(10)\n\t" \
  "v_mfma_f32_16x16x32_bf16 v[184:187], v[224:227], v[36:39], v[184:187]\n\t" \
  "v_mfma_f32_16x16x32_bf16 v[188:191], v[224:227], v[108:111], v[188:191]\n\t" \
  "ds_read_b64_tr_b16 v[224:225], v240 offset:27808\n\t" \
  "ds_read_b64_tr_b16 v[226:227], v240 offset:32416\n\t" \
  "s_waitcnt vmcnt(9)\n\t" \
  "s_waitcnt lgkmcnt(10)\n\t" \
  "v_mfma_f32_16x16x32_bf16 v[144:147], v[228:231], v[40:43], v[144:147]\n\t" \
  "v_mfma_f32_16x16x32_bf16 v[148:151], v[228:231], v[112:115], v[148:151]\n\t" \
  "s_mul_i32 s51, s68, 15\n\t" \
  "s_cmp_lg_u32 s60, 7\n\t" \
  "s_cbranch_scc1 L_p8_15%=\n\t" \
  "s_add_u32 m0, s67, 138240\n\t" \
  "s_nop 0\n\t" \
  "buffer_load_dwordx4 %[wvo8], s[36:39], s51 offen lds\n\t" \
  "L_p8_15%=:\n\t" \
  "s_add_u32 m0, s66, 138240\n\t" \
  "s_nop 0\n\t" \
  "buffer_load_dwordx4 %[wvo0], s[36:39], s51 offen lds\n\t" \
  "ds_read_b64_tr_b16 v[228:229], v240 offset:36864\n\t" \
  "ds_read_b64_tr_b16 v[230:231], v240 offset:41472\n\t" \
  "s_waitcnt lgkmcnt(10)\n\t" \
  "v_mfma_f32_16x16x32_bf16 v[152:155], v[232:235], v[40:43], v[152:155]\n\t" \
  "v_mfma_f32_16x16x32_bf16 v[156:159], v[232:235], v[112:115], v[156:159]\n\t" \
  "ds_read_b64_tr_b16 v[232:233], v240 offset:36896\n\t" \
  "ds_read_b64_tr_b16 v[234:235], v240 offset:41504\n\t" \
  "s_waitcnt lgkmcnt(10)\n\t" \
  "v_mfma_f32_16x16x32_bf16 v[160:163], v[236:239], v[40:43], v[160:163]\n\t" \
  "v_mfma_f32_16x16x32_bf16 v[164:167], v[236:239], v[112:115], v[164:167]\n\t" \
  "s_mov_b32 s52, 12288\n\t" \
  "buffer_load_dwordx4 v[56:59], %[avo], s[40:43], s52 offen offset:2048\n\t" \
  "ds_read_b64_tr_b16 v[236:237], v240 offset:36928\n\t" \
  "ds_read_b64_tr_b16 v[238:239], v240 offset:41536\n\t" \
  "s_waitcnt lgkmcnt(10)\n\t" \
  "v_mfma_f32_16x16x32_bf16 v[168:171], v[216:219], v[40:43], v[168:171]\n\t" \
  "v_mfma_f32_16x16x32_bf16 v[172:175], v[216:219], v[112:115], v[172:175]\n\t" \
  "ds_read_b64_tr_b16 v[216:217], v240 offset:36960\n\t" \
  "ds_read_b64_tr_b16 v[218:219], v240 offset:41568\n\t" \
  "s_waitcnt lgkmcnt(10)\n\t" \
  "v_mfma_f32_16x16x32_bf16 v[176:179], v[220:223], v[40:43], v[176:179]\n\t" \
  "v_mfma_f32_16x16x32_bf16 v[180:183], v[220:223], v[112:115], v[180:183]\n\t" \
  "s_add_u32 s52, s63, 12288\n\t" \
  "buffer_load_dwordx4 v[128:131], %[avo], s[40:43], s52 offen offset:2048\n\t" \
  "ds_read_b64_tr_b16 v[220:221], v240 offset:36992\n\t" \
  "ds_read_b64_tr_b16 v[222:223], v240 offset:41600\n\t" \
  "s_waitcnt lgkmcnt(10)\n\t" \
  "v_mfma_f32_16x16x32_bf16 v[184:187], v[224:227], v[40:43], v[184:187]\n\t" \
  "v_mfma_f32_16x16x32_bf16 v[188:191], v[224:227], v[112:115], v[188:191]\n\t" \
  "ds_read_b64_tr_b16 v[224:225], v240 offset:37024\n\t" \
  "ds_read_b64_tr_b16 v[226:227], v240 offset:41632\n\t" \
  "s_waitcnt vmcnt(9)\n\t" \
  "s_waitcnt lgkmcnt(10)\n\t" \
  "v_mfma_f32_16x16x32_bf16 v[144:147], v[228:231], v[44:47], v[144:147]\n\t" \
  "v_mfma_f32_16x16x32_bf16 v[148:151], v[228:231], v[116:119], v[148:151]\n\t" \
  "s_mul_i32 s51, s68, 16\n\t" \
  "s_cmp_lg_u32 s60, 0\n\t" \
  "s_cbranch_scc1 L_p8_16%=\n\t" \
  "s_add_u32 m0, s67, 147456\n\t" \
  "s_nop 0\n\t" \
  "buffer_load_dwordx4 %[wvo8], s[36:39], s51 offen lds\n\t" \
  "L_p8_16%=:\n\t" \
  "s_add_u32 m0, s66, 147456\n\t" \
  "s_nop 0\n\t" \
  "buffer_load_dwordx4 %[wvo0], s[36:39], s51 offen lds\n\t" \
  "s_waitcnt vmcnt(6)\n\t" \
  "s_barrier\n\t" \
  "ds_read_b64_tr_b16 v[228:229], v240 offset:46080\n\t" \
  "ds_read_b64_tr_b16 v[230:231], v240 offset:50688\n\t" \
  "s_waitcnt lgkmcnt(10)\n\t" \
  "v_mfma_f32_16x16x32_bf16 v[152:155], v[232:235], v[44:47], v[152:155]\n\t" \
  "v_mfma_f32_16x16x32_bf16 v[156:159], v[232:235], v[116:119], v[156:159]\n\t" \
  "ds_read_b64_tr_b16 v[232:233], v240 offset:46112\n\t" \
  "ds_read_b64_tr_b16 v[234:235], v240 offset:50720\n\t" \
  "s_waitcnt lgkmcnt(10)\n\t" \
  "v_mfma_f32_16x16x32_bf16 v[160:163], v[236:239], v[44:47], v[160:163]\n\t" \
  "v_mfma_f32_16x16x32_bf16 v[164:167], v[236:239], v[116:119], v[164:167]\n\t" \
  "s_mov_b32 s52, 12288\n\t" \
  "buffer_load_dwordx4 v[60:63], %[avo], s[40:43], s52 offen offset:3072\n\t" \
  "ds_read_b64_tr_b16 v[236:237], v240 offset:46144\n\t" \
  "ds_read_b64_tr_b16 v[238:239], v240 offset:50752\n\t" \
  "s_waitcnt lgkmcnt(10)\n\t" \
  "v_mfma_f32_16x16x32_bf16 v[168:171], v[216:219], v[44:47], v[168:171]\n\t" \
  "v_mfma_f32_16x16x32_bf16 v[172:175], v[216:219], v[116:119], v[172:175]\n\t" \
  "ds_read_b64_tr_b16 v[216:217], v240 offset:46176\n\t" \
  "ds_read_b64_tr_b16 v[218:219], v240 offset:50784\n\t" \
  "s_waitcnt lgkmcnt(10)\n\t" \
  "v_mfma_f32_16x16x32_bf16 v[176:179], v[220:223], v[44:47], v[176:179]\n\t" \
  "v_mfma_f32_16x16x32_bf16 v[180:183], v[220:223], v[116:119], v[180:183]\n\t" \
  "s_add_u32 s52, s63, 12288\n\t" \
  "buffer_load_dwordx4 v[132:135], %[avo], s[40:43], s52 offen offset:3072\n\t" \
  "ds_read_b64_tr_b16 v[220:221], v240 offset:46208\n\t" \
  "ds_read_b64_tr_b16 v[222:223], v240 offset:50816\n\t" \
  "s_waitcnt lgkmcnt(10)\n\t" \
  "v_mfma_f32_16x16x32_bf16 v[184:187], v[224:227], v[44:47], v[184:187]\n\t" \
  "v_mfma_f32_16x16x32_bf16 v[188:191], v[224:227], v[116:119], v[188:191]\n\t" \
  "ds_read_b64_tr_b16 v[224:225], v240 offset:46240\n\t" \
  "ds_read_b64_tr_b16 v[226:227], v240 offset:50848\n\t" \
  "s_waitcnt lgkmcnt(10)\n\t" \
  "v_mfma_f32_16x16x32_bf16 v[144:147], v[228:231], v[48:51], v[144:147]\n\t" \
  "v_mfma_f32_16x16x32_bf16 v[148:151], v[228:231], v[120:123], v[148:151]\n\t" \
  "s_mul_i32 s51, s68, 17\n\t" \
  "s_cmp_lg_u32 s60, 1\n\t" \
  "s_cbranch_scc1 L_p8_17%=\n\t" \
  "s_add_u32 m0, s67, 0\n\t" \
  "s_nop 0\n\t" \
  "buffer_load_dwordx4 %[wvo8], s[36:39], s51 offen lds\n\t" \
  "L_p8_17%=:\n\t" \
  "s_add_u32 m0, s66, 0\n\t" \
  "s_nop 0\n\t" \
  "buffer_load_dwordx4 %[wvo0], s[36:39], s51 offen lds\n\t" \
  "ds_read_b64_tr_b16 v[228:229], v240 offset:55296\n\t" \
  "ds_read_b64_tr_b16 v[230:231], v240 offset:59904\n\t" \
  "s_waitcnt lgkmcnt(10)\n\t" \
  "v_mfma_f32_16x16x32_bf16 v[152:155], v[232:235], v[48:51], v[152:155]\n\t" \
  "v_mfma_f32_16x16x32_bf16 v[156:159], v[232:235], v[120:123], v[156:159]\n\t" \
  "ds_read_b64_tr_b16 v[232:233], v240 offset:55328\n\t" \
  "ds_read_b64_tr_b16 v[234:235], v240 offset:59936\n\t" \
  "s_waitcnt lgkmcnt(10)\n\t" \
  "v_mfma_f32_16x16x32_bf16 v[160:163], v[236:239], v[48:51], v[160:163]\n\t" \
  "v_mfma_f32_16x16x32_bf16 v[164:167], v[236:239], v[120:123], v[164:167]\n\t" \
  "s_mov_b32 s52, 16384\n\t" \
  "buffer_load_dwordx4 v[64:67], %[avo], s[40:43], s52 offen offset:0\n\t" \
  "ds_read_b64_tr_b16 v[236:237], v240 offset:55360\n\t" \
  "ds_read_b64_tr_b16 v[238:239], v240 offset:59968\n\t" \
  "s_waitcnt lgkmcnt(10)\n\t" \
  "v_mfma_f32_16x16x32_bf16 v[168:171], v[216:219], v[48:51], v[168:171]\n\t" \
  "v_mfma_f32_16x16x32_bf16 v[172:175], v[216:219], v[120:123], v[172:175]\n\t" \
  "ds_read_b64_tr_b16 v[216:217], v240 offset:55392\n\t" \
  "ds_read_b64_tr_b16 v[218:219], v240 offset:60000\n\t" \
  "s_waitcnt lgkmcnt(10)\n\t" \
  "v_mfma_f32_16x16x32_bf16 v[176:179], v[220:223], v[48:51], v[176:179]\n\t" \
  "v_mfma_f32_16x16x32_bf16 v[180:183], v[220:223], v[120:123], v[180:183]\n\t" \
  "s_add_u32 s52, s63, 16384\n\t" \
  "buffer_load_dwordx4 v[136:139], %[avo], s[40:43], s52 offen offset:0\n\t" \
  "ds_read_b64_tr_b16 v[220:221], v240 offset:55424\n\t" \
  "ds_read_b64_tr_b16 v[222:223], v240 offset:60032\n\t" \
  "s_waitcnt lgkmcnt(10)\n\t" \
  "v_mfma_f32_16x16x32_bf16 v[184:187], v[224:227], v[48:51], v[184:187]\n\t" \
  "v_mfma_f32_16x16x32_bf16 v[188:191], v[224:227], v[120:123], v[188:191]\n\t" \
  "ds_read_b64_tr_b16 v[224:225], v240 offset:55456\n\t" \
  "ds_read_b64_tr_b16 v[226:227], v240 offset:60064\n\t" \
  "s_waitcnt vmcnt(9)\n\t" \
  "s_waitcnt lgkmcnt(10)\n\t" \
  "v_mfma_f32_16x16x32_bf16 v[144:147], v[228:231], v[52:55], v[144:147]\n\t" \
  "v_mfma_f32_16x16x32_bf16 v[148:151], v[228:231], v[124:127], v[148:151]\n\t" \
  "s_mov_b32 s52, 16384\n\t" \
  "buffer_load_dwordx4 v[68:71], %[avo], s[40:43], s52 offen offset:1024\n\t" \
  "v_add_u32_e32 v240, 64512, v240\n\t" \
  "ds_read_b64_tr_b16 v[228:229], v240 offset:0\n\t" \
  "ds_read_b64_tr_b16 v[230:231], v240 offset:4608\n\t" \
  "s_waitcnt lgkmcnt(10)\n\t" \
  "v_mfma_f32_16x16x32_bf16 v[152:155], v[232:235], v[52:55], v[152:155]\n\t" \
  "v_mfma_f32_16x16x32_bf16 v[156:159], v[232:235], v[124:127], v[156:159]\n\t" \
  "ds_read_b64_tr_b16 v[232:233], v240 offset:32\n\t" \
  "ds_read_b64_tr_b16 v[234:235], v240 offset:4640\n\t" \
  "s_waitcnt lgkmcnt(10)\n\t" \
  "v_mfma_f32_16x16x32_bf16 v[160:163], v[236:239], v[52:55], v[160:163]\n\t" \
  "v_mfma_f32_16x16x32_bf16 v[164:167], v[236:239], v[124:127], v[164:167]\n\t" \
  "s_add_u32 s52, s63, 16384\n\t" \
  "buffer_load_dwordx4 v[140:143], %[avo], s[40:43], s52 offen offset:1024\n\t" \
  "ds_read_b64_tr_b16 v[236:237], v240 offset:64\n\t" \
  "ds_read_b64_tr_b16 v[238:239], v240 offset:4672\n\t" \
  "s_waitcnt lgkmcnt(10)\n\t" \
  "v_mfma_f32_16x16x32_bf16 v[168:171], v[216:219], v[52:55], v[168:171]\n\t" \
  "v_mfma_f32_16x16x32_bf16 v[172:175], v[216:219], v[124:127], v[172:175]\n\t" \
  "ds_read_b64_tr_b16 v[216:217], v240 offset:96\n\t" \
  "ds_read_b64_tr_b16 v[218:219], v240 offset:4704\n\t" \
  "s_waitcnt lgkmcnt(10)\n\t" \
  "v_mfma_f32_16x16x32_bf16 v[176:179], v[220:223], v[52:55], v[176:179]\n\t" \
  "v_mfma_f32_16x16x32_bf16 v[180:183], v[220:223], v[124:127], v[180:183]\n\t" \
  "ds_read_b64_tr_b16 v[220:221], v240 offset:128\n\t" \
  "ds_read_b64_tr_b16 v[222:223], v240 offset:4736\n\t" \
  "s_waitcnt lgkmcnt(10)\n\t" \
  "v_mfma_f32_16x16x32_bf16 v[184:187], v[224:227], v[52:55], v[184:187]\n\t" \
  "v_mfma_f32_16x16x32_bf16 v[188:191], v[224:227], v[124:127], v[188:191]\n\t" \
  "ds_read_b64_tr_b16 v[224:225], v240 offset:160\n\t" \
  "ds_read_b64_tr_b16 v[226:227], v240 offset:4768\n\t" \
  "s_waitcnt vmcnt(8)\n\t" \
  "s_waitcnt lgkmcnt(10)\n\t" \
  "v_mfma_f32_16x16x32_bf16 v[144:147], v[228:231], v[56:59], v[144:147]\n\t" \
  "v_mfma_f32_16x16x32_bf16 v[148:151], v[228:231], v[128:131], v[148:151]\n\t" \
  "s_waitcnt vmcnt(4)\n\t" \
  "s_barrier\n\t" \
  "ds_read_b64_tr_b16 v[228:229], v240 offset:9216\n\t" \
  "ds_read_b64_tr_b16 v[230:231], v240 offset:13824\n\t" \
  "s_waitcnt lgkmcnt(10)\n\t" \
  "v_mfma_f32_16x16x32_bf16 v[152:155], v[232:235], v[56:59], v[152:155]\n\t" \
  "v_mfma_f32_16x16x32_bf16 v[156:159], v[232:235], v[128:131], v[156:159]\n\t" \
  "ds_read_b64_tr_b16 v[232:233], v240 offset:9248\n\t" \
  "ds_read_b64_tr_b16 v[234:235], v240 offset:13856\n\t" \
  "s_waitcnt lgkmcnt(10)\n\t" \
  "v_mfma_f32_16x16x32_bf16 v[160:163], v[236:239], v[56:59], v[160:163]\n\t" \
  "v_mfma_f32_16x16x32_bf16 v[164:167], v[236:239], v[128:131], v[164:167]\n\t" \
  "ds_read_b64_tr_b16 v[236:237], v240 offset:9280\n\t" \
  "ds_read_b64_tr_b16 v[238:239], v240 offset:13888\n\t" \
  "s_waitcnt lgkmcnt(10)\n\t" \
  "v_mfma_f32_16x16x32_bf16 v[168:171], v[216:219], v[56:59], v[168:171]\n\t" \
  "v_mfma_f32_16x16x32_bf16 v[172:175], v[216:219], v[128:131], v[172:175]\n\t" \
  "ds_read_b64_tr_b16 v[216:217], v240 offset:9312\n\t" \
  "ds_read_b64_tr_b16 v[218:219], v240 offset:13920\n\t" \
  "s_waitcnt lgkmcnt(10)\n\t" \
  "v_mfma_f32_16x16x32_bf16 v[176:179], v[220:223], v[56:59], v[176:179]\n\t" \
  "v_mfma_f32_16x16x32_bf16 v[180:183], v[220:223], v[128:131], v[180:183]\n\t" \
  "ds_read_b64_tr_b16 v[220:221], v240 offset:9344\n\t" \
  "ds_read_b64_tr_b16 v[222:223], v240 offset:13952\n\t" \
  "s_waitcnt lgkmcnt(10)\n\t" \
  "v_mfma_f32_16x16x32_bf16 v[184:187], v[224:227], v[56:59], v[184:187]\n\t" \
  "v_mfma_f32_16x16x32_bf16 v[188:191], v[224:227], v[128:131], v[188:191]\n\t" \
  "ds_read_b64_tr_b16 v[224:225], v240 offset:9376\n\t" \
  "ds_read_b64_tr_b16 v[226:227], v240 offset:13984\n\t" \
  "s_waitcnt lgkmcnt(10)\n\t" \
  "v_mfma_f32_16x16x32_bf16 v[144:147], v[228:231], v[60:63], v[144:147]\n\t" \
  "v_mfma_f32_16x16x32_bf16 v[148:151], v[228:231], v[132:135], v[148:151]\n\t" \
  "ds_read_b64_tr_b16 v[228:229], v240 offset:18432\n\t" \
  "ds_read_b64_tr_b16 v[230:231], v240 offset:23040\n\t" \
  "s_waitcnt lgkmcnt(10)\n\t" \
  "v_mfma_f32_16x16x32_bf16 v[152:155], v[232:235], v[60:63], v[152:155]\n\t" \
  "v_mfma_f32_16x16x32_bf16 v[156:159], v[232:235], v[132:135], v[156:159]\n\t" \
  "ds_read_b64_tr_b16 v[232:233], v240 offset:18464\n\t" \
  "ds_read_b64_tr_b16 v[234:235], v240 offset:23072\n\t" \
  "s_waitcnt lgkmcnt(10)\n\t" \
  "v_mfma_f32_16x16x32_bf16 v[160:163], v[236:239], v[60:63], v[160:163]\n\t" \
  "v_mfma_f32_16x16x32_bf16 v[164:167], v[236:239], v[132:135], v[164:167]\n\t" \
  "ds_read_b64_tr_b16 v[236:237], v240 offset:18496\n\t" \
  "ds_read_b64_tr_b16 v[238:239], v240 offset:23104\n\t" \
  "s_waitcnt lgkmcnt(10)\n\t" \
  "v_mfma_f32_16x16x32_bf16 v[168:171], v[216:219], v[60:63], v[168:171]\n\t" \
  "v_mfma_f32_16x16x32_bf16 v[172:175], v[216:219], v[132:135], v[172:175]\n\t" \
  "ds_read_b64_tr_b16 v[216:217], v240 offset:18528\n\t" \
  "ds_read_b64_tr_b16 v[218:219], v240 offset:23136\n\t" \
  "s_waitcnt lgkmcnt(10)\n\t" \
  "v_mfma_f32_16x16x32_bf16 v[176:179], v[220:223], v[60:63], v[176:179]\n\t" \
  "v_mfma_f32_16x16x32_bf16 v[180:183], v[220:223], v[132:135], v[180:183]\n\t" \
  "ds_read_b64_tr_b16 v[220:221], v240 offset:18560\n\t" \
  "ds_read_b64_tr_b16 v[222:223], v240 offset:23168\n\t" \
  "s_waitcnt lgkmcnt(10)\n\t" \
  "v_mfma_f32_16x16x32_bf16 v[184:187], v[224:227], v[60:63], v[184:187]\n\t" \
  "v_mfma_f32_16x16x32_bf16 v[188:191], v[224:227], v[132:135], v[188:191]\n\t" \
  "ds_read_b64_tr_b16 v[224:225], v240 offset:18592\n\t" \
  "ds_read_b64_tr_b16 v[226:227], v240 offset:23200\n\t" \
  "s_waitcnt vmcnt(2)\n\t" \
  "s_waitcnt lgkmcnt(10)\n\t" \
  "v_mfma_f32_16x16x32_bf16 v[144:147], v[228:231], v[64:67], v[144:147]\n\t" \
  "v_mfma_f32_16x16x32_bf16 v[148:151], v[228:231], v[136:139], v[148:151]\n\t" \
  "v_add_u32_e32 v240, 4294838272, v240\n\t" \
  "ds_read_b64_tr_b16 v[228:229], v240 offset:0\n\t" \
  "ds_read_b64_tr_b16 v[230:231], v240 offset:4608\n\t" \
  "s_waitcnt lgkmcnt(10)\n\t" \
  "v_mfma_f32_16x16x32_bf16 v[152:155], v[232:235], v[64:67], v[152:155]\n\t" \
  "v_mfma_f32_16x16x32_bf16 v[156:159], v[232:235], v[136:139], v[156:159]\n\t" \
  "ds_read_b64_tr_b16 v[232:233], v240 offset:32\n\t" \
  "ds_read_b64_tr_b16 v[234:235], v240 offset:4640\n\t" \
  "s_waitcnt lgkmcnt(10)\n\t" \
  "v_mfma_f32_16x16x32_bf16 v[160:163], v[236:239], v[64:67], v[160:163]\n\t" \
  "v_mfma_f32_16x16x32_bf16 v[164:167], v[236:239], v[136:139], v[164:167]\n\t" \
  "ds_read_b64_tr_b16 v[236:237], v240 offset:64\n\t" \
  "ds_read_b64_tr_b16 v[238:239], v240 offset:4672\n\t" \
  "s_waitcnt lgkmcnt(10)\n\t" \
  "v_mfma_f32_16x16x32_bf16 v[168:171], v[216:219], v[64:67], v[168:171]\n\t" \
  "v_mfma_f32_16x16x32_bf16 v[172:175], v[216:219], v[136:139], v[172:175]\n\t" \
  "ds_read_b64_tr_b16 v[216:217], v240 offset:96\n\t" \
  "ds_read_b64_tr_b16 v[218:219], v240 offset:4704\n\t" \
  "s_waitcnt lgkmcnt(10)\n\t" \
  "v_mfma_f32_16x16x32_bf16 v[176:179], v[220:223], v[64:67], v[176:179]\n\t" \
  "v_mfma_f32_16x16x32_bf16 v[180:183], v[220:223], v[136:139], v[180:183]\n\t" \
  "ds_read_b64_tr_b16 v[220:221], v240 offset:128\n\t" \
  "ds_read_b64_tr_b16 v[222:223], v240 offset:4736\n\t" \
  "s_waitcnt lgkmcnt(10)\n\t" \
  "v_mfma_f32_16x16x32_bf16 v[184:187], v[224:227], v[64:67], v[184:187]\n\t" \
  "v_mfma_f32_16x16x32_bf16 v[188:191], v[224:227], v[136:139], v[188:191]\n\t" \
  "ds_read_b64_tr_b16 v[224:225], v240 offset:160\n\t" \
  "ds_read_b64_tr_b16 v[226:227], v240 offset:4768\n\t" \
  "s_waitcnt vmcnt(0)\n\t" \
  "s_waitcnt lgkmcnt(10)\n\t" \
  "v_mfma_f32_16x16x32_bf16 v[144:147], v[228:231], v[68:71], v[144:147]\n\t" \
  "v_mfma_f32_16x16x32_bf16 v[148:151], v[228:231], v[140:143], v[148:151]\n\t" \
  "ds_read_b64_tr_b16 v[228:229], v240 offset:9408\n\t" \
  "ds_read_b64_tr_b16 v[230:231], v240 offset:14016\n\t" \
  "s_waitcnt lgkmcnt(10)\n\t" \
  "v_mfma_f32_16x16x32_bf16 v[152:155], v[232:235], v[68:71], v[152:155]\n\t" \
  "v_mfma_f32_16x16x32_bf16 v[156:159], v[232:235], v[140:143], v[156:159]\n\t" \
  "ds_read_b64_tr_b16 v[232:233], v240 offset:9440\n\t" \
  "ds_read_b64_tr_b16 v[234:235], v240 offset:14048\n\t" \
  "s_waitcnt lgkmcnt(10)\n\t" \
  "v_mfma_f32_16x16x32_bf16 v[160:163], v[236:239], v[68:71], v[160:163]\n\t" \
  "v_mfma_f32_16x16x32_bf16 v[164:167], v[236:239], v[140:143], v[164:167]\n\t" \
  "ds_read_b64_tr_b16 v[236:237], v241 offset:9216\n\t" \
  "ds_read_b64_tr_b16 v[238:239], v241 offset:13824\n\t" \
  "s_waitcnt lgkmcnt(10)\n\t" \
  "v_mfma_f32_16x16x32_bf16 v[168:171], v[216:219], v[68:71], v[168:171]\n\t" \
  "v_mfma_f32_16x16x32_bf16 v[172:175], v[216:219], v[140:143], v[172:175]\n\t" \
  "ds_read_b64_tr_b16 v[216:217], v240 offset:18624\n\t" \
  "ds_read_b64_tr_b16 v[218:219], v240 offset:23232\n\t" \
  "s_waitcnt lgkmcnt(10)\n\t" \
  "v_mfma_f32_16x16x32_bf16 v[176:179], v[220:223], v[68:71], v[176:179]\n\t" \
  "v_mfma_f32_16x16x32_bf16 v[180:183], v[220:223], v[140:143], v[180:183]\n\t" \
  "ds_read_b64_tr_b16 v[220:221], v240 offset:18656\n\t" \
  "ds_read_b64_tr_b16 v[222:223], v240 offset:23264\n\t" \
  "s_waitcnt lgkmcnt(10)\n\t" \
  "v_mfma_f32_16x16x32_bf16 v[184:187], v[224:227], v[68:71], v[184:187]\n\t" \
  "v_mfma_f32_16x16x32_bf16 v[188:191], v[224:227], v[140:143], v[188:191]\n\t" \
  "ds_read_b64_tr_b16 v[224:225], v241 offset:18432\n\t" \
  "ds_read_b64_tr_b16 v[226:227], v241 offset:23040\n\t" \
  "s_waitcnt lgkmcnt(10)\n\t" \
  "v_mfma_f32_16x16x32_bf16 v[192:195], v[228:231], v[4:7], v[192:195]\n\t" \
  "v_mfma_f32_16x16x32_bf16 v[196:199], v[228:231], v[76:79], v[196:199]\n\t" \
  "ds_read_b64_tr_b16 v[228:229], v240 offset:27840\n\t" \
  "ds_read_b64_tr_b16 v[230:231], v240 offset:32448\n\t" \
  "s_waitcnt lgkmcnt(10)\n\t" \
  "v_mfma_f32_16x16x32_bf16 v[200:203], v[232:235], v[4:7], v[200:203]\n\t" \
  "v_mfma_f32_16x16x32_bf16 v[204:207], v[232:235], v[76:79], v[204:207]\n\t" \
  "ds_read_b64_tr_b16 v[232:233], v240 offset:27872\n\t" \
  "ds_read_b64_tr_b16 v[234:235], v240 offset:32480\n\t" \
  "s_waitcnt lgkmcnt(10)\n\t" \
  "v_mfma_f32_16x16x32_bf16 v[208:211], v[236:239], v[4:7], v[208:211]\n\t" \
  "v_mfma_f32_16x16x32_bf16 v[212:215], v[236:239], v[76:79], v[212:215]\n\t" \
  "ds_read_b64_tr_b16 v[236:237], v241 offset:27648\n\t" \
  "ds_read_b64_tr_b16 v[238:239], v241 offset:32256\n\t" \
  "s_waitcnt lgkmcnt(10)\n\t" \
  "v_mfma_f32_16x16x32_bf16 v[192:195], v[216:219], v[8:11], v[192:195]\n\t" \
  "v_mfma_f32_16x16x32_bf16 v[196:199], v[216:219], v[80:83], v[196:199]\n\t" \
  "ds_read_b64_tr_b16 v[216:217], v240 offset:37056\n\t" \
  "ds_read_b64_tr_b16 v[218:219], v240 offset:41664\n\t" \
  "s_waitcnt lgkmcnt(10)\n\t" \
  "v_mfma_f32_16x16x32_bf16 v[200:203], v[220:223], v[8:11], v[200:203]\n\t" \
  "v_mfma_f32_16x16x32_bf16 v[204:207], v[220:223], v[80:83], v[204:207]\n\t" \
  "ds_read_b64_tr_b16 v[220:221], v240 offset:37088\n\t" \
  "ds_read_b64_tr_b16 v[222:223], v240 offset:41696\n\t" \
  "s_waitcnt lgkmcnt(10)\n\t" \
  "v_mfma_f32_16x16x32_bf16 v[208:211], v[224:227], v[8:11], v[208:211]\n\t" \
  "v_mfma_f32_16x16x32_bf16 v[212:215], v[224:227], v[80:83], v[212:215]\n\t" \
  "ds_read_b64_tr_b16 v[224:225], v241 offset:36864\n\t" \
  "ds_read_b64_tr_b16 v[226:227], v241 offset:41472\n\t" \
  "s_waitcnt lgkmcnt(10)\n\t" \
  "v_mfma_f32_16x16x32_bf16 v[192:195], v[228:231], v[12:15], v[192:195]\n\t" \
  "v_mfma_f32_16x16x32_bf16 v[196:199], v[228:231], v[84:87], v[196:199]\n\t" \
  "ds_read_b64_tr_b16 v[228:229], v240 offset:46272\n\t" \
  "ds_read_b64_tr_b16 v[230:231], v240 offset:50880\n\t" \
  "s_waitcnt lgkmcnt(10)\n\t" \
  "v_mfma_f32_16x16x32_bf16 v[200:203], v[232:235], v[12:15], v[200:203]\n\t" \
  "v_mfma_f32_16x16x32_bf16 v[204:207], v[232:235], v[84:87], v[204:207]\n\t" \
  "ds_read_b64_tr_b16 v[232:233], v240 offset:46304\n\t" \
  "ds_read_b64_tr_b16 v[234:235], v240 offset:50912\n\t" \
  "v_cvt_pk_bf16_f32 v144, v144, v145\n\t" \
  "v_cvt_pk_bf16_f32 v145, v146, v147\n\t" \
  "v_pk_max_i16 v144, v144, 0\n\t" \
  "v_pk_max_i16 v145, v145, 0\n\t" \
  "v_cvt_pk_bf16_f32 v146, v152, v153\n\t" \
  "v_cvt_pk_bf16_f32 v147, v154, v155\n\t" \
  "v_pk_max_i16 v146, v146, 0\n\t" \
  "v_pk_max_i16 v147, v147, 0\n\t" \
  "buffer_store_dwordx4 v[144:147], %[avo], s[44:47], s69 offen offset:0 sc1\n\t" \
  "s_waitcnt lgkmcnt(10)\n\t" \
  "v_mfma_f32_16x16x32_bf16 v[208:211], v[236:239], v[12:15], v[208:211]\n\t" \
  "v_mfma_f32_16x16x32_bf16 v[212:215], v[236:239], v[84:87], v[212:215]\n\t" \
  "ds_read_b64_tr_b16 v[236:237], v241 offset:46080\n\t" \
  "ds_read_b64_tr_b16 v[238:239], v241 offset:50688\n\t" \
  "s_waitcnt lgkmcnt(10)\n\t" \
  "v_mfma_f32_16x16x32_bf16 v[192:195], v[216:219], v[16:19], v[192:195]\n\t" \
  "v_mfma_f32_16x16x32_bf16 v[196:199], v[216:219], v[88:91], v[196:199]\n\t" \
  "ds_read_b64_tr_b16 v[216:217], v240 offset:55488\n\t" \
  "ds_read_b64_tr_b16 v[218:219], v240 offset:60096\n\t" \
  "s_waitcnt lgkmcnt(10)\n\t" \
  "v_mfma_f32_16x16x32_bf16 v[200:203], v[220:223], v[16:19], v[200:203]\n\t" \
  "v_mfma_f32_16x16x32_bf16 v[204:207], v[220:223], v[88:91], v[204:207]\n\t" \
  "ds_read_b64_tr_b16 v[220:221], v240 offset:55520\n\t" \
  "ds_read_b64_tr_b16 v[222:223], v240 offset:60128\n\t" \
  "s_waitcnt lgkmcnt(10)\n\t" \
  "v_mfma_f32_16x16x32_bf16 v[208:211], v[224:227], v[16:19], v[208:211]\n\t" \
  "v_mfma_f32_16x16x32_bf16 v[212:215], v[224:227], v[88:91], v[212:215]\n\t" \
  "ds_read_b64_tr_b16 v[224:225], v241 offset:55296\n\t" \
  "ds_read_b64_tr_b16 v[226:227], v241 offset:59904\n\t" \
  "s_waitcnt lgkmcnt(10)\n\t" \
  "v_mfma_f32_16x16x32_bf16 v[192:195], v[228:231], v[20:23], v[192:195]\n\t" \
  "v_mfma_f32_16x16x32_bf16 v[196:199], v[228:231], v[92:95], v[196:199]\n\t" \
  "v_add_u32_e32 v240, 64512, v240\n\t" \
  "ds_read_b64_tr_b16 v[228:229], v240 offset:192\n\t" \
  "ds_read_b64_tr_b16 v[230:231], v240 offset:4800\n\t" \
  "s_waitcnt lgkmcnt(10)\n\t" \
  "v_mfma_f32_16x16x32_bf16 v[200:203], v[232:235], v[20:23], v[200:203]\n\t" \
  "v_mfma_f32_16x16x32_bf16 v[204:207], v[232:235], v[92:95], v[204:207]\n\t" \
  "ds_read_b64_tr_b16 v[232:233], v240 offset:224\n\t" \
  "ds_read_b64_tr_b16 v[234:235], v240 offset:4832\n\t" \
  "s_waitcnt lgkmcnt(10)\n\t" \
  "v_mfma_f32_16x16x32_bf16 v[208:211], v[236:239], v[20:23], v[208:211]\n\t" \
  "v_mfma_f32_16x16x32_bf16 v[212:215], v[236:239], v[92:95], v[212:215]\n\t" \
  "v_add_u32_e32 v241, 64512, v241\n\t" \
  "ds_read_b64_tr_b16 v[236:237], v241 offset:0\n\t" \
  "ds_read_b64_tr_b16 v[238:239], v241 offset:4608\n\t" \
  "v_cvt_pk_bf16_f32 v148, v148, v149\n\t" \
  "v_cvt_pk_bf16_f32 v149, v150, v151\n\t" \
  "v_pk_max_i16 v148, v148, 0\n\t" \
  "v_pk_max_i16 v149, v149, 0\n\t" \
  "v_cvt_pk_bf16_f32 v150, v156, v157\n\t" \
  "v_cvt_pk_bf16_f32 v151, v158, v159\n\t" \
  "v_pk_max_i16 v150, v150, 0\n\t" \
  "v_pk_max_i16 v151, v151, 0\n\t" \
  "buffer_store_dwordx4 v[148:151], %[avo], s[44:47], s71 offen offset:0 sc1\n\t" \
  "s_waitcnt lgkmcnt(10)\n\t" \
  "v_mfma_f32_16x16x32_bf16 v[192:195], v[216:219], v[24:27], v[192:195]\n\t" \
  "v_mfma_f32_16x16x32_bf16 v[196:199], v[216:219], v[96:99], v[196:199]\n\t" \
  "ds_read_b64_tr_b16 v[216:217], v240 offset:9408\n\t" \
  "ds_read_b64_tr_b16 v[218:219], v240 offset:14016\n\t" \
  "s_waitcnt lgkmcnt(10)\n\t" \
  "v_mfma_f32_16x16x32_bf16 v[200:203], v[220:223], v[24:27], v[200:203]\n\t" \
  "v_mfma_f32_16x16x32_bf16 v[204:207], v[220:223], v[96:99], v[204:207]\n\t" \
  "ds_read_b64_tr_b16 v[220:221], v240 offset:9440\n\t" \
  "ds_read_b64_tr_b16 v[222:223], v240 offset:14048\n\t" \
  "s_waitcnt lgkmcnt(10)\n\t" \
  "v_mfma_f32_16x16x32_bf16 v[208:211], v[224:227], v[24:27], v[208:211]\n\t" \
  "v_mfma_f32_16x16x32_bf16 v[212:215], v[224:227], v[96:99], v[212:215]\n\t" \
  "ds_read_b64_tr_b16 v[224:225], v241 offset:9216\n\t" \
  "ds_read_b64_tr_b16 v[226:227], v241 offset:13824\n\t" \
  "s_waitcnt lgkmcnt(10)\n\t" \
  "v_mfma_f32_16x16x32_bf16 v[192:195], v[228:231], v[28:31], v[192:195]\n\t" \
  "v_mfma_f32_16x16x32_bf16 v[196:199], v[228:231], v[100:103], v[196:199]\n\t" \
  "ds_read_b64_tr_b16 v[228:229], v240 offset:18624\n\t" \
  "ds_read_b64_tr_b16 v[230:231], v240 offset:23232\n\t" \
  "s_waitcnt lgkmcnt(10)\n\t" \
  "v_mfma_f32_16x16x32_bf16 v[200:203], v[232:235], v[28:31], v[200:203]\n\t" \
  "v_mfma_f32_16x16x32_bf16 v[204:207], v[232:235], v[100:103], v[204:207]\n\t" \
  "ds_read_b64_tr_b16 v[232:233], v240 offset:18656\n\t" \
  "ds_read_b64_tr_b16 v[234:235], v240 offset:23264\n\t" \
  "s_waitcnt lgkmcnt(10)\n\t" \
  "v_mfma_f32_16x16x32_bf16 v[208:211], v[236:239], v[28:31], v[208:211]\n\t" \
  "v_mfma_f32_16x16x32_bf16 v[212:215], v[236:239], v[100:103], v[212:215]\n\t" \
  "ds_read_b64_tr_b16 v[236:237], v241 offset:18432\n\t" \
  "ds_read_b64_tr_b16 v[238:239], v241 offset:23040\n\t" \
  "s_waitcnt lgkmcnt(10)\n\t" \
  "v_mfma_f32_16x16x32_bf16 v[192:195], v[216:219], v[32:35], v[192:195]\n\t" \
  "v_mfma_f32_16x16x32_bf16 v[196:199], v[216:219], v[104:107], v[196:199]\n\t" \
  "ds_read_b64_tr_b16 v[216:217], v240 offset:27840\n\t" \
  "ds_read_b64_tr_b16 v[218:219], v240 offset:32448\n\t" \
  "v_cvt_pk_bf16_f32 v160, v160, v161\n\t" \
  "v_cvt_pk_bf16_f32 v161, v162, v163\n\t" \
  "v_pk_max_i16 v160, v160, 0\n\t" \
  "v_pk_max_i16 v161, v161, 0\n\t" \
  "v_cvt_pk_bf16_f32 v162, v168, v169\n\t" \
  "v_cvt_pk_bf16_f32 v163, v170, v171\n\t" \
  "v_pk_max_i16 v162, v162, 0\n\t" \
  "v_pk_max_i16 v163, v163, 0\n\t" \
  "buffer_store_dwordx4 v[160:163], %[avo], s[44:47], s69 offen offset:1024 sc1\n\t" \
  "s_waitcnt lgkmcnt(10)\n\t" \
  "v_mfma_f32_16x16x32_bf16 v[200:203], v[220:223], v[32:35], v[200:203]\n\t" \
  "v_mfma_f32_16x16x32_bf16 v[204:207], v[220:223], v[104:107], v[204:207]\n\t" \
  "ds_read_b64_tr_b16 v[220:221], v240 offset:27872\n\t" \
  "ds_read_b64_tr_b16 v[222:223], v240 offset:32480\n\t" \
  "s_waitcnt lgkmcnt(10)\n\t" \
  "v_mfma_f32_16x16x32_bf16 v[208:211], v[224:227], v[32:35], v[208:211]\n\t" \
  "v_mfma_f32_16x16x32_bf16 v[212:215], v[224:227], v[104:107], v[212:215]\n\t" \
  "ds_read_b64_tr_b16 v[224:225], v241 offset:27648\n\t" \
  "ds_read_b64_tr_b16 v[226:227], v241 offset:32256\n\t" \
  "s_waitcnt lgkmcnt(10)\n\t" \
  "v_mfma_f32_16x16x32_bf16 v[192:195], v[228:231], v[36:39], v[192:195]\n\t" \
  "v_mfma_f32_16x16x32_bf16 v[196:199], v[228:231], v[108:111], v[196:199]\n\t" \
  "ds_read_b64_tr_b16 v[228:229], v240 offset:37056\n\t" \
  "ds_read_b64_tr_b16 v[230:231], v240 offset:41664\n\t" \
  "s_waitcnt lgkmcnt(10)\n\t" \
  "v_mfma_f32_16x16x32_bf16 v[200:203], v[232:235], v[36:39], v[200:203]\n\t" \
  "v_mfma_f32_16x16x32_bf16 v[204:207], v[232:235], v[108:111], v[204:207]\n\t" \
  "ds_read_b64_tr_b16 v[232:233], v240 offset:37088\n\t" \
  "ds_read_b64_tr_b16 v[234:235], v240 offset:41696\n\t" \
  "s_waitcnt lgkmcnt(10)\n\t" \
  "v_mfma_f32_16x16x32_bf16 v[208:211], v[236:239], v[36:39], v[208:211]\n\t" \
  "v_mfma_f32_16x16x32_bf16 v[212:215], v[236:239], v[108:111], v[212:215]\n\t" \
  "ds_read_b64_tr_b16 v[236:237], v241 offset:36864\n\t" \
  "ds_read_b64_tr_b16 v[238:239], v241 offset:41472\n\t" \
  "s_waitcnt lgkmcnt(10)\n\t" \
  "v_mfma_f32_16x16x32_bf16 v[192:195], v[216:219], v[40:43], v[192:195]\n\t" \
  "v_mfma_f32_16x16x32_bf16 v[196:199], v[216:219], v[112:115], v[196:199]\n\t" \
  "ds_read_b64_tr_b16 v[216:217], v240 offset:46272\n\t" \
  "ds_read_b64_tr_b16 v[218:219], v240 offset:50880\n\t" \
  "s_waitcnt lgkmcnt(10)\n\t" \
  "v_mfma_f32_16x16x32_bf16 v[200:203], v[220:223], v[40:43], v[200:203]\n\t" \
  "v_mfma_f32_16x16x32_bf16 v[204:207], v[220:223], v[112:115], v[204:207]\n\t" \
  "ds_read_b64_tr_b16 v[220:221], v240 offset:46304\n\t" \
  "ds_read_b64_tr_b16 v[222:223], v240 offset:50912\n\t" \
  "v_cvt_pk_bf16_f32 v164, v164, v165\n\t" \
  "v_cvt_pk_bf16_f32 v165, v166, v167\n\t" \
  "v_pk_max_i16 v164, v164, 0\n\t" \
  "v_pk_max_i16 v165, v165, 0\n\t" \
  "v_cvt_pk_bf16_f32 v166, v172, v173\n\t" \
  "v_cvt_pk_bf16_f32 v167, v174, v175\n\t" \
  "v_pk_max_i16 v166, v166, 0\n\t" \
  "v_pk_max_i16 v167, v167, 0\n\t" \
  "buffer_store_dwordx4 v[164:167], %[avo], s[44:47], s71 offen offset:1024 sc1\n\t" \
  "s_waitcnt lgkmcnt(10)\n\t" \
  "v_mfma_f32_16x16x32_bf16 v[208:211], v[224:227], v[40:43], v[208:211]\n\t" \
  "v_mfma_f32_16x16x32_bf16 v[212:215], v[224:227], v[112:115], v[212:215]\n\t" \
  "ds_read_b64_tr_b16 v[224:225], v241 offset:46080\n\t" \
  "ds_read_b64_tr_b16 v[226:227], v241 offset:50688\n\t" \
  "s_waitcnt lgkmcnt(10)\n\t" \
  "v_mfma_f32_16x16x32_bf16 v[192:195], v[228:231], v[44:47], v[192:195]\n\t" \
  "v_mfma_f32_16x16x32_bf16 v[196:199], v[228:231], v[116:119], v[196:199]\n\t" \
  "ds_read_b64_tr_b16 v[228:229], v240 offset:55488\n\t" \
  "ds_read_b64_tr_b16 v[230:231], v240 offset:60096\n\t" \
  "s_waitcnt lgkmcnt(10)\n\t" \
  "v_mfma_f32_16x16x32_bf16 v[200:203], v[232:235], v[44:47], v[200:203]\n\t" \
  "v_mfma_f32_16x16x32_bf16 v[204:207], v[232:235], v[116:119], v[204:207]\n\t" \
  "ds_read_b64_tr_b16 v[232:233], v240 offset:55520\n\t" \
  "ds_read_b64_tr_b16 v[234:235], v240 offset:60128\n\t" \
  "s_waitcnt lgkmcnt(10)\n\t" \
  "v_mfma_f32_16x16x32_bf16 v[208:211], v[236:239], v[44:47], v[208:211]\n\t" \
  "v_mfma_f32_16x16x32_bf16 v[212:215], v[236:239], v[116:119], v[212:215]\n\t" \
  "ds_read_b64_tr_b16 v[236:237], v241 offset:55296\n\t" \
  "ds_read_b64_tr_b16 v[238:239], v241 offset:59904\n\t" \
  "s_waitcnt lgkmcnt(10)\n\t" \
  "v_mfma_f32_16x16x32_bf16 v[192:195], v[216:219], v[48:51], v[192:195]\n\t" \
  "v_mfma_f32_16x16x32_bf16 v[196:199], v[216:219], v[120:123], v[196:199]\n\t" \
  "v_add_u32_e32 v240, 64512, v240\n\t" \
  "ds_read_b64_tr_b16 v[216:217], v240 offset:192\n\t" \
  "ds_read_b64_tr_b16 v[218:219], v240 offset:4800\n\t" \
  "s_waitcnt lgkmcnt(10)\n\t" \
  "v_mfma_f32_16x16x32_bf16 v[200:203], v[220:223], v[48:51], v[200:203]\n\t" \
  "v_mfma_f32_16x16x32_bf16 v[204:207], v[220:223], v[120:123], v[204:207]\n\t" \
  "ds_read_b64_tr_b16 v[220:221], v240 offset:224\n\t" \
  "ds_read_b64_tr_b16 v[222:223], v240 offset:4832\n\t" \
  "s_waitcnt lgkmcnt(10)\n\t" \
  "v_mfma_f32_16x16x32_bf16 v[208:211], v[224:227], v[48:51], v[208:211]\n\t" \
  "v_mfma_f32_16x16x32_bf16 v[212:215], v[224:227], v[120:123], v[212:215]\n\t" \
  "v_add_u32_e32 v241, 64512, v241\n\t" \
  "ds_read_b64_tr_b16 v[224:225], v241 offset:0\n\t" \
  "ds_read_b64_tr_b16 v[226:227], v241 offset:4608\n\t" \
  "v_cvt_pk_bf16_f32 v176, v176, v177\n\t" \
  "v_cvt_pk_bf16_f32 v177, v178, v179\n\t" \
  "v_pk_max_i16 v176, v176, 0\n\t" \
  "v_pk_max_i16 v177, v177, 0\n\t" \
  "v_cvt_pk_bf16_f32 v178, v184, v185\n\t" \
  "v_cvt_pk_bf16_f32 v179, v186, v187\n\t" \
  "v_pk_max_i16 v178, v178, 0\n\t" \
  "v_pk_max_i16 v179, v179, 0\n\t" \
  "buffer_store_dwordx4 v[176:179], %[avo], s[44:47], s69 offen offset:2048 sc1\n\t" \
  "s_waitcnt lgkmcnt(10)\n\t" \
  "v_mfma_f32_16x16x32_bf16 v[192:195], v[228:231], v[52:55], v[192:195]\n\t" \
  "v_mfma_f32_16x16x32_bf16 v[196:199], v[228:231], v[124:127], v[196:199]\n\t" \
  "ds_read_b64_tr_b16 v[228:229], v240 offset:9408\n\t" \
  "ds_read_b64_tr_b16 v[230:231], v240 offset:14016\n\t" \
  "s_waitcnt lgkmcnt(10)\n\t" \
  "v_mfma_f32_16x16x32_bf16 v[200:203], v[232:235], v[52:55], v[200:203]\n\t" \
  "v_mfma_f32_16x16x32_bf16 v[204:207], v[232:235], v[124:127], v[204:207]\n\t" \
  "ds_read_b64_tr_b16 v[232:233], v240 offset:9440\n\t" \
  "ds_read_b64_tr_b16 v[234:235], v240 offset:14048\n\t" \
  "s_waitcnt lgkmcnt(10)\n\t" \
  "v_mfma_f32_16x16x32_bf16 v[208:211], v[236:239], v[52:55], v[208:211]\n\t" \
  "v_mfma_f32_16x16x32_bf16 v[212:215], v[236:239], v[124:127], v[212:215]\n\t" \
  "ds_read_b64_tr_b16 v[236:237], v241 offset:9216\n\t" \
  "ds_read_b64_tr_b16 v[238:239], v241 offset:13824\n\t" \
  "s_waitcnt lgkmcnt(10)\n\t" \
  "v_mfma_f32_16x16x32_bf16 v[192:195], v[216:219], v[56:59], v[192:195]\n\t" \
  "v_mfma_f32_16x16x32_bf16 v[196:199], v[216:219], v[128:131], v[196:199]\n\t" \
  "ds_read_b64_tr_b16 v[216:217], v240 offset:18624\n\t" \
  "ds_read_b64_tr_b16 v[218:219], v240 offset:23232\n\t" \
  "s_waitcnt lgkmcnt(10)\n\t" \
  "v_mfma_f32_16x16x32_bf16 v[200:203], v[220:223], v[56:59], v[200:203]\n\t" \
  "v_mfma_f32_16x16x32_bf16 v[204:207], v[220:223], v[128:131], v[204:207]\n\t" \
  "ds_read_b64_tr_b16 v[220:221], v240 offset:18656\n\t" \
  "ds_read_b64_tr_b16 v[222:223], v240 offset:23264\n\t" \
  "s_waitcnt lgkmcnt(10)\n\t" \
  "v_mfma_f32_16x16x32_bf16 v[208:211], v[224:227], v[56:59], v[208:211]\n\t" \
  "v_mfma_f32_16x16x32_bf16 v[212:215], v[224:227], v[128:131], v[212:215]\n\t" \
  "ds_read_b64_tr_b16 v[224:225], v241 offset:18432\n\t" \
  "ds_read_b64_tr_b16 v[226:227], v241 offset:23040\n\t" \
  "s_waitcnt lgkmcnt(10)\n\t" \
  "v_mfma_f32_16x16x32_bf16 v[192:195], v[228:231], v[60:63], v[192:195]\n\t" \
  "v_mfma_f32_16x16x32_bf16 v[196:199], v[228:231], v[132:135], v[196:199]\n\t" \
  "v_add_u32_e32 v240, 4294838272, v240\n\t" \
  "ds_read_b64_tr_b16 v[228:229], v240 offset:192\n\t" \
  "ds_read_b64_tr_b16 v[230:231], v240 offset:4800\n\t" \
  "v_cvt_pk_bf16_f32 v180, v180, v181\n\t" \
  "v_cvt_pk_bf16_f32 v181, v182, v183\n\t" \
  "v_pk_max_i16 v180, v180, 0\n\t" \
  "v_pk_max_i16 v181, v181, 0\n\t" \
  "v_cvt_pk_bf16_f32 v182, v188, v189\n\t" \
  "v_cvt_pk_bf16_f32 v183, v190, v191\n\t" \
  "v_pk_max_i16 v182, v182, 0\n\t" \
  "v_pk_max_i16 v183, v183, 0\n\t" \
  "buffer_store_dwordx4 v[180:183], %[avo], s[44:47], s71 offen offset:2048 sc1\n\t" \
  "s_waitcnt lgkmcnt(10)\n\t" \
  "v_mfma_f32_16x16x32_bf16 v[200:203], v[232:235], v[60:63], v[200:203]\n\t" \
  "v_mfma_f32_16x16x32_bf16 v[204:207], v[232:235], v[132:135], v[204:207]\n\t" \
  "ds_read_b64_tr_b16 v[232:233], v240 offset:224\n\t" \
  "ds_read_b64_tr_b16 v[234:235], v240 offset:4832\n\t" \
  "s_waitcnt lgkmcnt(10)\n\t" \
  "v_mfma_f32_16x16x32_bf16 v[208:211], v[236:239], v[60:63], v[208:211]\n\t" \
  "v_mfma_f32_16x16x32_bf16 v[212:215], v[236:239], v[132:135], v[212:215]\n\t" \
  "v_add_u32_e32 v241, 4294838272, v241\n\t" \
  "ds_read_b64_tr_b16 v[236:237], v241 offset:0\n\t" \
  "ds_read_b64_tr_b16 v[238:239], v241 offset:4608\n\t" \
  "s_waitcnt lgkmcnt(10)\n\t" \
  "v_mfma_f32_16x16x32_bf16 v[192:195], v[216:219], v[64:67], v[192:195]\n\t" \
  "v_mfma_f32_16x16x32_bf16 v[196:199], v[216:219], v[136:139], v[196:199]\n\t" \
  "s_waitcnt lgkmcnt(8)\n\t" \
  "v_mfma_f32_16x16x32_bf16 v[200:203], v[220:223], v[64:67], v[200:203]\n\t" \
  "v_mfma_f32_16x16x32_bf16 v[204:207], v[220:223], v[136:139], v[204:207]\n\t" \
  "s_waitcnt lgkmcnt(6)\n\t" \
  "v_mfma_f32_16x16x32_bf16 v[208:211], v[224:227], v[64:67], v[208:211]\n\t" \
  "v_mfma_f32_16x16x32_bf16 v[212:215], v[224:227], v[136:139], v[212:215]\n\t" \
  "s_waitcnt lgkmcnt(4)\n\t" \
  "v_mfma_f32_16x16x32_bf16 v[192:195], v[228:231], v[68:71], v[192:195]\n\t" \
  "v_mfma_f32_16x16x32_bf16 v[196:199], v[228:231], v[140:143], v[196:199]\n\t" \
  "s_waitcnt lgkmcnt(2)\n\t" \
  "v_mfma_f32_16x16x32_bf16 v[200:203], v[232:235], v[68:71], v[200:203]\n\t" \
  "v_mfma_f32_16x16x32_bf16 v[204:207], v[232:235], v[140:143], v[204:207]\n\t" \
  "s_waitcnt lgkmcnt(0)\n\t" \
  "v_mfma_f32_16x16x32_bf16 v[208:211], v[236:239], v[68:71], v[208:211]\n\t" \
  "v_mfma_f32_16x16x32_bf16 v[212:215], v[236:239], v[140:143], v[212:215]\n\t" \
  "s_nop 7\n\t" \
  "v_cvt_pk_bf16_f32 v192, v192, v193\n\t" \
  "v_cvt_pk_bf16_f32 v193, v194, v195\n\t" \
  "v_pk_max_i16 v192, v192, 0\n\t" \
  "v_pk_max_i16 v193, v193, 0\n\t" \
  "v_cvt_pk_bf16_f32 v194, v200, v201\n\t" \
  "v_cvt_pk_bf16_f32 v195, v202, v203\n\t" \
  "v_pk_max_i16 v194, v194, 0\n\t" \
  "v_pk_max_i16 v195, v195, 0\n\t" \
  "buffer_store_dwordx4 v[192:195], %[avo], s[44:47], s69 offen offset:3072 sc1\n\t" \
  "v_cvt_pk_bf16_f32 v196, v196, v197\n\t" \
  "v_cvt_pk_bf16_f32 v197, v198, v199\n\t" \
  "v_pk_max_i16 v196, v196, 0\n\t" \
  "v_pk_max_i16 v197, v197, 0\n\t" \
  "v_cvt_pk_bf16_f32 v198, v204, v205\n\t" \
  "v_cvt_pk_bf16_f32 v199, v206, v207\n\t" \
  "v_pk_max_i16 v198, v198, 0\n\t" \
  "v_pk_max_i16 v199, v199, 0\n\t" \
  "buffer_store_dwordx4 v[196:199], %[avo], s[44:47], s71 offen offset:3072 sc1\n\t" \
  "v_cvt_pk_bf16_f32 v208, v208, v209\n\t" \
  "v_cvt_pk_bf16_f32 v209, v210, v211\n\t" \
  "v_pk_max_i16 v208, v208, 0\n\t" \
  "v_pk_max_i16 v209, v209, 0\n\t" \
  "buffer_store_dwordx2 v[208:209], %[avo], s[44:47], s70 offen sc1\n\t" \
  "v_cvt_pk_bf16_f32 v212, v212, v213\n\t" \
  "v_cvt_pk_bf16_f32 v213, v214, v215\n\t" \
  "v_pk_max_i16 v212, v212, 0\n\t" \
  "v_pk_max_i16 v213, v213, 0\n\t" \
  "buffer_store_dwordx2 v[212:213], %[avo], s[44:47], s72 offen sc1\n\t" \
  ""
#define GEMM_FFN1_CLOB "memory","vcc","scc","v0","v1","v2","v3","v4","v5","v6","v7","v8","v9","v10","v11","v12","v13","v14","v15","v16","v17","v18","v19","v20","v21","v22","v23","v24","v25","v26","v27","v28","v29","v30","v31","v32","v33","v34","v35","v36","v37","v38","v39","v40","v41","v42","v43","v44","v45","v46","v47","v48","v49","v50","v51","v52","v53","v54","v55","v56","v57","v58","v59","v60","v61","v62","v63","v64","v65","v66","v67","v68","v69","v70","v71","v72","v73","v74","v75","v76","v77","v78","v79","v80","v81","v82","v83","v84","v85","v86","v87","v88","v89","v90","v91","v92","v93","v94","v95","v96","v97","v98","v99","v100","v101","v102","v103","v104","v105","v106","v107","v108","v109","v110","v111","v112","v113","v114","v115","v116","v117","v118","v119","v120","v121","v122","v123","v124","v125","v126","v127","v128","v129","v130","v131","v132","v133","v134","v135","v136","v137","v138","v139","v140","v141","v142","v143","v144","v145","v146","v147","v148","v149","v150","v151","v152","v153","v154","v155","v156","v157","v158","v159","v160","v161","v162","v163","v164","v165","v166","v167","v168","v169","v170","v171","v172","v173","v174","v175","v176","v177","v178","v179","v180","v181","v182","v183","v184","v185","v186","v187","v188","v189","v190","v191","v192","v193","v194","v195","v196","v197","v198","v199","v200","v201","v202","v203","v204","v205","v206","v207","v208","v209","v210","v211","v212","v213","v214","v215","v216","v217","v218","v219","v220","v221","v222","v223","v224","v225","v226","v227","v228","v229","v230","v231","v232","v233","v234","v235","v236","v237","v238","v239","v240","v241","s36","s37","s38","s39","s40","s41","s42","s43","s44","s45","s46","s47","s48","s49","s50","s51","s52","s53","s54","s55","s56","s57","s58","s59","s60","s61","s62","s63","s64","s65","s66","s67","s68","s69","s70","s71","s72","s73"
#define GEMM_P18_TEXT \
  "s_mov_b32 s60, %[wid]\n\t" \
  "s_mov_b32 s61, %[lds0]\n\t" \
  "s_mov_b32 s62, %[wpitch]\n\t" \
  "s_mov_b32 s63, %[ars]\n\t" \
  "s_mov_b32 s64, %[ors]\n\t" \
  "s_mov_b32 s65, %[par]\n\t" \
  "s_mov_b64 s[36:37], %[wptr]\n\t" \
  "s_and_b32 s37, s37, 0xffff\n\t" \
  "s_mov_b32 s38, 0x7fffffff\n\t" \
  "s_mov_b32 s39, 0x20000\n\t" \
  "s_lshl_b32 s48, s60, 1\n\t" \
  "s_mul_i32 s49, s48, s63\n\t" \
  "s_mov_b64 s[40:41], %[aptr]\n\t" \
  "s_add_u32 s40, s40, s49\n\t" \
  "s_addc_u32 s41, s41, 0\n\t" \
  "s_and_b32 s41, s41, 0xffff\n\t" \
  "s_mov_b32 s42, 0x7fffffff\n\t" \
  "s_mov_b32 s43, 0x20000\n\t" \
  "s_lshl_b32 s48, s60, 10\n\t" \
  "s_add_u32 s66, s61, s48\n\t" \
  "s_add_u32 s67, s61, 8192\n\t" \
  "s_lshl_b32 s68, s62, 5\n\t" \
  "s_mul_i32 s51, s68, 0\n\t" \
  "s_cmp_lg_u32 s60, 0\n\t" \
  "s_cbranch_scc1 L_p8_0%=\n\t" \
  "s_add_u32 m0, s67, 0\n\t" \
  "s_nop 0\n\t" \
  "buffer_load_dwordx4 %[wvo8], s[36:39], s51 offen lds\n\t" \
  "L_p8_0%=:\n\t" \
  "s_add_u32 m0, s66, 0\n\t" \
  "s_nop 0\n\t" \
  "buffer_load_dwordx4 %[wvo0], s[36:39], s51 offen lds\n\t" \
  "s_mov_b32 s52, 0\n\t" \
  "buffer_load_dwordx4 v[0:3], %[avo], s[40:43], s52 offen offset:0\n\t" \
  "s_add_u32 s52, s63, 0\n\t" \
  "buffer_load_dwordx4 v[72:75], %[avo], s[40:43], s52 offen offset:0\n\t" \
  "s_mul_i32 s51, s68, 1\n\t" \
  "s_cmp_lg_u32 s60, 1\n\t" \
  "s_cbranch_scc1 L_p8_1%=\n\t" \
  "s_add_u32 m0, s67, 9216\n\t" \
  "s_nop 0\n\t" \
  "buffer_load_dwordx4 %[wvo8], s[36:39], s51 offen lds\n\t" \
  "L_p8_1%=:\n\t" \
  "s_add_u32 m0, s66, 9216\n\t" \
  "s_nop 0\n\t" \
  "buffer_load_dwordx4 %[wvo0], s[36:39], s51 offen lds\n\t" \
  "s_mov_b32 s52, 0\n\t" \
  "buffer_load_dwordx4 v[4:7], %[avo], s[40:43], s52 offen offset:1024\n\t" \
  "s_add_u32 s52, s63, 0\n\t" \
  "buffer_load_dwordx4 v[76:79], %[avo], s[40:43], s52 offen offset:1024\n\t" \
  "s_mul_i32 s51, s68, 2\n\t" \
  "s_cmp_lg_u32 s60, 2\n\t" \
  "s_cbranch_scc1 L_p8_2%=\n\t" \
  "s_add_u32 m0, s67, 18432\n\t" \
  "s_nop 0\n\t" \
  "buffer_load_dwordx4 %[wvo8], s[36:39], s51 offen lds\n\t" \
  "L_p8_2%=:\n\t" \
  "s_add_u32 m0, s66, 18432\n\t" \
  "s_nop 0\n\t" \
  "buffer_load_dwordx4 %[wvo0], s[36:39], s51 offen lds\n\t" \
  "s_mov_b32 s52, 0\n\t" \
  "buffer_load_dwordx4 v[8:11], %[avo], s[40:43], s52 offen offset:2048\n\t" \
  "s_add_u32 s52, s63, 0\n\t" \
  "buffer_load_dwordx4 v[80:83], %[avo], s[40:43], s52 offen offset:2048\n\t" \
  "s_mul_i32 s51, s68, 3\n\t" \
  "s_cmp_lg_u32 s60, 3\n\t" \
  "s_cbranch_scc1 L_p8_3%=\n\t" \
  "s_add_u32 m0, s67, 27648\n\t" \
  "s_nop 0\n\t" \
  "buffer_load_dwordx4 %[wvo8], s[36:39], s51 offen lds\n\t" \
  "L_p8_3%=:\n\t" \
  "s_add_u32 m0, s66, 27648\n\t" \
  "s_nop 0\n\t" \
  "buffer_load_dwordx4 %[wvo0], s[36:39], s51 offen lds\n\t" \
  "s_mov_b32 s52, 0\n\t" \
  "buffer_load_dwordx4 v[12:15], %[avo], s[40:43], s52 offen offset:3072\n\t" \
  "s_add_u32 s52, s63, 0\n\t" \
  "buffer_load_dwordx4 v[84:87], %[avo], s[40:43], s52 offen offset:3072\n\t" \
  "s_mul_i32 s51, s68, 4\n\t" \
  "s_cmp_lg_u32 s60, 4\n\t" \
  "s_cbranch_scc1 L_p8_4%=\n\t" \
  "s_add_u32 m0, s67, 36864\n\t" \
  "s_nop 0\n\t" \
  "buffer_load_dwordx4 %[wvo8], s[36:39], s51 offen lds\n\t" \
  "L_p8_4%=:\n\t" \
  "s_add_u32 m0, s66, 36864\n\t" \
  "s_nop 0\n\t" \
  "buffer_load_dwordx4 %[wvo0], s[36:39], s51 offen lds\n\t" \
  "s_lshl_b32 s48, s60, 1\n\t" \
  "s_mul_i32 s49, s48, s64\n\t" \
  "s_mov_b64 s[44:45], %[optr]\n\t" \
  "s_add_u32 s44, s44, s49\n\t" \
  "s_addc_u32 s45, s45, 0\n\t" \
  "s_and_b32 s45, s45, 0xffff\n\t" \
  "s_mov_b32 s46, 0x7fffffff\n\t" \
  "s_mov_b32 s47, 0x20000\n\t" \
  "s_lshl_b32 s69, s65, 10\n\t" \
  "s_add_u32 s71, s69, s64\n\t" \
  "s_cmp_lg_u32 s65, 0\n\t" \
  "s_cselect_b32 s70, 8, 4096\n\t" \
  "s_add_u32 s72, s70, s64\n\t" \
  "s_lshl_b32 s48, s65, 5\n\t" \
  "v_add_u32_e32 v240, s48, %[tro]\n\t" \
  "s_cmp_lg_u32 s65, 0\n\t" \
  "s_cselect_b32 s48, 0, 256\n\t" \
  "v_add_u32_e32 v241, s48, %[tro]\n\t" \
  "s_waitcnt vmcnt(12)\n\t" \
  "s_barrier\n\t" \
  "ds_read_b64_tr_b16 v[216:217], v240 offset:0\n\t" \
  "ds_read_b64_tr_b16 v[218:219], v240 offset:4608\n\t" \
  "ds_read_b64_tr_b16 v[220:221], v240 offset:32\n\t" \
  "ds_read_b64_tr_b16 v[222:223], v240 offset:4640\n\t" \
  "ds_read_b64_tr_b16 v[224:225], v240 offset:64\n\t" \
  "ds_read_b64_tr_b16 v[226:227], v240 offset:4672\n\t" \
  "ds_read_b64_tr_b16 v[228:229], v240 offset:96\n\t" \
  "ds_read_b64_tr_b16 v[230:231], v240 offset:4704\n\t" \
  "ds_read_b64_tr_b16 v[232:233], v240 offset:128\n\t" \
  "ds_read_b64_tr_b16 v[234:235], v240 offset:4736\n\t" \
  "ds_read_b64_tr_b16 v[236:237], v240 offset:160\n\t" \
  "ds_read_b64_tr_b16 v[238:239], v240 offset:4768\n\t" \
  "s_waitcnt vmcnt(10)\n\t" \
  "s_waitcnt lgkmcnt(10)\n\t" \
  "v_mfma_f32_16x16x32_bf16 v[144:147], v[216:219], v[0:3], 0\n\t" \
  "v_mfma_f32_16x16x32_bf16 v[148:151], v[216:219], v[72:75], 0\n\t" \
  "ds_read_b64_tr_b16 v[216:217], v240 offset:192\n\t" \
  "ds_read_b64_tr_b16 v[218:219], v240 offset:4800\n\t" \
  "s_waitcnt lgkmcnt(10)\n\t" \
  "v_mfma_f32_16x16x32_bf16 v[152:155], v[220:223], v[0:3], 0\n\t" \
  "v_mfma_f32_16x16x32_bf16 v[156:159], v[220:223], v[72:75], 0\n\t" \
  "s_mul_i32 s51, s68, 5\n\t" \
  "s_cmp_lg_u32 s60, 5\n\t" \
  "s_cbranch_scc1 L_p8_5%=\n\t" \
  "s_add_u32 m0, s67, 46080\n\t" \
  "s_nop 0\n\t" \
  "buffer_load_dwordx4 %[wvo8], s[36:39], s51 offen lds\n\t" \
  "L_p8_5%=:\n\t" \
  "s_add_u32 m0, s66, 46080\n\t" \
  "s_nop 0\n\t" \
  "buffer_load_dwordx4 %[wvo0], s[36:39], s51 offen lds\n\t" \
  "ds_read_b64_tr_b16 v[220:221], v240 offset:224\n\t" \
  "ds_read_b64_tr_b16 v[222:223], v240 offset:4832\n\t" \
  "s_waitcnt lgkmcnt(10)\n\t" \
  "v_mfma_f32_16x16x32_bf16 v[160:163], v[224:227], v[0:3], 0\n\t" \
  "v_mfma_f32_16x16x32_bf16 v[164:167], v[224:227], v[72:75], 0\n\t" \
  "ds_read_b64_tr_b16 v[224:225], v241 offset:0\n\t" \
  "ds_read_b64_tr_b16 v[226:227], v241 offset:4608\n\t" \
  "s_waitcnt lgkmcnt(10)\n\t" \
  "v_mfma_f32_16x16x32_bf16 v[168:171], v[228:231], v[0:3], 0\n\t" \
  "v_mfma_f32_16x16x32_bf16 v[172:175], v[228:231], v[72:75], 0\n\t" \
  "s_mov_b32 s52, 4096\n\t" \
  "buffer_load_dwordx4 v[16:19], %[avo], s[40:43], s52 offen offset:0\n\t" \
  "s_waitcnt vmcnt(8)\n\t" \
  "s_barrier\n\t" \
  "ds_read_b64_tr_b16 v[228:229], v240 offset:9216\n\t" \
  "ds_read_b64_tr_b16 v[230:231], v240 offset:13824\n\t" \
  "s_waitcnt lgkmcnt(10)\n\t" \
  "v_mfma_f32_16x16x32_bf16 v[176:179], v[232:235], v[0:3], 0\n\t" \
  "v_mfma_f32_16x16x32_bf16 v[180:183], v[232:235], v[72:75], 0\n\t" \
  "ds_read_b64_tr_b16 v[232:233], v240 offset:9248\n\t" \
  "ds_read_b64_tr_b16 v[234:235], v240 offset:13856\n\t" \
  "s_waitcnt lgkmcnt(10)\n\t" \
  "v_mfma_f32_16x16x32_bf16 v[184:187], v[236:239], v[0:3], 0\n\t" \
  "v_mfma_f32_16x16x32_bf16 v[188:191], v[236:239], v[72:75], 0\n\t" \
  "s_add_u32 s52, s63, 4096\n\t" \
  "buffer_load_dwordx4 v[88:91], %[avo], s[40:43], s52 offen offset:0\n\t" \
  "ds_read_b64_tr_b16 v[236:237], v240 offset:9280\n\t" \
  "ds_read_b64_tr_b16 v[238:239], v240 offset:13888\n\t" \
  "s_waitcnt lgkmcnt(10)\n\t" \
  "v_mfma_f32_16x16x32_bf16 v[192:195], v[216:219], v[0:3], 0\n\t" \
  "v_mfma_f32_16x16x32_bf16 v[196:199], v[216:219], v[72:75], 0\n\t" \
  "ds_read_b64_tr_b16 v[216:217], v240 offset:9312\n\t" \
  "ds_read_b64_tr_b16 v[218:219], v240 offset:13920\n\t" \
  "s_waitcnt lgkmcnt(10)\n\t" \
  "v_mfma_f32_16x16x32_bf16 v[200:203], v[220:223], v[0:3], 0\n\t" \
  "v_mfma_f32_16x16x32_bf16 v[204:207], v[220:223], v[72:75], 0\n\t" \
  "ds_read_b64_tr_b16 v[220:221], v240 offset:9344\n\t" \
  "ds_read_b64_tr_b16 v[222:223], v240 offset:13952\n\t" \
  "s_waitcnt lgkmcnt(10)\n\t" \
  "v_mfma_f32_16x16x32_bf16 v[208:211], v[224:227], v[0:3], 0\n\t" \
  "v_mfma_f32_16x16x32_bf16 v[212:215], v[224:227], v[72:75], 0\n\t" \
  "ds_read_b64_tr_b16 v[224:225], v240 offset:9376\n\t" \
  "ds_read_b64_tr_b16 v[226:227], v240 offset:13984\n\t" \
  "s_waitcnt lgkmcnt(10)\n\t" \
  "v_mfma_f32_16x16x32_bf16 v[144:147], v[228:231], v[4:7], v[144:147]\n\t" \
  "v_mfma_f32_16x16x32_bf16 v[148:151], v[228:231], v[76:79], v[148:151]\n\t" \
  "s_mul_i32 s51, s68, 6\n\t" \
  "s_cmp_lg_u32 s60, 6\n\t" \
  "s_cbranch_scc1 L_p8_6%=\n\t" \
  "s_add_u32 m0, s67, 55296\n\t" \
  "s_nop 0\n\t" \
  "buffer_load_dwordx4 %[wvo8], s[36:39], s51 offen lds\n\t" \
  "L_p8_6%=:\n\t" \
  "s_add_u32 m0, s66, 55296\n\t" \
  "s_nop 0\n\t" \
  "buffer_load_dwordx4 %[wvo0], s[36:39], s51 offen lds\n\t" \
  "ds_read_b64_tr_b16 v[228:229], v240 offset:18432\n\t" \
  "ds_read_b64_tr_b16 v[230:231], v240 offset:23040\n\t" \
  "s_waitcnt lgkmcnt(10)\n\t" \
  "v_mfma_f32_16x16x32_bf16 v[152:155], v[232:235], v[4:7], v[152:155]\n\t" \
  "v_mfma_f32_16x16x32_bf16 v[156:159], v[232:235], v[76:79], v[156:159]\n\t" \
  "ds_read_b64_tr_b16 v[232:233], v240 offset:18464\n\t" \
  "ds_read_b64_tr_b16 v[234:235], v240 offset:23072\n\t" \
  "s_waitcnt lgkmcnt(10)\n\t" \
  "v_mfma_f32_16x16x32_bf16 v[160:163], v[236:239], v[4:7], v[160:163]\n\t" \
  "v_mfma_f32_16x16x32_bf16 v[164:167], v[236:239], v[76:79], v[164:167]\n\t" \
  "s_mov_b32 s52, 4096\n\t" \
  "buffer_load_dwordx4 v[20:23], %[avo], s[40:43], s52 offen offset:1024\n\t" \
  "ds_read_b64_tr_b16 v[236:237], v240 offset:18496\n\t" \
  "ds_read_b64_tr_b16 v[238:239], v240 offset:23104\n\t" \
  "s_waitcnt lgkmcnt(10)\n\t" \
  "v_mfma_f32_16x16x32_bf16 v[168:171], v[216:219], v[4:7], v[168:171]\n\t" \
  "v_mfma_f32_16x16x32_bf16 v[172:175], v[216:219], v[76:79], v[172:175]\n\t" \
  "ds_read_b64_tr_b16 v[216:217], v240 offset:18528\n\t" \
  "ds_read_b64_tr_b16 v[218:219], v240 offset:23136\n\t" \
  "s_waitcnt lgkmcnt(10)\n\t" \
  "v_mfma_f32_16x16x32_bf16 v[176:179], v[220:223], v[4:7], v[176:179]\n\t" \
  "v_mfma_f32_16x16x32_bf16 v[180:183], v[220:223], v[76:79], v[180:183]\n\t" \
  "s_add_u32 s52, s63, 4096\n\t" \
  "buffer_load_dwordx4 v[92:95], %[avo], s[40:43], s52 offen offset:1024\n\t" \
  "ds_read_b64_tr_b16 v[220:221], v240 offset:18560\n\t" \
  "ds_read_b64_tr_b16 v[222:223], v240 offset:23168\n\t" \
  "s_waitcnt lgkmcnt(10)\n\t" \
  "v_mfma_f32_16x16x32_bf16 v[184:187], v[224:227], v[4:7], v[184:187]\n\t" \
  "v_mfma_f32_16x16x32_bf16 v[188:191], v[224:227], v[76:79], v[188:191]\n\t" \
  "ds_read_b64_tr_b16 v[224:225], v240 offset:18592\n\t" \
  "ds_read_b64_tr_b16 v[226:227], v240 offset:23200\n\t" \
  "s_waitcnt vmcnt(10)\n\t" \
  "s_waitcnt lgkmcnt(10)\n\t" \
  "v_mfma_f32_16x16x32_bf16 v[144:147], v[228:231], v[8:11], v[144:147]\n\t" \
  "v_mfma_f32_16x16x32_bf16 v[148:151], v[228:231], v[80:83], v[148:151]\n\t" \
  "s_mul_i32 s51, s68, 7\n\t" \
  "s_cmp_lg_u32 s60, 7\n\t" \
  "s_cbranch_scc1 L_p8_7%=\n\t" \
  "s_add_u32 m0, s67, 64512\n\t" \
  "s_nop 0\n\t" \
  "buffer_load_dwordx4 %[wvo8], s[36:39], s51 offen lds\n\t" \
  "L_p8_7%=:\n\t" \
  "s_add_u32 m0, s66, 64512\n\t" \
  "s_nop 0\n\t" \
  "buffer_load_dwordx4 %[wvo0], s[36:39], s51 offen lds\n\t" \
  "s_waitcnt vmcnt(6)\n\t" \
  "s_barrier\n\t" \
  "ds_read_b64_tr_b16 v[228:229], v240 offset:27648\n\t" \
  "ds_read_b64_tr_b16 v[230:231], v240 offset:32256\n\t" \
  "s_waitcnt lgkmcnt(10)\n\t" \
  "v_mfma_f32_16x16x32_bf16 v[152:155], v[232:235], v[8:11], v[152:155]\n\t" \
  "v_mfma_f32_16x16x32_bf16 v[156:159], v[232:235], v[80:83], v[156:159]\n\t" \
  "ds_read_b64_tr_b16 v[232:233], v240 offset:27680\n\t" \
  "ds_read_b64_tr_b16 v[234:235], v240 offset:32288\n\t" \
  "s_waitcnt lgkmcnt(10)\n\t" \
  "v_mfma_f32_16x16x32_bf16 v[160:163], v[236:239], v[8:11], v[160:163]\n\t" \
  "v_mfma_f32_16x16x32_bf16 v[164:167], v[236:239], v[80:83], v[164:167]\n\t" \
  "s_mov_b32 s52, 4096\n\t" \
  "buffer_load_dwordx4 v[24:27], %[avo], s[40:43], s52 offen offset:2048\n\t" \
  "ds_read_b64_tr_b16 v[236:237], v240 offset:27712\n\t" \
  "ds_read_b64_tr_b16 v[238:239], v240 offset:32320\n\t" \
  "s_waitcnt lgkmcnt(10)\n\t" \
  "v_mfma_f32_16x16x32_bf16 v[168:171], v[216:219], v[8:11], v[168:171]\n\t" \
  "v_mfma_f32_16x16x32_bf16 v[172:175], v[216:219], v[80:83], v[172:175]\n\t" \
  "ds_read_b64_tr_b16 v[216:217], v240 offset:27744\n\t" \
  "ds_read_b64_tr_b16 v[218:219], v240 offset:32352\n\t" \
  "s_waitcnt lgkmcnt(10)\n\t" \
  "v_mfma_f32_16x16x32_bf16 v[176:179], v[220:223], v[8:11], v[176:179]\n\t" \
  "v_mfma_f32_16x16x32_bf16 v[180:183], v[220:223], v[80:83], v[180:183]\n\t" \
  "s_add_u32 s52, s63, 4096\n\t" \
  "buffer_load_dwordx4 v[96:99], %[avo], s[40:43], s52 offen offset:2048\n\t" \
  "ds_read_b64_tr_b16 v[220:221], v240 offset:27776\n\t" \
  "ds_read_b64_tr_b16 v[222:223], v240 offset:32384\n\t" \
  "s_waitcnt lgkmcnt(10)\n\t" \
  "v_mfma_f32_16x16x32_bf16 v[184:187], v[224:227], v[8:11], v[184:187]\n\t" \
  "v_mfma_f32_16x16x32_bf16 v[188:191], v[224:227], v[80:83], v[188:191]\n\t" \
  "ds_read_b64_tr_b16 v[224:225], v240 offset:27808\n\t" \
  "ds_read_b64_tr_b16 v[226:227], v240 offset:32416\n\t" \
  "s_waitcnt lgkmcnt(10)\n\t" \
  "v_mfma_f32_16x16x32_bf16 v[144:147], v[228:231], v[12:15], v[144:147]\n\t" \
  "v_mfma_f32_16x16x32_bf16 v[148:151], v[228:231], v[84:87], v[148:151]\n\t" \
  "s_mul_i32 s51, s68, 8\n\t" \
  "s_cmp_lg_u32 s60, 0\n\t" \
  "s_cbranch_scc1 L_p8_8%=\n\t" \
  "s_add_u32 m0, s67, 73728\n\t" \
  "s_nop 0\n\t" \
  "buffer_load_dwordx4 %[wvo8], s[36:39], s51 offen lds\n\t" \
  "L_p8_8%=:\n\t" \
  "s_add_u32 m0, s66, 73728\n\t" \
  "s_nop 0\n\t" \
  "buffer_load_dwordx4 %[wvo0], s[36:39], s51 offen lds\n\t" \
  "ds_read_b64_tr_b16 v[228:229], v240 offset:36864\n\t" \
  "ds_read_b64_tr_b16 v[230:231], v240 offset:41472\n\t" \
  "s_waitcnt lgkmcnt(10)\n\t" \
  "v_mfma_f32_16x16x32_bf16 v[152:155], v[232:235], v[12:15], v[152:155]\n\t" \
  "v_mfma_f32_16x16x32_bf16 v[156:159], v[232:235], v[84:87], v[156:159]\n\t" \
  "ds_read_b64_tr_b16 v[232:233], v240 offset:36896\n\t" \
  "ds_read_b64_tr_b16 v[234:235], v240 offset:41504\n\t" \
  "s_waitcnt lgkmcnt(10)\n\t" \
  "v_mfma_f32_16x16x32_bf16 v[160:163], v[236:239], v[12:15], v[160:163]\n\t" \
  "v_mfma_f32_16x16x32_bf16 v[164:167], v[236:239], v[84:87], v[164:167]\n\t" \
  "s_mov_b32 s52, 4096\n\t" \
  "buffer_load_dwordx4 v[28:31], %[avo], s[40:43], s52 offen offset:3072\n\t" \
  "ds_read_b64_tr_b16 v[236:237], v240 offset:36928\n\t" \
  "ds_read_b64_tr_b16 v[238:239], v240 offset:41536\n\t" \
  "s_waitcnt lgkmcnt(10)\n\t" \
  "v_mfma_f32_16x16x32_bf16 v[168:171], v[216:219], v[12:15], v[168:171]\n\t" \
  "v_mfma_f32_16x16x32_bf16 v[172:175], v[216:219], v[84:87], v[172:175]\n\t" \
  "ds_read_b64_tr_b16 v[216:217], v240 offset:36960\n\t" \
  "ds_read_b64_tr_b16 v[218:219], v240 offset:41568\n\t" \
  "s_waitcnt lgkmcnt(10)\n\t" \
  "v_mfma_f32_16x16x32_bf16 v[176:179], v[220:223], v[12:15], v[176:179]\n\t" \
  "v_mfma_f32_16x16x32_bf16 v[180:183], v[220:223], v[84:87], v[180:183]\n\t" \
  "s_add_u32 s52, s63, 4096\n\t" \
  "buffer_load_dwordx4 v[100:103], %[avo], s[40:43], s52 offen offset:3072\n\t" \
  "ds_read_b64_tr_b16 v[220:221], v240 offset:36992\n\t" \
  "ds_read_b64_tr_b16 v[222:223], v240 offset:41600\n\t" \
  "s_waitcnt lgkmcnt(10)\n\t" \
  "v_mfma_f32_16x16x32_bf16 v[184:187], v[224:227], v[12:15], v[184:187]\n\t" \
  "v_mfma_f32_16x16x32_bf16 v[188:191], v[224:227], v[84:87], v[188:191]\n\t" \
  "ds_read_b64_tr_b16 v[224:225], v240 offset:37024\n\t" \
  "ds_read_b64_tr_b16 v[226:227], v240 offset:41632\n\t" \
  "s_waitcnt vmcnt(9)\n\t" \
  "s_waitcnt lgkmcnt(10)\n\t" \
  "v_mfma_f32_16x16x32_bf16 v[144:147], v[228:231], v[16:19], v[144:147]\n\t" \
  "v_mfma_f32_16x16x32_bf16 v[148:151], v[228:231], v[88:91], v[148:151]\n\t" \
  "s_mul_i32 s51, s68, 9\n\t" \
  "s_cmp_lg_u32 s60, 1\n\t" \
  "s_cbranch_scc1 L_p8_9%=\n\t" \
  "s_add_u32 m0, s67, 82944\n\t" \
  "s_nop 0\n\t" \
  "buffer_load_dwordx4 %[wvo8], s[36:39], s51 offen lds\n\t" \
  "L_p8_9%=:\n\t" \
  "s_add_u32 m0, s66, 82944\n\t" \
  "s_nop 0\n\t" \
  "buffer_load_dwordx4 %[wvo0], s[36:39], s51 offen lds\n\t" \
  "ds_read_b64_tr_b16 v[228:229], v240 offset:46080\n\t" \
  "ds_read_b64_tr_b16 v[230:231], v240 offset:50688\n\t" \
  "s_waitcnt lgkmcnt(10)\n\t" \
  "v_mfma_f32_16x16x32_bf16 v[152:155], v[232:235], v[16:19], v[152:155]\n\t" \
  "v_mfma_f32_16x16x32_bf16 v[156:159], v[232:235], v[88:91], v[156:159]\n\t" \
  "ds_read_b64_tr_b16 v[232:233], v240 offset:46112\n\t" \
  "ds_read_b64_tr_b16 v[234:235], v240 offset:50720\n\t" \
  "s_waitcnt lgkmcnt(10)\n\t" \
  "v_mfma_f32_16x16x32_bf16 v[160:163], v[236:239], v[16:19], v[160:163]\n\t" \
  "v_mfma_f32_16x16x32_bf16 v[164:167], v[236:239], v[88:91], v[164:167]\n\t" \
  "s_mov_b32 s52, 8192\n\t" \
  "buffer_load_dwordx4 v[32:35], %[avo], s[40:43], s52 offen offset:0\n\t" \
  "ds_read_b64_tr_b16 v[236:237], v240 offset:46144\n\t" \
  "ds_read_b64_tr_b16 v[238:239], v240 offset:50752\n\t" \
  "s_waitcnt lgkmcnt(10)\n\t" \
  "v_mfma_f32_16x16x32_bf16 v[168:171], v[216:219], v[16:19], v[168:171]\n\t" \
  "v_mfma_f32_16x16x32_bf16 v[172:175], v[216:219], v[88:91], v[172:175]\n\t" \
  "ds_read_b64_tr_b16 v[216:217], v240 offset:46176\n\t" \
  "ds_read_b64_tr_b16 v[218:219], v240 offset:50784\n\t" \
  "s_waitcnt lgkmcnt(10)\n\t" \
  "v_mfma_f32_16x16x32_bf16 v[176:179], v[220:223], v[16:19], v[176:179]\n\t" \
  "v_mfma_f32_16x16x32_bf16 v[180:183], v[220:223], v[88:91], v[180:183]\n\t" \
  "s_add_u32 s52, s63, 8192\n\t" \
  "buffer_load_dwordx4 v[104:107], %[avo], s[40:43], s52 offen offset:0\n\t" \
  "ds_read_b64_tr_b16 v[220:221], v240 offset:46208\n\t" \
  "ds_read_b64_tr_b16 v[222:223], v240 offset:50816\n\t" \
  "s_waitcnt lgkmcnt(10)\n\t" \
  "v_mfma_f32_16x16x32_bf16 v[184:187], v[224:227], v[16:19], v[184:187]\n\t" \
  "v_mfma_f32_16x16x32_bf16 v[188:191], v[224:227], v[88:91], v[188:191]\n\t" \
  "ds_read_b64_tr_b16 v[224:225], v240 offset:46240\n\t" \
  "ds_read_b64_tr_b16 v[226:227], v240 offset:50848\n\t" \
  "s_waitcnt vmcnt(9)\n\t" \
  "s_waitcnt lgkmcnt(10)\n\t" \
  "v_mfma_f32_16x16x32_bf16 v[144:147], v[228:231], v[20:23], v[144:147]\n\t" \
  "v_mfma_f32_16x16x32_bf16 v[148:151], v[228:231], v[92:95], v[148:151]\n\t" \
  "s_mul_i32 s51, s68, 10\n\t" \
  "s_cmp_lg_u32 s60, 2\n\t" \
  "s_cbranch_scc1 L_p8_10%=\n\t" \
  "s_add_u32 m0, s67, 92160\n\t" \
  "s_nop 0\n\t" \
  "buffer_load_dwordx4 %[wvo8], s[36:39], s51 offen lds\n\t" \
  "L_p8_10%=:\n\t" \
  "s_add_u32 m0, s66, 92160\n\t" \
  "s_nop 0\n\t" \
  "buffer_load_dwordx4 %[wvo0], s[36:39], s51 offen lds\n\t" \
  "s_waitcnt vmcnt(6)\n\t" \
  "s_barrier\n\t" \
  "ds_read_b64_tr_b16 v[228:229], v240 offset:55296\n\t" \
  "ds_read_b64_tr_b16 v[230:231], v240 offset:59904\n\t" \
  "s_waitcnt lgkmcnt(10)\n\t" \
  "v_mfma_f32_16x16x32_bf16 v[152:155], v[232:235], v[20:23], v[152:155]\n\t" \
  "v_mfma_f32_16x16x32_bf16 v[156:159], v[232:235], v[92:95], v[156:159]\n\t" \
  "ds_read_b64_tr_b16 v[232:233], v240 offset:55328\n\t" \
  "ds_read_b64_tr_b16 v[234:235], v240 offset:59936\n\t" \
  "s_waitcnt lgkmcnt(10)\n\t" \
  "v_mfma_f32_16x16x32_bf16 v[160:163], v[236:239], v[20:23], v[160:163]\n\t" \
  "v_mfma_f32_16x16x32_bf16 v[164:167], v[236:239], v[92:95], v[164:167]\n\t" \
  "s_mov_b32 s52, 8192\n\t" \
  "buffer_load_dwordx4 v[36:39], %[avo], s[40:43], s52 offen offset:1024\n\t" \
  "ds_read_b64_tr_b16 v[236:237], v240 offset:55360\n\t" \
  "ds_read_b64_tr_b16 v[238:239], v240 offset:59968\n\t" \
  "s_waitcnt lgkmcnt(10)\n\t" \
  "v_mfma_f32_16x16x32_bf16 v[168:171], v[216:219], v[20:23], v[168:171]\n\t" \
  "v_mfma_f32_16x16x32_bf16 v[172:175], v[216:219], v[92:95], v[172:175]\n\t" \
  "ds_read_b64_tr_b16 v[216:217], v240 offset:55392\n\t" \
  "ds_read_b64_tr_b16 v[218:219], v240 offset:60000\n\t" \
  "s_waitcnt lgkmcnt(10)\n\t" \
  "v_mfma_f32_16x16x32_bf16 v[176:179], v[220:223], v[20:23], v[176:179]\n\t" \
  "v_mfma_f32_16x16x32_bf16 v[180:183], v[220:223], v[92:95], v[180:183]\n\t" \
  "s_add_u32 s52, s63, 8192\n\t" \
  "buffer_load_dwordx4 v[108:111], %[avo], s[40:43], s52 offen offset:1024\n\t" \
  "ds_read_b64_tr_b16 v[220:221], v240 offset:55424\n\t" \
  "ds_read_b64_tr_b16 v[222:223], v240 offset:60032\n\t" \
  "s_waitcnt lgkmcnt(10)\n\t" \
  "v_mfma_f32_16x16x32_bf16 v[184:187], v[224:227], v[20:23], v[184:187]\n\t" \
  "v_mfma_f32_16x16x32_bf16 v[188:191], v[224:227], v[92:95], v[188:191]\n\t" \
  "ds_read_b64_tr_b16 v[224:225], v240 offset:55456\n\t" \
  "ds_read_b64_tr_b16 v[226:227], v240 offset:60064\n\t" \
  "s_waitcnt lgkmcnt(10)\n\t" \
  "v_mfma_f32_16x16x32_bf16 v[144:147], v[228:231], v[24:27], v[144:147]\n\t" \
  "v_mfma_f32_16x16x32_bf16 v[148:151], v[228:231], v[96:99], v[148:151]\n\t" \
  "s_mul_i32 s51, s68, 11\n\t" \
  "s_cmp_lg_u32 s60, 3\n\t" \
  "s_cbranch_scc1 L_p8_11%=\n\t" \
  "s_add_u32 m0, s67, 101376\n\t" \
  "s_nop 0\n\t" \
  "buffer_load_dwordx4 %[wvo8], s[36:39], s51 offen lds\n\t" \
  "L_p8_11%=:\n\t" \
  "s_add_u32 m0, s66, 101376\n\t" \
  "s_nop 0\n\t" \
  "buffer_load_dwordx4 %[wvo0], s[36:39], s51 offen lds\n\t" \
  "v_add_u32_e32 v240, 64512, v240\n\t" \
  "ds_read_b64_tr_b16 v[228:229], v240 offset:0\n\t" \
  "ds_read_b64_tr_b16 v[230:231], v240 offset:4608\n\t" \
  "s_waitcnt lgkmcnt(10)\n\t" \
  "v_mfma_f32_16x16x32_bf16 v[152:155], v[232:235], v[24:27], v[152:155]\n\t" \
  "v_mfma_f32_16x16x32_bf16 v[156:159], v[232:235], v[96:99], v[156:159]\n\t" \
  "ds_read_b64_tr_b16 v[232:233], v240 offset:32\n\t" \
  "ds_read_b64_tr_b16 v[234:235], v240 offset:4640\n\t" \
  "s_waitcnt lgkmcnt(10)\n\t" \
  "v_mfma_f32_16x16x32_bf16 v[160:163], v[236:239], v[24:27], v[160:163]\n\t" \
  "v_mfma_f32_16x16x32_bf16 v[164:167], v[236:239], v[96:99], v[164:167]\n\t" \
  "s_mov_b32 s52, 8192\n\t" \
  "buffer_load_dwordx4 v[40:43], %[avo], s[40:43], s52 offen offset:2048\n\t" \
  "ds_read_b64_tr_b16 v[236:237], v240 offset:64\n\t" \
  "ds_read_b64_tr_b16 v[238:239], v240 offset:4672\n\t" \
  "s_waitcnt lgkmcnt(10)\n\t" \
  "v_mfma_f32_16x16x32_bf16 v[168:171], v[216:219], v[24:27], v[168:171]\n\t" \
  "v_mfma_f32_16x16x32_bf16 v[172:175], v[216:219], v[96:99], v[172:175]\n\t" \
  "ds_read_b64_tr_b16 v[216:217], v240 offset:96\n\t" \
  "ds_read_b64_tr_b16 v[218:219], v240 offset:4704\n\t" \
  "s_waitcnt lgkmcnt(10)\n\t" \
  "v_mfma_f32_16x16x32_bf16 v[176:179], v[220:223], v[24:27], v[176:179]\n\t" \
  "v_mfma_f32_16x16x32_bf16 v[180:183], v[220:223], v[96:99], v[180:183]\n\t" \
  "s_add_u32 s52, s63, 8192\n\t" \
  "buffer_load_dwordx4 v[112:115], %[avo], s[40:43], s52 offen offset:2048\n\t" \
  "ds_read_b64_tr_b16 v[220:221], v240 offset:128\n\t" \
  "ds_read_b64_tr_b16 v[222:223], v240 offset:4736\n\t" \
  "s_waitcnt lgkmcnt(10)\n\t" \
  "v_mfma_f32_16x16x32_bf16 v[184:187], v[224:227], v[24:27], v[184:187]\n\t" \
  "v_mfma_f32_16x16x32_bf16 v[188:191], v[224:227], v[96:99], v[188:191]\n\t" \
  "ds_read_b64_tr_b16 v[224:225], v240 offset:160\n\t" \
  "ds_read_b64_tr_b16 v[226:227], v240 offset:4768\n\t" \
  "s_waitcnt vmcnt(9)\n\t" \
  "s_waitcnt lgkmcnt(10)\n\t" \
  "v_mfma_f32_16x16x32_bf16 v[144:147], v[228:231], v[28:31], v[144:147]\n\t" \
  "v_mfma_f32_16x16x32_bf16 v[148:151], v[228:231], v[100:103], v[148:151]\n\t" \
  "s_mul_i32 s51, s68, 12\n\t" \
  "s_cmp_lg_u32 s60, 4\n\t" \
  "s_cbranch_scc1 L_p8_12%=\n\t" \
  "s_add_u32 m0, s67, 110592\n\t" \
  "s_nop 0\n\t" \
  "buffer_load_dwordx4 %[wvo8], s[36:39], s51 offen lds\n\t" \
  "L_p8_12%=:\n\t" \
  "s_add_u32 m0, s66, 110592\n\t" \
  "s_nop 0\n\t" \
  "buffer_load_dwordx4 %[wvo0], s[36:39], s51 offen lds\n\t" \
  "ds_read_b64_tr_b16 v[228:229], v240 offset:9216\n\t" \
  "ds_read_b64_tr_b16 v[230:231], v240 offset:13824\n\t" \
  "s_waitcnt lgkmcnt(10)\n\t" \
  "v_mfma_f32_16x16x32_bf16 v[152:155], v[232:235], v[28:31], v[152:155]\n\t" \
  "v_mfma_f32_16x16x32_bf16 v[156:159], v[232:235], v[100:103], v[156:159]\n\t" \
  "ds_read_b64_tr_b16 v[232:233], v240 offset:9248\n\t" \
  "ds_read_b64_tr_b16 v[234:235], v240 offset:13856\n\t" \
  "s_waitcnt lgkmcnt(10)\n\t" \
  "v_mfma_f32_16x16x32_bf16 v[160:163], v[236:239], v[28:31], v[160:163]\n\t" \
  "v_mfma_f32_16x16x32_bf16 v[164:167], v[236:239], v[100:103], v[164:167]\n\t" \
  "s_mov_b32 s52, 8192\n\t" \
  "buffer_load_dwordx4 v[44:47], %[avo], s[40:43], s52 offen offset:3072\n\t" \
  "ds_read_b64_tr_b16 v[236:237], v240 offset:9280\n\t" \
  "ds_read_b64_tr_b16 v[238:239], v240 offset:13888\n\t" \
  "s_waitcnt lgkmcnt(10)\n\t" \
  "v_mfma_f32_16x16x32_bf16 v[168:171], v[216:219], v[28:31], v[168:171]\n\t" \
  "v_mfma_f32_16x16x32_bf16 v[172:175], v[216:219], v[100:103], v[172:175]\n\t" \
  "ds_read_b64_tr_b16 v[216:217], v240 offset:9312\n\t" \
  "ds_read_b64_tr_b16 v[218:219], v240 offset:13920\n\t" \
  "s_waitcnt lgkmcnt(10)\n\t" \
  "v_mfma_f32_16x16x32_bf16 v[176:179], v[220:223], v[28:31], v[176:179]\n\t" \
  "v_mfma_f32_16x16x32_bf16 v[180:183], v[220:223], v[100:103], v[180:183]\n\t" \
  "s_add_u32 s52, s63, 8192\n\t" \
  "buffer_load_dwordx4 v[116:119], %[avo], s[40:43], s52 offen offset:3072\n\t" \
  "ds_read_b64_tr_b16 v[220:221], v240 offset:9344\n\t" \
  "ds_read_b64_tr_b16 v[222:223], v240 offset:13952\n\t" \
  "s_waitcnt lgkmcnt(10)\n\t" \
  "v_mfma_f32_16x16x32_bf16 v[184:187], v[224:227], v[28:31], v[184:187]\n\t" \
  "v_mfma_f32_16x16x32_bf16 v[188:191], v[224:227], v[100:103], v[188:191]\n\t" \
  "ds_read_b64_tr_b16 v[224:225], v240 offset:9376\n\t" \
  "ds_read_b64_tr_b16 v[226:227], v240 offset:13984\n\t" \
  "s_waitcnt vmcnt(9)\n\t" \
  "s_waitcnt lgkmcnt(10)\n\t" \
  "v_mfma_f32_16x16x32_bf16 v[144:147], v[228:231], v[32:35], v[144:147]\n\t" \
  "v_mfma_f32_16x16x32_bf16 v[148:151], v[228:231], v[104:107], v[148:151]\n\t" \
  "s_mul_i32 s51, s68, 13\n\t" \
  "s_cmp_lg_u32 s60, 5\n\t" \
  "s_cbranch_scc1 L_p8_13%=\n\t" \
  "s_add_u32 m0, s67, 119808\n\t" \
  "s_nop 0\n\t" \
  "buffer_load_dwordx4 %[wvo8], s[36:39], s51 offen lds\n\t" \
  "L_p8_13%=:\n\t" \
  "s_add_u32 m0, s66, 119808\n\t" \
  "s_nop 0\n\t" \
  "buffer_load_dwordx4 %[wvo0], s[36:39], s51 offen lds\n\t" \
  "s_waitcnt vmcnt(6)\n\t" \
  "s_barrier\n\t" \
  "ds_read_b64_tr_b16 v[228:229], v240 offset:18432\n\t" \
  "ds_read_b64_tr_b16 v[230:231], v240 offset:23040\n\t" \
  "s_waitcnt lgkmcnt(10)\n\t" \
  "v_mfma_f32_16x16x32_bf16 v[152:155], v[232:235], v[32:35], v[152:155]\n\t" \
  "v_mfma_f32_16x16x32_bf16 v[156:159], v[232:235], v[104:107], v[156:159]\n\t" \
  "ds_read_b64_tr_b16 v[232:233], v240 offset:18464\n\t" \
  "ds_read_b64_tr_b16 v[234:235], v240 offset:23072\n\t" \
  "s_waitcnt lgkmcnt(10)\n\t" \
  "v_mfma_f32_16x16x32_bf16 v[160:163], v[236:239], v[32:35], v[160:163]\n\t" \
  "v_mfma_f32_16x16x32_bf16 v[164:167], v[236:239], v[104:107], v[164:167]\n\t" \
  "s_mov_b32 s52, 12288\n\t" \
  "buffer_load_dwordx4 v[48:51], %[avo], s[40:43], s52 offen offset:0\n\t" \
  "ds_read_b64_tr_b16 v[236:237], v240 offset:18496\n\t" \
  "ds_read_b64_tr_b16 v[238:239], v240 offset:23104\n\t" \
  "s_waitcnt lgkmcnt(10)\n\t" \
  "v_mfma_f32_16x16x32_bf16 v[168:171], v[216:219], v[32:35], v[168:171]\n\t" \
  "v_mfma_f32_16x16x32_bf16 v[172:175], v[216:219], v[104:107], v[172:175]\n\t" \
  "ds_read_b64_tr_b16 v[216:217], v240 offset:18528\n\t" \
  "ds_read_b64_tr_b16 v[218:219], v240 offset:23136\n\t" \
  "s_waitcnt lgkmcnt(10)\n\t" \
  "v_mfma_f32_16x16x32_bf16 v[176:179], v[220:223], v[32:35], v[176:179]\n\t" \
  "v_mfma_f32_16x16x32_bf16 v[180:183], v[220:223], v[104:107], v[180:183]\n\t" \
  "s_add_u32 s52, s63, 12288\n\t" \
  "buffer_load_dwordx4 v[120:123], %[avo], s[40:43], s52 offen offset:0\n\t" \
  "ds_read_b64_tr_b16 v[220:221], v240 offset:18560\n\t" \
  "ds_read_b64_tr_b16 v[222:223], v240 offset:23168\n\t" \
  "s_waitcnt lgkmcnt(10)\n\t" \
  "v_mfma_f32_16x16x32_bf16 v[184:187], v[224:227], v[32:35], v[184:187]\n\t" \
  "v_mfma_f32_16x16x32_bf16 v[188:191], v[224:227], v[104:107], v[188:191]\n\t" \
  "ds_read_b64_tr_b16 v[224:225], v240 offset:18592\n\t" \
  "ds_read_b64_tr_b16 v[226:227], v240 offset:23200\n\t" \
  "s_waitcnt lgkmcnt(10)\n\t" \
  "v_mfma_f32_16x16x32_bf16 v[144:147], v[228:231], v[36:39], v[144:147]\n\t" \
  "v_mfma_f32_16x16x32_bf16 v[148:151], v[228:231], v[108:111], v[148:151]\n\t" \
  "s_mul_i32 s51, s68, 14\n\t" \
  "s_cmp_lg_u32 s60, 6\n\t" \
  "s_cbranch_scc1 L_p8_14%=\n\t" \
  "s_add_u32 m0, s67, 129024\n\t" \
  "s_nop 0\n\t" \
  "buffer_load_dwordx4 %[wvo8], s[36:39], s51 offen lds\n\t" \
  "L_p8_14%=:\n\t" \
  "s_add_u32 m0, s66, 129024\n\t" \
  "s_nop 0\n\t" \
  "buffer_load_dwordx4 %[wvo0], s[36:39], s51 offen lds\n\t" \
  "ds_read_b64_tr_b16 v[228:229], v240 offset:27648\n\t" \
  "ds_read_b64_tr_b16 v[230:231], v240 offset:32256\n\t" \
  "s_waitcnt lgkmcnt(10)\n\t" \
  "v_mfma_f32_16x16x32_bf16 v[152:155], v[232:235], v[36:39], v[152:155]\n\t" \
  "v_mfma_f32_16x16x32_bf16 v[156:159], v[232:235], v[108:111], v[156:159]\n\t" \
  "ds_read_b64_tr_b16 v[232:233], v240 offset:27680\n\t" \
  "ds_read_b64_tr_b16 v[234:235], v240 offset:32288\n\t" \
  "s_waitcnt lgkmcnt(10)\n\t" \
  "v_mfma_f32_16x16x32_bf16 v[160:163], v[236:239], v[36:39], v[160:163]\n\t" \
  "v_mfma_f32_16x16x32_bf16 v[164:167], v[236:239], v[108:111], v[164:167]\n\t" \
  "s_mov_b32 s52, 12288\n\t" \
  "buffer_load_dwordx4 v[52:55], %[avo], s[40:43], s52 offen offset:1024\n\t" \
  "ds_read_b64_tr_b16 v[236:237], v240 offset:27712\n\t" \
  "ds_read_b64_tr_b16 v[238:239], v240 offset:32320\n\t" \
  "s_waitcnt lgkmcnt(10)\n\t" \
  "v_mfma_f32_16x16x32_bf16 v[168:171], v[216:219], v[36:39], v[168:171]\n\t" \
  "v_mfma_f32_16x16x32_bf16 v[172:175], v[216:219], v[108:111], v[172:175]\n\t" \
  "ds_read_b64_tr_b16 v[216:217], v240 offset:27744\n\t" \
  "ds_read_b64_tr_b16 v[218:219], v240 offset:32352\n\t" \
  "s_waitcnt lgkmcnt(10)\n\t" \
  "v_mfma_f32_16x16x32_bf16 v[176:179], v[220:223], v[36:39], v[176:179]\n\t" \
  "v_mfma_f32_16x16x32_bf16 v[180:183], v[220:223], v[108:111], v[180:183]\n\t" \
  "s_add_u32 s52, s63, 12288\n\t" \
  "buffer_load_dwordx4 v[124:127], %[avo], s[40:43], s52 offen offset:1024\n\t" \
  "ds_read_b64_tr_b16 v[220:221], v240 offset:27776\n\t" \
  "ds_read_b64_tr_b16 v[222:223], v240 offset:32384\n\t" \
  "s_waitcnt lgkmcnt(10)\n\t" \
  "v_mfma_f32_16x16x32_bf16 v[184:187], v[224:227], v[36:39], v[184:187]\n\t" \
  "v_mfma_f32_16x16x32_bf16 v[188:191], v[224:227], v[108:111], v[188:191]\n\t" \
  "ds_read_b64_tr_b16 v[224:225], v240 offset:27808\n\t" \
  "ds_read_b64_tr_b16 v[226:227], v240 offset:32416\n\t" \
  "s_waitcnt vmcnt(9)\n\t" \
  "s_waitcnt lgkmcnt(10)\n\t" \
  "v_mfma_f32_16x16x32_bf16 v[144:147], v[228:231], v[40:43], v[144:147]\n\t" \
  "v_mfma_f32_16x16x32_bf16 v[148:151], v[228:231], v[112:115], v[148:151]\n\t" \
  "s_mul_i32 s51, s68, 15\n\t" \
  "s_cmp_lg_u32 s60, 7\n\t" \
  "s_cbranch_scc1 L_p8_15%=\n\t" \
  "s_add_u32 m0, s67, 138240\n\t" \
  "s_nop 0\n\t" \
  "buffer_load_dwordx4 %[wvo8], s[36:39], s51 offen lds\n\t" \
  "L_p8_15%=:\n\t" \
  "s_add_u32 m0, s66, 138240\n\t" \
  "s_nop 0\n\t" \
  "buffer_load_dwordx4 %[wvo0], s[36:39], s51 offen lds\n\t" \
  "ds_read_b64_tr_b16 v[228:229], v240 offset:36864\n\t" \
  "ds_read_b64_tr_b16 v[230:231], v240 offset:41472\n\t" \
  "s_waitcnt lgkmcnt(10)\n\t" \
  "v_mfma_f32_16x16x32_bf16 v[152:155], v[232:235], v[40:43], v[152:155]\n\t" \
  "v_mfma_f32_16x16x32_bf16 v[156:159], v[232:235], v[112:115], v[156:159]\n\t" \
  "ds_read_b64_tr_b16 v[232:233], v240 offset:36896\n\t" \
  "ds_read_b64_tr_b16 v[234:235], v240 offset:41504\n\t" \
  "s_waitcnt lgkmcnt(10)\n\t" \
  "v_mfma_f32_16x16x32_bf16 v[160:163], v[236:239], v[40:43], v[160:163]\n\t" \
  "v_mfma_f32_16x16x32_bf16 v[164:167], v[236:239], v[112:115], v[164:167]\n\t" \
  "s_mov_b32 s52, 12288\n\t" \
  "buffer_load_dwordx4 v[56:59], %[avo], s[40:43], s52 offen offset:2048\n\t" \
  "ds_read_b64_tr_b16 v[236:237], v240 offset:36928\n\t" \
  "ds_read_b64_tr_b16 v[238:239], v240 offset:41536\n\t" \
  "s_waitcnt lgkmcnt(10)\n\t" \
  "v_mfma_f32_16x16x32_bf16 v[168:171], v[216:219], v[40:43], v[168:171]\n\t" \
  "v_mfma_f32_16x16x32_bf16 v[172:175], v[216:219], v[112:115], v[172:175]\n\t" \
  "ds_read_b64_tr_b16 v[216:217], v240 offset:36960\n\t" \
  "ds_read_b64_tr_b16 v[218:219], v240 offset:41568\n\t" \
  "s_waitcnt lgkmcnt(10)\n\t" \
  "v_mfma_f32_16x16x32_bf16 v[176:179], v[220:223], v[40:43], v[176:179]\n\t" \
  "v_mfma_f32_16x16x32_bf16 v[180:183], v[220:223], v[112:115], v[180:183]\n\t" \
  "s_add_u32 s52, s63, 12288\n\t" \
  "buffer_load_dwordx4 v[128:131], %[avo], s[40:43], s52 offen offset:2048\n\t" \
  "ds_read_b64_tr_b16 v[220:221], v240 offset:36992\n\t" \
  "ds_read_b64_tr_b16 v[222:223], v240 offset:41600\n\t" \
  "s_waitcnt lgkmcnt(10)\n\t" \
  "v_mfma_f32_16x16x32_bf16 v[184:187], v[224:227], v[40:43], v[184:187]\n\t" \
  "v_mfma_f32_16x16x32_bf16 v[188:191], v[224:227], v[112:115], v[188:191]\n\t" \
  "ds_read_b64_tr_b16 v[224:225], v240 offset:37024\n\t" \
  "ds_read_b64_tr_b16 v[226:227], v240 offset:41632\n\t" \
  "s_waitcnt vmcnt(9)\n\t" \
  "s_waitcnt lgkmcnt(10)\n\t" \
  "v_mfma_f32_16x16x32_bf16 v[144:147], v[228:231], v[44:47], v[144:147]\n\t" \
  "v_mfma_f32_16x16x32_bf16 v[148:151], v[228:231], v[116:119], v[148:151]\n\t" \
  "s_mul_i32 s51, s68, 16\n\t" \
  "s_cmp_lg_u32 s60, 0\n\t" \
  "s_cbranch_scc1 L_p8_16%=\n\t" \
  "s_add_u32 m0, s67, 147456\n\t" \
  "s_nop 0\n\t" \
  "buffer_load_dwordx4 %[wvo8], s[36:39], s51 offen lds\n\t" \
  "L_p8_16%=:\n\t" \
  "s_add_u32 m0, s66, 147456\n\t" \
  "s_nop 0\n\t" \
  "buffer_load_dwordx4 %[wvo0], s[36:39], s51 offen lds\n\t" \
  "s_waitcnt vmcnt(6)\n\t" \
  "s_barrier\n\t" \
  "ds_read_b64_tr_b16 v[228:229], v240 offset:46080\n\t" \
  "ds_read_b64_tr_b16 v[230:231], v240 offset:50688\n\t" \
  "s_waitcnt lgkmcnt(10)\n\t" \
  "v_mfma_f32_16x16x32_bf16 v[152:155], v[232:235], v[44:47], v[152:155]\n\t" \
  "v_mfma_f32_16x16x32_bf16 v[156:159], v[232:235], v[116:119], v[156:159]\n\t" \
  "ds_read_b64_tr_b16 v[232:233], v240 offset:46112\n\t" \
  "ds_read_b64_tr_b16 v[234:235], v240 offset:50720\n\t" \
  "s_waitcnt lgkmcnt(10)\n\t" \
  "v_mfma_f32_16x16x32_bf16 v[160:163], v[236:239], v[44:47], v[160:163]\n\t" \
  "v_mfma_f32_16x16x32_bf16 v[164:167], v[236:239], v[116:119], v[164:167]\n\t" \
  "s_mov_b32 s52, 12288\n\t" \
  "buffer_load_dwordx4 v[60:63], %[avo], s[40:43], s52 offen offset:3072\n\t" \
  "ds_read_b64_tr_b16 v[236:237], v240 offset:46144\n\t" \
  "ds_read_b64_tr_b16 v[238:239], v240 offset:50752\n\t" \
  "s_waitcnt lgkmcnt(10)\n\t" \
  "v_mfma_f32_16x16x32_bf16 v[168:171], v[216:219], v[44:47], v[168:171]\n\t" \
  "v_mfma_f32_16x16x32_bf16 v[172:175], v[216:219], v[116:119], v[172:175]\n\t" \
  "ds_read_b64_tr_b16 v[216:217], v240 offset:46176\n\t" \
  "ds_read_b64_tr_b16 v[218:219], v240 offset:50784\n\t" \
  "s_waitcnt lgkmcnt(10)\n\t" \
  "v_mfma_f32_16x16x32_bf16 v[176:179], v[220:223], v[44:47], v[176:179]\n\t" \
  "v_mfma_f32_16x16x32_bf16 v[180:183], v[220:223], v[116:119], v[180:183]\n\t" \
  "s_add_u32 s52, s63, 12288\n\t" \
  "buffer_load_dwordx4 v[132:135], %[avo], s[40:43], s52 offen offset:3072\n\t" \
  "ds_read_b64_tr_b16 v[220:221], v240 offset:46208\n\t" \
  "ds_read_b64_tr_b16 v[222:223], v240 offset:50816\n\t" \
  "s_waitcnt lgkmcnt(10)\n\t" \
  "v_mfma_f32_16x16x32_bf16 v[184:187], v[224:227], v[44:47], v[184:187]\n\t" \
  "v_mfma_f32_16x16x32_bf16 v[188:191], v[224:227], v[116:119], v[188:191]\n\t" \
  "ds_read_b64_tr_b16 v[224:225], v240 offset:46240\n\t" \
  "ds_read_b64_tr_b16 v[226:227], v240 offset:50848\n\t" \
  "s_waitcnt lgkmcnt(10)\n\t" \
  "v_mfma_f32_16x16x32_bf16 v[144:147], v[228:231], v[48:51], v[144:147]\n\t" \
  "v_mfma_f32_16x16x32_bf16 v[148:151], v[228:231], v[120:123], v[148:151]\n\t" \
  "s_mul_i32 s51, s68, 17\n\t" \
  "s_cmp_lg_u32 s60, 1\n\t" \
  "s_cbranch_scc1 L_p8_17%=\n\t" \
  "s_add_u32 m0, s67, 0\n\t" \
  "s_nop 0\n\t" \
  "buffer_load_dwordx4 %[wvo8], s[36:39], s51 offen lds\n\t" \
  "L_p8_17%=:\n\t" \
  "s_add_u32 m0, s66, 0\n\t" \
  "s_nop 0\n\t" \
  "buffer_load_dwordx4 %[wvo0], s[36:39], s51 offen lds\n\t" \
  "ds_read_b64_tr_b16 v[228:229], v240 offset:55296\n\t" \
  "ds_read_b64_tr_b16 v[230:231], v240 offset:59904\n\t" \
  "s_waitcnt lgkmcnt(10)\n\t" \
  "v_mfma_f32_16x16x32_bf16 v[152:155], v[232:235], v[48:51], v[152:155]\n\t" \
  "v_mfma_f32_16x16x32_bf16 v[156:159], v[232:235], v[120:123], v[156:159]\n\t" \
  "ds_read_b64_tr_b16 v[232:233], v240 offset:55328\n\t" \
  "ds_read_b64_tr_b16 v[234:235], v240 offset:59936\n\t" \
  "s_waitcnt lgkmcnt(10)\n\t" \
  "v_mfma_f32_16x16x32_bf16 v[160:163], v[236:239], v[48:51], v[160:163]\n\t" \
  "v_mfma_f32_16x16x32_bf16 v[164:167], v[236:239], v[120:123], v[164:167]\n\t" \
  "s_mov_b32 s52, 16384\n\t" \
  "buffer_load_dwordx4 v[64:67], %[avo], s[40:43], s52 offen offset:0\n\t" \
  "ds_read_b64_tr_b16 v[236:237], v240 offset:55360\n\t" \
  "ds_read_b64_tr_b16 v[238:239], v240 offset:59968\n\t" \
  "s_waitcnt lgkmcnt(10)\n\t" \
  "v_mfma_f32_16x16x32_bf16 v[168:171], v[216:219], v[48:51], v[168:171]\n\t" \
  "v_mfma_f32_16x16x32_bf16 v[172:175], v[216:219], v[120:123], v[172:175]\n\t" \
  "ds_read_b64_tr_b16 v[216:217], v240 offset:55392\n\t" \
  "ds_read_b64_tr_b16 v[218:219], v240 offset:60000\n\t" \
  "s_waitcnt lgkmcnt(10)\n\t" \
  "v_mfma_f32_16x16x32_bf16 v[176:179], v[220:223], v[48:51], v[176:179]\n\t" \
  "v_mfma_f32_16x16x32_bf16 v[180:183], v[220:223], v[120:123], v[180:183]\n\t" \
  "s_add_u32 s52, s63, 16384\n\t" \
  "buffer_load_dwordx4 v[136:139], %[avo], s[40:43], s52 offen offset:0\n\t" \
  "ds_read_b64_tr_b16 v[220:221], v240 offset:55424\n\t" \
  "ds_read_b64_tr_b16 v[222:223], v240 offset:60032\n\t" \
  "s_waitcnt lgkmcnt(10)\n\t" \
  "v_mfma_f32_16x16x32_bf16 v[184:187], v[224:227], v[48:51], v[184:187]\n\t" \
  "v_mfma_f32_16x16x32_bf16 v[188:191], v[224:227], v[120:123], v[188:191]\n\t" \
  "ds_read_b64_tr_b16 v[224:225], v240 offset:55456\n\t" \
  "ds_read_b64_tr_b16 v[226:227], v240 offset:60064\n\t" \
  "s_waitcnt vmcnt(9)\n\t" \
  "s_waitcnt lgkmcnt(10)\n\t" \
  "v_mfma_f32_16x16x32_bf16 v[144:147], v[228:231], v[52:55], v[144:147]\n\t" \
  "v_mfma_f32_16x16x32_bf16 v[148:151], v[228:231], v[124:127], v[148:151]\n\t" \
  "s_mov_b32 s52, 16384\n\t" \
  "buffer_load_dwordx4 v[68:71], %[avo], s[40:43], s52 offen offset:1024\n\t" \
  "v_add_u32_e32 v240, 64512, v240\n\t" \
  "ds_read_b64_tr_b16 v[228:229], v240 offset:0\n\t" \
  "ds_read_b64_tr_b16 v[230:231], v240 offset:4608\n\t" \
  "s_waitcnt lgkmcnt(10)\n\t" \
  "v_mfma_f32_16x16x32_bf16 v[152:155], v[232:235], v[52:55], v[152:155]\n\t" \
  "v_mfma_f32_16x16x32_bf16 v[156:159], v[232:235], v[124:127], v[156:159]\n\t" \
  "ds_read_b64_tr_b16 v[232:233], v240 offset:32\n\t" \
  "ds_read_b64_tr_b16 v[234:235], v240 offset:4640\n\t" \
  "s_waitcnt lgkmcnt(10)\n\t" \
  "v_mfma_f32_16x16x32_bf16 v[160:163], v[236:239], v[52:55], v[160:163]\n\t" \
  "v_mfma_f32_16x16x32_bf16 v[164:167], v[236:239], v[124:127], v[164:167]\n\t" \
  "s_add_u32 s52, s63, 16384\n\t" \
  "buffer_load_dwordx4 v[140:143], %[avo], s[40:43], s52 offen offset:1024\n\t" \
  "ds_read_b64_tr_b16 v[236:237], v240 offset:64\n\t" \
  "ds_read_b64_tr_b16 v[238:239], v240 offset:4672\n\t" \
  "s_waitcnt lgkmcnt(10)\n\t" \
  "v_mfma_f32_16x16x32_bf16 v[168:171], v[216:219], v[52:55], v[168:171]\n\t" \
  "v_mfma_f32_16x16x32_bf16 v[172:175], v[216:219], v[124:127], v[172:175]\n\t" \
  "ds_read_b64_tr_b16 v[216:217], v240 offset:96\n\t" \
  "ds_read_b64_tr_b16 v[218:219], v240 offset:4704\n\t" \
  "s_waitcnt lgkmcnt(10)\n\t" \
  "v_mfma_f32_16x16x32_bf16 v[176:179], v[220:223], v[52:55], v[176:179]\n\t" \
  "v_mfma_f32_16x16x32_bf16 v[180:183], v[220:223], v[124:127], v[180:183]\n\t" \
  "ds_read_b64_tr_b16 v[220:221], v240 offset:128\n\t" \
  "ds_read_b64_tr_b16 v[222:223], v240 offset:4736\n\t" \
  "s_waitcnt lgkmcnt(10)\n\t" \
  "v_mfma_f32_16x16x32_bf16 v[184:187], v[224:227], v[52:55], v[184:187]\n\t" \
  "v_mfma_f32_16x16x32_bf16 v[188:191], v[224:227], v[124:127], v[188:191]\n\t" \
  "ds_read_b64_tr_b16 v[224:225], v240 offset:160\n\t" \
  "ds_read_b64_tr_b16 v[226:227], v240 offset:4768\n\t" \
  "s_waitcnt vmcnt(8)\n\t" \
  "s_waitcnt lgkmcnt(10)\n\t" \
  "v_mfma_f32_16x16x32_bf16 v[144:147], v[228:231], v[56:59], v[144:147]\n\t" \
  "v_mfma_f32_16x16x32_bf16 v[148:151], v[228:231], v[128:131], v[148:151]\n\t" \
  "s_waitcnt vmcnt(4)\n\t" \
  "s_barrier\n\t" \
  "ds_read_b64_tr_b16 v[228:229], v240 offset:9216\n\t" \
  "ds_read_b64_tr_b16 v[230:231], v240 offset:13824\n\t" \
  "s_waitcnt lgkmcnt(10)\n\t" \
  "v_mfma_f32_16x16x32_bf16 v[152:155], v[232:235], v[56:59], v[152:155]\n\t" \
  "v_mfma_f32_16x16x32_bf16 v[156:159], v[232:235], v[128:131], v[156:159]\n\t" \
  "ds_read_b64_tr_b16 v[232:233], v240 offset:9248\n\t" \
  "ds_read_b64_tr_b16 v[234:235], v240 offset:13856\n\t" \
  "s_waitcnt lgkmcnt(10)\n\t" \
  "v_mfma_f32_16x16x32_bf16 v[160:163], v[236:239], v[56:59], v[160:163]\n\t" \
  "v_mfma_f32_16x16x32_bf16 v[164:167], v[236:239], v[128:131], v[164:167]\n\t" \
  "ds_read_b64_tr_b16 v[236:237], v240 offset:9280\n\t" \
  "ds_read_b64_tr_b16 v[238:239], v240 offset:13888\n\t" \
  "s_waitcnt lgkmcnt(10)\n\t" \
  "v_mfma_f32_16x16x32_bf16 v[168:171], v[216:219], v[56:59], v[168:171]\n\t" \
  "v_mfma_f32_16x16x32_bf16 v[172:175], v[216:219], v[128:131], v[172:175]\n\t" \
  "ds_read_b64_tr_b16 v[216:217], v240 offset:9312\n\t" \
  "ds_read_b64_tr_b16 v[218:219], v240 offset:13920\n\t" \
  "s_waitcnt lgkmcnt(10)\n\t" \
  "v_mfma_f32_16x16x32_bf16 v[176:179], v[220:223], v[56:59], v[176:179]\n\t" \
  "v_mfma_f32_16x16x32_bf16 v[180:183], v[220:223], v[128:131], v[180:183]\n\t" \
  "ds_read_b64_tr_b16 v[220:221], v240 offset:9344\n\t" \
  "ds_read_b64_tr_b16 v[222:223], v240 offset:13952\n\t" \
  "s_waitcnt lgkmcnt(10)\n\t" \
  "v_mfma_f32_16x16x32_bf16 v[184:187], v[224:227], v[56:59], v[184:187]\n\t" \
  "v_mfma_f32_16x16x32_bf16 v[188:191], v[224:227], v[128:131], v[188:191]\n\t" \
  "ds_read_b64_tr_b16 v[224:225], v240 offset:9376\n\t" \
  "ds_read_b64_tr_b16 v[226:227], v240 offset:13984\n\t" \
  "s_waitcnt lgkmcnt(10)\n\t" \
  "v_mfma_f32_16x16x32_bf16 v[144:147], v[228:231], v[60:63], v[144:147]\n\t" \
  "v_mfma_f32_16x16x32_bf16 v[148:151], v[228:231], v[132:135], v[148:151]\n\t" \
  "ds_read_b64_tr_b16 v[228:229], v240 offset:18432\n\t" \
  "ds_read_b64_tr_b16 v[230:231], v240 offset:23040\n\t" \
  "s_waitcnt lgkmcnt(10)\n\t" \
  "v_mfma_f32_16x16x32_bf16 v[152:155], v[232:235], v[60:63], v[152:155]\n\t" \
  "v_mfma_f32_16x16x32_bf16 v[156:159], v[232:235], v[132:135], v[156:159]\n\t" \
  "ds_read_b64_tr_b16 v[232:233], v240 offset:18464\n\t" \
  "ds_read_b64_tr_b16 v[234:235], v240 offset:23072\n\t" \
  "s_waitcnt lgkmcnt(10)\n\t" \
  "v_mfma_f32_16x16x32_bf16 v[160:163], v[236:239], v[60:63], v[160:163]\n\t" \
  "v_mfma_f32_16x16x32_bf16 v[164:167], v[236:239], v[132:135], v[164:167]\n\t" \
  "ds_read_b64_tr_b16 v[236:237], v240 offset:18496\n\t" \
  "ds_read_b64_tr_b16 v[238:239], v240 offset:23104\n\t" \
  "s_waitcnt lgkmcnt(10)\n\t" \
  "v_mfma_f32_16x16x32_bf16 v[168:171], v[216:219], v[60:63], v[168:171]\n\t" \
  "v_mfma_f32_16x16x32_bf16 v[172:175], v[216:219], v[132:135], v[172:175]\n\t" \
  "ds_read_b64_tr_b16 v[216:217], v240 offset:18528\n\t" \
  "ds_read_b64_tr_b16 v[218:219], v240 offset:23136\n\t" \
  "s_waitcnt lgkmcnt(10)\n\t" \
  "v_mfma_f32_16x16x32_bf16 v[176:179], v[220:223], v[60:63], v[176:179]\n\t" \
  "v_mfma_f32_16x16x32_bf16 v[180:183], v[220:223], v[132:135], v[180:183]\n\t" \
  "ds_read_b64_tr_b16 v[220:221], v240 offset:18560\n\t" \
  "ds_read_b64_tr_b16 v[222:223], v240 offset:23168\n\t" \
  "s_waitcnt lgkmcnt(10)\n\t" \
  "v_mfma_f32_16x16x32_bf16 v[184:187], v[224:227], v[60:63], v[184:187]\n\t" \
  "v_mfma_f32_16x16x32_bf16 v[188:191], v[224:227], v[132:135], v[188:191]\n\t" \
  "ds_read_b64_tr_b16 v[224:225], v240 offset:18592\n\t" \
  "ds_read_b64_tr_b16 v[226:227], v240 offset:23200\n\t" \
  "s_waitcnt vmcnt(2)\n\t" \
  "s_waitcnt lgkmcnt(10)\n\t" \
  "v_mfma_f32_16x16x32_bf16 v[144:147], v[228:231], v[64:67], v[144:147]\n\t" \
  "v_mfma_f32_16x16x32_bf16 v[148:151], v[228:231], v[136:139], v[148:151]\n\t" \
  "v_add_u32_e32 v240, 4294838272, v240\n\t" \
  "ds_read_b64_tr_b16 v[228:229], v240 offset:0\n\t" \
  "ds_read_b64_tr_b16 v[230:231], v240 offset:4608\n\t" \
  "s_waitcnt lgkmcnt(10)\n\t" \
  "v_mfma_f32_16x16x32_bf16 v[152:155], v[232:235], v[64:67], v[152:155]\n\t" \
  "v_mfma_f32_16x16x32_bf16 v[156:159], v[232:235], v[136:139], v[156:159]\n\t" \
  "ds_read_b64_tr_b16 v[232:233], v240 offset:32\n\t" \
  "ds_read_b64_tr_b16 v[234:235], v240 offset:4640\n\t" \
  "s_waitcnt lgkmcnt(10)\n\t" \
  "v_mfma_f32_16x16x32_bf16 v[160:163], v[236:239], v[64:67], v[160:163]\n\t" \
  "v_mfma_f32_16x16x32_bf16 v[164:167], v[236:239], v[136:139], v[164:167]\n\t" \
  "ds_read_b64_tr_b16 v[236:237], v240 offset:64\n\t" \
  "ds_read_b64_tr_b16 v[238:239], v240 offset:4672\n\t" \
  "s_waitcnt lgkmcnt(10)\n\t" \
  "v_mfma_f32_16x16x32_bf16 v[168:171], v[216:219], v[64:67], v[168:171]\n\t" \
  "v_mfma_f32_16x16x32_bf16 v[172:175], v[216:219], v[136:139], v[172:175]\n\t" \
  "ds_read_b64_tr_b16 v[216:217], v240 offset:96\n\t" \
  "ds_read_b64_tr_b16 v[218:219], v240 offset:4704\n\t" \
  "s_waitcnt lgkmcnt(10)\n\t" \
  "v_mfma_f32_16x16x32_bf16 v[176:179], v[220:223], v[64:67], v[176:179]\n\t" \
  "v_mfma_f32_16x16x32_bf16 v[180:183], v[220:223], v[136:139], v[180:183]\n\t" \
  "ds_read_b64_tr_b16 v[220:221], v240 offset:128\n\t" \
  "ds_read_b64_tr_b16 v[222:223], v240 offset:4736\n\t" \
  "s_waitcnt lgkmcnt(10)\n\t" \
  "v_mfma_f32_16x16x32_bf16 v[184:187], v[224:227], v[64:67], v[184:187]\n\t" \
  "v_mfma_f32_16x16x32_bf16 v[188:191], v[224:227], v[136:139], v[188:191]\n\t" \
  "ds_read_b64_tr_b16 v[224:225], v240 offset:160\n\t" \
  "ds_read_b64_tr_b16 v[226:227], v240 offset:4768\n\t" \
  "s_waitcnt vmcnt(0)\n\t" \
  "s_waitcnt lgkmcnt(10)\n\t" \
  "v_mfma_f32_16x16x32_bf16 v[144:147], v[228:231], v[68:71], v[144:147]\n\t" \
  "v_mfma_f32_16x16x32_bf16 v[148:151], v[228:231], v[140:143], v[148:151]\n\t" \
  "ds_read_b64_tr_b16 v[228:229], v240 offset:9408\n\t" \
  "ds_read_b64_tr_b16 v[230:231], v240 offset:14016\n\t" \
  "s_waitcnt lgkmcnt(10)\n\t" \
  "v_mfma_f32_16x16x32_bf16 v[152:155], v[232:235], v[68:71], v[152:155]\n\t" \
  "v_mfma_f32_16x16x32_bf16 v[156:159], v[232:235], v[140:143], v[156:159]\n\t" \
  "ds_read_b64_tr_b16 v[232:233], v240 offset:9440\n\t" \
  "ds_read_b64_tr_b16 v[234:235], v240 offset:14048\n\t" \
  "s_waitcnt lgkmcnt(10)\n\t" \
  "v_mfma_f32_16x16x32_bf16 v[160:163], v[236:239], v[68:71], v[160:163]\n\t" \
  "v_mfma_f32_16x16x32_bf16 v[164:167], v[236:239], v[140:143], v[164:167]\n\t" \
  "ds_read_b64_tr_b16 v[236:237], v241 offset:9216\n\t" \
  "ds_read_b64_tr_b16 v[238:239], v241 offset:13824\n\t" \
  "s_waitcnt lgkmcnt(10)\n\t" \
  "v_mfma_f32_16x16x32_bf16 v[168:171], v[216:219], v[68:71], v[168:171]\n\t" \
  "v_mfma_f32_16x16x32_bf16 v[172:175], v[216:219], v[140:143], v[172:175]\n\t" \
  "ds_read_b64_tr_b16 v[216:217], v240 offset:18624\n\t" \
  "ds_read_b64_tr_b16 v[218:219], v240 offset:23232\n\t" \
  "s_waitcnt lgkmcnt(10)\n\t" \
  "v_mfma_f32_16x16x32_bf16 v[176:179], v[220:223], v[68:71], v[176:179]\n\t" \
  "v_mfma_f32_16x16x32_bf16 v[180:183], v[220:223], v[140:143], v[180:183]\n\t" \
  "ds_read_b64_tr_b16 v[220:221], v240 offset:18656\n\t" \
  "ds_read_b64_tr_b16 v[222:223], v240 offset:23264\n\t" \
  "s_waitcnt lgkmcnt(10)\n\t" \
  "v_mfma_f32_16x16x32_bf16 v[184:187], v[224:227], v[68:71], v[184:187]\n\t" \
  "v_mfma_f32_16x16x32_bf16 v[188:191], v[224:227], v[140:143], v[188:191]\n\t" \
  "ds_read_b64_tr_b16 v[224:225], v241 offset:18432\n\t" \
  "ds_read_b64_tr_b16 v[226:227], v241 offset:23040\n\t" \
  "s_waitcnt lgkmcnt(10)\n\t" \
  "v_mfma_f32_16x16x32_bf16 v[192:195], v[228:231], v[4:7], v[192:195]\n\t" \
  "v_mfma_f32_16x16x32_bf16 v[196:199], v[228:231], v[76:79], v[196:199]\n\t" \
  "ds_read_b64_tr_b16 v[228:229], v240 offset:27840\n\t" \
  "ds_read_b64_tr_b16 v[230:231], v240 offset:32448\n\t" \
  "s_waitcnt lgkmcnt(10)\n\t" \
  "v_mfma_f32_16x16x32_bf16 v[200:203], v[232:235], v[4:7], v[200:203]\n\t" \
  "v_mfma_f32_16x16x32_bf16 v[204:207], v[232:235], v[76:79], v[204:207]\n\t" \
  "ds_read_b64_tr_b16 v[232:233], v240 offset:27872\n\t" \
  "ds_read_b64_tr_b16 v[234:235], v240 offset:32480\n\t" \
  "s_waitcnt lgkmcnt(10)\n\t" \
  "v_mfma_f32_16x16x32_bf16 v[208:211], v[236:239], v[4:7], v[208:211]\n\t" \
  "v_mfma_f32_16x16x32_bf16 v[212:215], v[236:239], v[76:79], v[212:215]\n\t" \
  "ds_read_b64_tr_b16 v[236:237], v241 offset:27648\n\t" \
  "ds_read_b64_tr_b16 v[238:239], v241 offset:32256\n\t" \
  "s_waitcnt lgkmcnt(10)\n\t" \
  "v_mfma_f32_16x16x32_bf16 v[192:195], v[216:219], v[8:11], v[192:195]\n\t" \
  "v_mfma_f32_16x16x32_bf16 v[196:199], v[216:219], v[80:83], v[196:199]\n\t" \
  "ds_read_b64_tr_b16 v[216:217], v240 offset:37056\n\t" \
  "ds_read_b64_tr_b16 v[218:219], v240 offset:41664\n\t" \
  "s_waitcnt lgkmcnt(10)\n\t" \
  "v_mfma_f32_16x16x32_bf16 v[200:203], v[220:223], v[8:11], v[200:203]\n\t" \
  "v_mfma_f32_16x16x32_bf16 v[204:207], v[220:223], v[80:83], v[204:207]\n\t" \
  "ds_read_b64_tr_b16 v[220:221], v240 offset:37088\n\t" \
  "ds_read_b64_tr_b16 v[222:223], v240 offset:41696\n\t" \
  "s_waitcnt lgkmcnt(10)\n\t" \
  "v_mfma_f32_16x16x32_bf16 v[208:211], v[224:227], v[8:11], v[208:211]\n\t" \
  "v_mfma_f32_16x16x32_bf16 v[212:215], v[224:227], v[80:83], v[212:215]\n\t" \
  "ds_read_b64_tr_b16 v[224:225], v241 offset:36864\n\t" \
  "ds_read_b64_tr_b16 v[226:227], v241 offset:41472\n\t" \
  "s_waitcnt lgkmcnt(10)\n\t" \
  "v_mfma_f32_16x16x32_bf16 v[192:195], v[228:231], v[12:15], v[192:195]\n\t" \
  "v_mfma_f32_16x16x32_bf16 v[196:199], v[228:231], v[84:87], v[196:199]\n\t" \
  "ds_read_b64_tr_b16 v[228:229], v240 offset:46272\n\t" \
  "ds_read_b64_tr_b16 v[230:231], v240 offset:50880\n\t" \
  "s_waitcnt lgkmcnt(10)\n\t" \
  "v_mfma_f32_16x16x32_bf16 v[200:203], v[232:235], v[12:15], v[200:203]\n\t" \
  "v_mfma_f32_16x16x32_bf16 v[204:207], v[232:235], v[84:87], v[204:207]\n\t" \
  "ds_read_b64_tr_b16 v[232:233], v240 offset:46304\n\t" \
  "ds_read_b64_tr_b16 v[234:235], v240 offset:50912\n\t" \
  "v_cvt_pk_bf16_f32 v144, v144, v145\n\t" \
  "v_cvt_pk_bf16_f32 v145, v146, v147\n\t" \
  "v_cvt_pk_bf16_f32 v146, v152, v153\n\t" \
  "v_cvt_pk_bf16_f32 v147, v154, v155\n\t" \
  "buffer_store_dwordx4 v[144:147], %[avo], s[44:47], s69 offen offset:0 sc1\n\t" \
  "s_waitcnt lgkmcnt(10)\n\t" \
  "v_mfma_f32_16x16x32_bf16 v[208:211], v[236:239], v[12:15], v[208:211]\n\t" \
  "v_mfma_f32_16x16x32_bf16 v[212:215], v[236:239], v[84:87], v[212:215]\n\t" \
  "ds_read_b64_tr_b16 v[236:237], v241 offset:46080\n\t" \
  "ds_read_b64_tr_b16 v[238:239], v241 offset:50688\n\t" \
  "s_waitcnt lgkmcnt(10)\n\t" \
  "v_mfma_f32_16x16x32_bf16 v[192:195], v[216:219], v[16:19], v[192:195]\n\t" \
  "v_mfma_f32_16x16x32_bf16 v[196:199], v[216:219], v[88:91], v[196:199]\n\t" \
  "ds_read_b64_tr_b16 v[216:217], v240 offset:55488\n\t" \
  "ds_read_b64_tr_b16 v[218:219], v240 offset:60096\n\t" \
  "s_waitcnt lgkmcnt(10)\n\t" \
  "v_mfma_f32_16x16x32_bf16 v[200:203], v[220:223], v[16:19], v[200:203]\n\t" \
  "v_mfma_f32_16x16x32_bf16 v[204:207], v[220:223], v[88:91], v[204:207]\n\t" \
  "ds_read_b64_tr_b16 v[220:221], v240 offset:55520\n\t" \
  "ds_read_b64_tr_b16 v[222:223], v240 offset:60128\n\t" \
  "s_waitcnt lgkmcnt(10)\n\t" \
  "v_mfma_f32_16x16x32_bf16 v[208:211], v[224:227], v[16:19], v[208:211]\n\t" \
  "v_mfma_f32_16x16x32_bf16 v[212:215], v[224:227], v[88:91], v[212:215]\n\t" \
  "ds_read_b64_tr_b16 v[224:225], v241 offset:55296\n\t" \
  "ds_read_b64_tr_b16 v[226:227], v241 offset:59904\n\t" \
  "s_waitcnt lgkmcnt(10)\n\t" \
  "v_mfma_f32_16x16x32_bf16 v[192:195], v[228:231], v[20:23], v[192:195]\n\t" \
  "v_mfma_f32_16x16x32_bf16 v[196:199], v[228:231], v[92:95], v[196:199]\n\t" \
  "v_add_u32_e32 v240, 64512, v240\n\t" \
  "ds_read_b64_tr_b16 v[228:229], v240 offset:192\n\t" \
  "ds_read_b64_tr_b16 v[230:231], v240 offset:4800\n\t" \
  "s_waitcnt lgkmcnt(10)\n\t" \
  "v_mfma_f32_16x16x32_bf16 v[200:203], v[232:235], v[20:23], v[200:203]\n\t" \
  "v_mfma_f32_16x16x32_bf16 v[204:207], v[232:235], v[92:95], v[204:207]\n\t" \
  "ds_read_b64_tr_b16 v[232:233], v240 offset:224\n\t" \
  "ds_read_b64_tr_b16 v[234:235], v240 offset:4832\n\t" \
  "s_waitcnt lgkmcnt(10)\n\t" \
  "v_mfma_f32_16x16x32_bf16 v[208:211], v[236:239], v[20:23], v[208:211]\n\t" \
  "v_mfma_f32_16x16x32_bf16 v[212:215], v[236:239], v[92:95], v[212:215]\n\t" \
  "v_add_u32_e32 v241, 64512, v241\n\t" \
  "ds_read_b64_tr_b16 v[236:237], v241 offset:0\n\t" \
  "ds_read_b64_tr_b16 v[238:239], v241 offset:4608\n\t" \
  "v_cvt_pk_bf16_f32 v148, v148, v149\n\t" \
  "v_cvt_pk_bf16_f32 v149, v150, v151\n\t" \
  "v_cvt_pk_bf16_f32 v150, v156, v157\n\t" \
  "v_cvt_pk_bf16_f32 v151, v158, v159\n\t" \
  "buffer_store_dwordx4 v[148:151], %[avo], s[44:47], s71 offen offset:0 sc1\n\t" \
  "s_waitcnt lgkmcnt(10)\n\t" \
  "v_mfma_f32_16x16x32_bf16 v[192:195], v[216:219], v[24:27], v[192:195]\n\t" \
  "v_mfma_f32_16x16x32_bf16 v[196:199], v[216:219], v[96:99], v[196:199]\n\t" \
  "ds_read_b64_tr_b16 v[216:217], v240 offset:9408\n\t" \
  "ds_read_b64_tr_b16 v[218:219], v240 offset:14016\n\t" \
  "s_waitcnt lgkmcnt(10)\n\t" \
  "v_mfma_f32_16x16x32_bf16 v[200:203], v[220:223], v[24:27], v[200:203]\n\t" \
  "v_mfma_f32_16x16x32_bf16 v[204:207], v[220:223], v[96:99], v[204:207]\n\t" \
  "ds_read_b64_tr_b16 v[220:221], v240 offset:9440\n\t" \
  "ds_read_b64_tr_b16 v[222:223], v240 offset:14048\n\t" \
  "s_waitcnt lgkmcnt(10)\n\t" \
  "v_mfma_f32_16x16x32_bf16 v[208:211], v[224:227], v[24:27], v[208:211]\n\t" \
  "v_mfma_f32_16x16x32_bf16 v[212:215], v[224:227], v[96:99], v[212:215]\n\t" \
  "ds_read_b64_tr_b16 v[224:225], v241 offset:9216\n\t" \
  "ds_read_b64_tr_b16 v[226:227], v241 offset:13824\n\t" \
  "s_waitcnt lgkmcnt(10)\n\t" \
  "v_mfma_f32_16x16x32_bf16 v[192:195], v[228:231], v[28:31], v[192:195]\n\t" \
  "v_mfma_f32_16x16x32_bf16 v[196:199], v[228:231], v[100:103], v[196:199]\n\t" \
  "ds_read_b64_tr_b16 v[228:229], v240 offset:18624\n\t" \
  "ds_read_b64_tr_b16 v[230:231], v240 offset:23232\n\t" \
  "s_waitcnt lgkmcnt(10)\n\t" \
  "v_mfma_f32_16x16x32_bf16 v[200:203], v[232:235], v[28:31], v[200:203]\n\t" \
  "v_mfma_f32_16x16x32_bf16 v[204:207], v[232:235], v[100:103], v[204:207]\n\t" \
  "ds_read_b64_tr_b16 v[232:233], v240 offset:18656\n\t" \
  "ds_read_b64_tr_b16 v[234:235], v240 offset:23264\n\t" \
  "s_waitcnt lgkmcnt(10)\n\t" \
  "v_mfma_f32_16x16x32_bf16 v[208:211], v[236:239], v[28:31], v[208:211]\n\t" \
  "v_mfma_f32_16x16x32_bf16 v[212:215], v[236:239], v[100:103], v[212:215]\n\t" \
  "ds_read_b64_tr_b16 v[236:237], v241 offset:18432\n\t" \
  "ds_read_b64_tr_b16 v[238:239], v241 offset:23040\n\t" \
  "s_waitcnt lgkmcnt(10)\n\t" \
  "v_mfma_f32_16x16x32_bf16 v[192:195], v[216:219], v[32:35], v[192:195]\n\t" \
  "v_mfma_f32_16x16x32_bf16 v[196:199], v[216:219], v[104:107], v[196:199]\n\t" \
  "ds_read_b64_tr_b16 v[216:217], v240 offset:27840\n\t" \
  "ds_read_b64_tr_b16 v[218:219], v240 offset:32448\n\t" \
  "v_cvt_pk_bf16_f32 v160, v160, v161\n\t" \
  "v_cvt_pk_bf16_f32 v161, v162, v163\n\t" \
  "v_cvt_pk_bf16_f32 v162, v168, v169\n\t" \
  "v_cvt_pk_bf16_f32 v163, v170, v171\n\t" \
  "buffer_store_dwordx4 v[160:163], %[avo], s[44:47], s69 offen offset:1024 sc1\n\t" \
  "s_waitcnt lgkmcnt(10)\n\t" \
  "v_mfma_f32_16x16x32_bf16 v[200:203], v[220:223], v[32:35], v[200:203]\n\t" \
  "v_mfma_f32_16x16x32_bf16 v[204:207], v[220:223], v[104:107], v[204:207]\n\t" \
  "ds_read_b64_tr_b16 v[220:221], v240 offset:27872\n\t" \
  "ds_read_b64_tr_b16 v[222:223], v240 offset:32480\n\t" \
  "s_waitcnt lgkmcnt(10)\n\t" \
  "v_mfma_f32_16x16x32_bf16 v[208:211], v[224:227], v[32:35], v[208:211]\n\t" \
  "v_mfma_f32_16x16x32_bf16 v[212:215], v[224:227], v[104:107], v[212:215]\n\t" \
  "ds_read_b64_tr_b16 v[224:225], v241 offset:27648\n\t" \
  "ds_read_b64_tr_b16 v[226:227], v241 offset:32256\n\t" \
  "s_waitcnt lgkmcnt(10)\n\t" \
  "v_mfma_f32_16x16x32_bf16 v[192:195], v[228:231], v[36:39], v[192:195]\n\t" \
  "v_mfma_f32_16x16x32_bf16 v[196:199], v[228:231], v[108:111], v[196:199]\n\t" \
  "ds_read_b64_tr_b16 v[228:229], v240 offset:37056\n\t" \
  "ds_read_b64_tr_b16 v[230:231], v240 offset:41664\n\t" \
  "s_waitcnt lgkmcnt(10)\n\t" \
  "v_mfma_f32_16x16x32_bf16 v[200:203], v[232:235], v[36:39], v[200:203]\n\t" \
  "v_mfma_f32_16x16x32_bf16 v[204:207], v[232:235], v[108:111], v[204:207]\n\t" \
  "ds_read_b64_tr_b16 v[232:233], v240 offset:37088\n\t" \
  "ds_read_b64_tr_b16 v[234:235], v240 offset:41696\n\t" \
  "s_waitcnt lgkmcnt(10)\n\t" \
  "v_mfma_f32_16x16x32_bf16 v[208:211], v[236:239], v[36:39], v[208:211]\n\t" \
  "v_mfma_f32_16x16x32_bf16 v[212:215], v[236:239], v[108:111], v[212:215]\n\t" \
  "ds_read_b64_tr_b16 v[236:237], v241 offset:36864\n\t" \
  "ds_read_b64_tr_b16 v[238:239], v241 offset:41472\n\t" \
  "s_waitcnt lgkmcnt(10)\n\t" \
  "v_mfma_f32_16x16x32_bf16 v[192:195], v[216:219], v[40:43], v[192:195]\n\t" \
  "v_mfma_f32_16x16x32_bf16 v[196:199], v[216:219], v[112:115], v[196:199]\n\t" \
  "ds_read_b64_tr_b16 v[216:217], v240 offset:46272\n\t" \
  "ds_read_b64_tr_b16 v[218:219], v240 offset:50880\n\t" \
  "s_waitcnt lgkmcnt(10)\n\t" \
  "v_mfma_f32_16x16x32_bf16 v[200:203], v[220:223], v[40:43], v[200:203]\n\t" \
  "v_mfma_f32_16x16x32_bf16 v[204:207], v[220:223], v[112:115], v[204:207]\n\t" \
  "ds_read_b64_tr_b16 v[220:221], v240 offset:46304\n\t" \
  "ds_read_b64_tr_b16 v[222:223], v240 offset:50912\n\t" \
  "v_cvt_pk_bf16_f32 v164, v164, v165\n\t" \
  "v_cvt_pk_bf16_f32 v165, v166, v167\n\t" \
  "v_cvt_pk_bf16_f32 v166, v172, v173\n\t" \
  "v_cvt_pk_bf16_f32 v167, v174, v175\n\t" \
  "buffer_store_dwordx4 v[164:167], %[avo], s[44:47], s71 offen offset:1024 sc1\n\t" \
  "s_waitcnt lgkmcnt(10)\n\t" \
  "v_mfma_f32_16x16x32_bf16 v[208:211], v[224:227], v[40:43], v[208:211]\n\t" \
  "v_mfma_f32_16x16x32_bf16 v[212:215], v[224:227], v[112:115], v[212:215]\n\t" \
  "ds_read_b64_tr_b16 v[224:225], v241 offset:46080\n\t" \
  "ds_read_b64_tr_b16 v[226:227], v241 offset:50688\n\t" \
  "s_waitcnt lgkmcnt(10)\n\t" \
  "v_mfma_f32_16x16x32_bf16 v[192:195], v[228:231], v[44:47], v[192:195]\n\t" \
  "v_mfma_f32_16x16x32_bf16 v[196:199], v[228:231], v[116:119], v[196:199]\n\t" \
  "ds_read_b64_tr_b16 v[228:229], v240 offset:55488\n\t" \
  "ds_read_b64_tr_b16 v[230:231], v240 offset:60096\n\t" \
  "s_waitcnt lgkmcnt(10)\n\t" \
  "v_mfma_f32_16x16x32_bf16 v[200:203], v[232:235], v[44:47], v[200:203]\n\t" \
  "v_mfma_f32_16x16x32_bf16 v[204:207], v[232:235], v[116:119], v[204:207]\n\t" \
  "ds_read_b64_tr_b16 v[232:233], v240 offset:55520\n\t" \
  "ds_read_b64_tr_b16 v[234:235], v240 offset:60128\n\t" \
  "s_waitcnt lgkmcnt(10)\n\t" \
  "v_mfma_f32_16x16x32_bf16 v[208:211], v[236:239], v[44:47], v[208:211]\n\t" \
  "v_mfma_f32_16x16x32_bf16 v[212:215], v[236:239], v[116:119], v[212:215]\n\t" \
  "ds_read_b64_tr_b16 v[236:237], v241 offset:55296\n\t" \
  "ds_read_b64_tr_b16 v[238:239], v241 offset:59904\n\t" \
  "s_waitcnt lgkmcnt(10)\n\t" \
  "v_mfma_f32_16x16x32_bf16 v[192:195], v[216:219], v[48:51], v[192:195]\n\t" \
  "v_mfma_f32_16x16x32_bf16 v[196:199], v[216:219], v[120:123], v[196:199]\n\t" \
  "v_add_u32_e32 v240, 64512, v240\n\t" \
  "ds_read_b64_tr_b16 v[216:217], v240 offset:192\n\t" \
  "ds_read_b64_tr_b16 v[218:219], v240 offset:4800\n\t" \
  "s_waitcnt lgkmcnt(10)\n\t" \
  "v_mfma_f32_16x16x32_bf16 v[200:203], v[220:223], v[48:51], v[200:203]\n\t" \
  "v_mfma_f32_16x16x32_bf16 v[204:207], v[220:223], v[120:123], v[204:207]\n\t" \
  "ds_read_b64_tr_b16 v[220:221], v240 offset:224\n\t" \
  "ds_read_b64_tr_b16 v[222:223], v240 offset:4832\n\t" \
  "s_waitcnt lgkmcnt(10)\n\t" \
  "v_mfma_f32_16x16x32_bf16 v[208:211], v[224:227], v[48:51], v[208:211]\n\t" \
  "v_mfma_f32_16x16x32_bf16 v[212:215], v[224:227], v[120:123], v[212:215]\n\t" \
  "v_add_u32_e32 v241, 64512, v241\n\t" \
  "ds_read_b64_tr_b16 v[224:225], v241 offset:0\n\t" \
  "ds_read_b64_tr_b16 v[226:227], v241 offset:4608\n\t" \
  "v_cvt_pk_bf16_f32 v176, v176, v177\n\t" \
  "v_cvt_pk_bf16_f32 v177, v178, v179\n\t" \
  "v_cvt_pk_bf16_f32 v178, v184, v185\n\t" \
  "v_cvt_pk_bf16_f32 v179, v186, v187\n\t" \
  "buffer_store_dwordx4 v[176:179], %[avo], s[44:47], s69 offen offset:2048 sc1\n\t" \
  "s_waitcnt lgkmcnt(10)\n\t" \
  "v_mfma_f32_16x16x32_bf16 v[192:195], v[228:231], v[52:55], v[192:195]\n\t" \
  "v_mfma_f32_16x16x32_bf16 v[196:199], v[228:231], v[124:127], v[196:199]\n\t" \
  "ds_read_b64_tr_b16 v[228:229], v240 offset:9408\n\t" \
  "ds_read_b64_tr_b16 v[230:231], v240 offset:14016\n\t" \
  "s_waitcnt lgkmcnt(10)\n\t" \
  "v_mfma_f32_16x16x32_bf16 v[200:203], v[232:235], v[52:55], v[200:203]\n\t" \
  "v_mfma_f32_16x16x32_bf16 v[204:207], v[232:235], v[124:127], v[204:207]\n\t" \
  "ds_read_b64_tr_b16 v[232:233], v240 offset:9440\n\t" \
  "ds_read_b64_tr_b16 v[234:235], v240 offset:14048\n\t" \
  "s_waitcnt lgkmcnt(10)\n\t" \
  "v_mfma_f32_16x16x32_bf16 v[208:211], v[236:239], v[52:55], v[208:211]\n\t" \
  "v_mfma_f32_16x16x32_bf16 v[212:215], v[236:239], v[124:127], v[212:215]\n\t" \
  "ds_read_b64_tr_b16 v[236:237], v241 offset:9216\n\t" \
  "ds_read_b64_tr_b16 v[238:239], v241 offset:13824\n\t" \
  "s_waitcnt lgkmcnt(10)\n\t" \
  "v_mfma_f32_16x16x32_bf16 v[192:195], v[216:219], v[56:59], v[192:195]\n\t" \
  "v_mfma_f32_16x16x32_bf16 v[196:199], v[216:219], v[128:131], v[196:199]\n\t" \
  "ds_read_b64_tr_b16 v[216:217], v240 offset:18624\n\t" \
  "ds_read_b64_tr_b16 v[218:219], v240 offset:23232\n\t" \
  "s_waitcnt lgkmcnt(10)\n\t" \
  "v_mfma_f32_16x16x32_bf16 v[200:203], v[220:223], v[56:59], v[200:203]\n\t" \
  "v_mfma_f32_16x16x32_bf16 v[204:207], v[220:223], v[128:131], v[204:207]\n\t" \
  "ds_read_b64_tr_b16 v[220:221], v240 offset:18656\n\t" \
  "ds_read_b64_tr_b16 v[222:223], v240 offset:23264\n\t" \
  "s_waitcnt lgkmcnt(10)\n\t" \
  "v_mfma_f32_16x16x32_bf16 v[208:211], v[224:227], v[56:59], v[208:211]\n\t" \
  "v_mfma_f32_16x16x32_bf16 v[212:215], v[224:227], v[128:131], v[212:215]\n\t" \
  "ds_read_b64_tr_b16 v[224:225], v241 offset:18432\n\t" \
  "ds_read_b64_tr_b16 v[226:227], v241 offset:23040\n\t" \
  "s_waitcnt lgkmcnt(10)\n\t" \
  "v_mfma_f32_16x16x32_bf16 v[192:195], v[228:231], v[60:63], v[192:195]\n\t" \
  "v_mfma_f32_16x16x32_bf16 v[196:199], v[228:231], v[132:135], v[196:199]\n\t" \
  "v_add_u32_e32 v240, 4294838272, v240\n\t" \
  "ds_read_b64_tr_b16 v[228:229], v240 offset:192\n\t" \
  "ds_read_b64_tr_b16 v[230:231], v240 offset:4800\n\t" \
  "v_cvt_pk_bf16_f32 v180, v180, v181\n\t" \
  "v_cvt_pk_bf16_f32 v181, v182, v183\n\t" \
  "v_cvt_pk_bf16_f32 v182, v188, v189\n\t" \
  "v_cvt_pk_bf16_f32 v183, v190, v191\n\t" \
  "buffer_store_dwordx4 v[180:183], %[avo], s[44:47], s71 offen offset:2048 sc1\n\t" \
  "s_waitcnt lgkmcnt(10)\n\t" \
  "v_mfma_f32_16x16x32_bf16 v[200:203], v[232:235], v[60:63], v[200:203]\n\t" \
  "v_mfma_f32_16x16x32_bf16 v[204:207], v[232:235], v[132:135], v[204:207]\n\t" \
  "ds_read_b64_tr_b16 v[232:233], v240 offset:224\n\t" \
  "ds_read_b64_tr_b16 v[234:235], v240 offset:4832\n\t" \
  "s_waitcnt lgkmcnt(10)\n\t" \
  "v_mfma_f32_16x16x32_bf16 v[208:211], v[236:239], v[60:63], v[208:211]\n\t" \
  "v_mfma_f32_16x16x32_bf16 v[212:215], v[236:239], v[132:135], v[212:215]\n\t" \
  "v_add_u32_e32 v241, 4294838272, v241\n\t" \
  "ds_read_b64_tr_b16 v[236:237], v241 offset:0\n\t" \
  "ds_read_b64_tr_b16 v[238:239], v241 offset:4608\n\t" \
  "s_waitcnt lgkmcnt(10)\n\t" \
  "v_mfma_f32_16x16x32_bf16 v[192:195], v[216:219], v[64:67], v[192:195]\n\t" \
  "v_mfma_f32_16x16x32_bf16 v[196:199], v[216:219], v[136:139], v[196:199]\n\t" \
  "s_waitcnt lgkmcnt(8)\n\t" \
  "v_mfma_f32_16x16x32_bf16 v[200:203], v[220:223], v[64:67], v[200:203]\n\t" \
  "v_mfma_f32_16x16x32_bf16 v[204:207], v[220:223], v[136:139], v[204:207]\n\t" \
  "s_waitcnt lgkmcnt(6)\n\t" \
  "v_mfma_f32_16x16x32_bf16 v[208:211], v[224:227], v[64:67], v[208:211]\n\t" \
  "v_mfma_f32_16x16x32_bf16 v[212:215], v[224:227], v[136:139], v[212:215]\n\t" \
  "s_waitcnt lgkmcnt(4)\n\t" \
  "v_mfma_f32_16x16x32_bf16 v[192:195], v[228:231], v[68:71], v[192:195]\n\t" \
  "v_mfma_f32_16x16x32_bf16 v[196:199], v[228:231], v[140:143], v[196:199]\n\t" \
  "s_waitcnt lgkmcnt(2)\n\t" \
  "v_mfma_f32_16x16x32_bf16 v[200:203], v[232:235], v[68:71], v[200:203]\n\t" \
  "v_mfma_f32_16x16x32_bf16 v[204:207], v[232:235], v[140:143], v[204:207]\n\t" \
  "s_waitcnt lgkmcnt(0)\n\t" \
  "v_mfma_f32_16x16x32_bf16 v[208:211], v[236:239], v[68:71], v[208:211]\n\t" \
  "v_mfma_f32_16x16x32_bf16 v[212:215], v[236:239], v[140:143], v[212:215]\n\t" \
  "s_nop 7\n\t" \
  "v_cvt_pk_bf16_f32 v192, v192, v193\n\t" \
  "v_cvt_pk_bf16_f32 v193, v194, v195\n\t" \
  "v_cvt_pk_bf16_f32 v194, v200, v201\n\t" \
  "v_cvt_pk_bf16_f32 v195, v202, v203\n\t" \
  "buffer_store_dwordx4 v[192:195], %[avo], s[44:47], s69 offen offset:3072 sc1\n\t" \
  "v_cvt_pk_bf16_f32 v196, v196, v197\n\t" \
  "v_cvt_pk_bf16_f32 v197, v198, v199\n\t" \
  "v_cvt_pk_bf16_f32 v198, v204, v205\n\t" \
  "v_cvt_pk_bf16_f32 v199, v206, v207\n\t" \
  "buffer_store_dwordx4 v[196:199], %[avo], s[44:47], s71 offen offset:3072 sc1\n\t" \
  "v_cvt_pk_bf16_f32 v208, v208, v209\n\t" \
  "v_cvt_pk_bf16_f32 v209, v210, v211\n\t" \
  "buffer_store_dwordx2 v[208:209], %[avo], s[44:47], s70 offen sc1\n\t" \
  "v_cvt_pk_bf16_f32 v212, v212, v213\n\t" \
  "v_cvt_pk_bf16_f32 v213, v214, v215\n\t" \
  "buffer_store_dwordx2 v[212:213], %[avo], s[44:47], s72 offen sc1\n\t" \
  ""
#define GEMM_P18_CLOB "memory","vcc","scc","v0","v1","v2","v3","v4","v5","v6","v7","v8","v9","v10","v11","v12","v13","v14","v15","v16","v17","v18","v19","v20","v21","v22","v23","v24","v25","v26","v27","v28","v29","v30","v31","v32","v33","v34","v35","v36","v37","v38","v39","v40","v41","v42","v43","v44","v45","v46","v47","v48","v49","v50","v51","v52","v53","v54","v55","v56","v57","v58","v59","v60","v61","v62","v63","v64","v65","v66","v67","v68","v69","v70","v71","v72","v73","v74","v75","v76","v77","v78","v79","v80","v81","v82","v83","v84","v85","v86","v87","v88","v89","v90","v91","v92","v93","v94","v95","v96","v97","v98","v99","v100","v101","v102","v103","v104","v105","v106","v107","v108","v109","v110","v111","v112","v113","v114","v115","v116","v117","v118","v119","v120","v121","v122","v123","v124","v125","v126","v127","v128","v129","v130","v131","v132","v133","v134","v135","v136","v137","v138","v139","v140","v141","v142","v143","v144","v145","v146","v147","v148","v149","v150","v151","v152","v153","v154","v155","v156","v157","v158","v159","v160","v161","v162","v163","v164","v165","v166","v167","v168","v169","v170","v171","v172","v173","v174","v175","v176","v177","v178","v179","v180","v181","v182","v183","v184","v185","v186","v187","v188","v189","v190","v191","v192","v193","v194","v195","v196","v197","v198","v199","v200","v201","v202","v203","v204","v205","v206","v207","v208","v209","v210","v211","v212","v213","v214","v215","v216","v217","v218","v219","v220","v221","v222","v223","v224","v225","v226","v227","v228","v229","v230","v231","v232","v233","v234","v235","v236","v237","v238","v239","v240","v241","s36","s37","s38","s39","s40","s41","s42","s43","s44","s45","s46","s47","s48","s49","s50","s51","s52","s53","s54","s55","s56","s57","s58","s59","s60","s61","s62","s63","s64","s65","s66","s67","s68","s69","s70","s71","s72","s73"
#define GEMM_P16_TEXT \
  "s_mov_b32 s60, %[wid]\n\t" \
  "s_mov_b32 s61, %[lds0]\n\t" \
  "s_mov_b32 s62, %[wpitch]\n\t" \
  "s_mov_b32 s63, %[ars]\n\t" \
  "s_mov_b32 s64, %[ors]\n\t" \
  "s_mov_b32 s65, %[par]\n\t" \
  "s_mov_b64 s[36:37], %[wptr]\n\t" \
  "s_and_b32 s37, s37, 0xffff\n\t" \
  "s_mov_b32 s38, 0x7fffffff\n\t" \
  "s_mov_b32 s39, 0x20000\n\t" \
  "s_lshl_b32 s48, s60, 1\n\t" \
  "s_mul_i32 s49, s48, s63\n\t" \
  "s_mov_b64 s[40:41], %[aptr]\n\t" \
  "s_add_u32 s40, s40, s49\n\t" \
  "s_addc_u32 s41, s41, 0\n\t" \
  "s_and_b32 s41, s41, 0xffff\n\t" \
  "s_mov_b32 s42, 0x7fffffff\n\t" \
  "s_mov_b32 s43, 0x20000\n\t" \
  "s_lshl_b32 s48, s60, 10\n\t" \
  "s_add_u32 s66, s61, s48\n\t" \
  "s_add_u32 s67, s61, 8192\n\t" \
  "s_lshl_b32 s68, s62, 5\n\t" \
  "s_mul_i32 s51, s68, 0\n\t" \
  "s_cmp_lg_u32 s60, 0\n\t" \
  "s_cbranch_scc1 L_p8_0%=\n\t" \
  "s_add_u32 m0, s67, 0\n\t" \
  "s_nop 0\n\t" \
  "buffer_load_dwordx4 %[wvo8], s[36:39], s51 offen lds\n\t" \
  "L_p8_0%=:\n\t" \
  "s_add_u32 m0, s66, 0\n\t" \
  "s_nop 0\n\t" \
  "buffer_load_dwordx4 %[wvo0], s[36:39], s51 offen lds\n\t" \
  "s_mov_b32 s52, 0\n\t" \
  "buffer_load_dwordx4 v[0:3], %[avo], s[40:43], s52 offen offset:0\n\t" \
  "s_add_u32 s52, s63, 0\n\t" \
  "buffer_load_dwordx4 v[64:67], %[avo], s[40:43], s52 offen offset:0\n\t" \
  "s_mul_i32 s51, s68, 1\n\t" \
  "s_cmp_lg_u32 s60, 1\n\t" \
  "s_cbranch_scc1 L_p8_1%=\n\t" \
  "s_add_u32 m0, s67, 9216\n\t" \
  "s_nop 0\n\t" \
  "buffer_load_dwordx4 %[wvo8], s[36:39], s51 offen lds\n\t" \
  "L_p8_1%=:\n\t" \
  "s_add_u32 m0, s66, 9216\n\t" \
  "s_nop 0\n\t" \
  "buffer_load_dwordx4 %[wvo0], s[36:39], s51 offen lds\n\t" \
  "s_mov_b32 s52, 0\n\t" \
  "buffer_load_dwordx4 v[4:7], %[avo], s[40:43], s52 offen offset:1024\n\t" \
  "s_add_u32 s52, s63, 0\n\t" \
  "buffer_load_dwordx4 v[68:71], %[avo], s[40:43], s52 offen offset:1024\n\t" \
  "s_mul_i32 s51, s68, 2\n\t" \
  "s_cmp_lg_u32 s60, 2\n\t" \
  "s_cbranch_scc1 L_p8_2%=\n\t" \
  "s_add_u32 m0, s67, 18432\n\t" \
  "s_nop 0\n\t" \
  "buffer_load_dwordx4 %[wvo8], s[36:39], s51 offen lds\n\t" \
  "L_p8_2%=:\n\t" \
  "s_add_u32 m0, s66, 18432\n\t" \
  "s_nop 0\n\t" \
  "buffer_load_dwordx4 %[wvo0], s[36:39], s51 offen lds\n\t" \
  "s_mov_b32 s52, 0\n\t" \
  "buffer_load_dwordx4 v[8:11], %[avo], s[40:43], s52 offen offset:2048\n\t" \
  "s_add_u32 s52, s63, 0\n\t" \
  "buffer_load_dwordx4 v[72:75], %[avo], s[40:43], s52 offen offset:2048\n\t" \
  "s_mul_i32 s51, s68, 3\n\t" \
  "s_cmp_lg_u32 s60, 3\n\t" \
  "s_cbranch_scc1 L_p8_3%=\n\t" \
  "s_add_u32 m0, s67, 27648\n\t" \
  "s_nop 0\n\t" \
  "buffer_load_dwordx4 %[wvo8], s[36:39], s51 offen lds\n\t" \
  "L_p8_3%=:\n\t" \
  "s_add_u32 m0, s66, 27648\n\t" \
  "s_nop 0\n\t" \
  "buffer_load_dwordx4 %[wvo0], s[36:39], s51 offen lds\n\t" \
  "s_mov_b32 s52, 0\n\t" \
  "buffer_load_dwordx4 v[12:15], %[avo], s[40:43], s52 offen offset:3072\n\t" \
  "s_add_u32 s52, s63, 0\n\t" \
  "buffer_load_dwordx4 v[76:79], %[avo], s[40:43], s52 offen offset:3072\n\t" \
  "s_mul_i32 s51, s68, 4\n\t" \
  "s_cmp_lg_u32 s60, 4\n\t" \
  "s_cbranch_scc1 L_p8_4%=\n\t" \
  "s_add_u32 m0, s67, 36864\n\t" \
  "s_nop 0\n\t" \
  "buffer_load_dwordx4 %[wvo8], s[36:39], s51 offen lds\n\t" \
  "L_p8_4%=:\n\t" \
  "s_add_u32 m0, s66, 36864\n\t" \
  "s_nop 0\n\t" \
  "buffer_load_dwordx4 %[wvo0], s[36:39], s51 offen lds\n\t" \
  "s_lshl_b32 s48, s60, 1\n\t" \
  "s_mul_i32 s49, s48, s64\n\t" \
  "s_mov_b64 s[44:45], %[optr]\n\t" \
  "s_add_u32 s44, s44, s49\n\t" \
  "s_addc_u32 s45, s45, 0\n\t" \
  "s_and_b32 s45, s45, 0xffff\n\t" \
  "s_mov_b32 s46, 0x7fffffff\n\t" \
  "s_mov_b32 s47, 0x20000\n\t" \
  "s_lshl_b32 s69, s65, 10\n\t" \
  "s_add_u32 s71, s69, s64\n\t" \
  "s_cmp_lg_u32 s65, 0\n\t" \
  "s_cselect_b32 s70, 8, 4096\n\t" \
  "s_add_u32 s72, s70, s64\n\t" \
  "s_lshl_b32 s48, s65, 5\n\t" \
  "v_add_u32_e32 v240, s48, %[tro]\n\t" \
  "s_cmp_lg_u32 s65, 0\n\t" \
  "s_cselect_b32 s48, 0, 256\n\t" \
  "v_add_u32_e32 v241, s48, %[tro]\n\t" \
  "s_waitcnt vmcnt(12)\n\t" \
  "s_barrier\n\t" \
  "ds_read_b64_tr_b16 v[216:217], v240 offset:0\n\t" \
  "ds_read_b64_tr_b16 v[218:219], v240 offset:4608\n\t" \
  "ds_read_b64_tr_b16 v[220:221], v240 offset:32\n\t" \
  "ds_read_b64_tr_b16 v[222:223], v240 offset:4640\n\t" \
  "ds_read_b64_tr_b16 v[224:225], v240 offset:64\n\t" \
  "ds_read_b64_tr_b16 v[226:227], v240 offset:4672\n\t" \
  "ds_read_b64_tr_b16 v[228:229], v240 offset:96\n\t" \
  "ds_read_b64_tr_b16 v[230:231], v240 offset:4704\n\t" \
  "ds_read_b64_tr_b16 v[232:233], v240 offset:128\n\t" \
  "ds_read_b64_tr_b16 v[234:235], v240 offset:4736\n\t" \
  "ds_read_b64_tr_b16 v[236:237], v240 offset:160\n\t" \
  "ds_read_b64_tr_b16 v[238:239], v240 offset:4768\n\t" \
  "s_waitcnt vmcnt(10)\n\t" \
  "s_waitcnt lgkmcnt(10)\n\t" \
  "v_mfma_f32_16x16x32_bf16 v[144:147], v[216:219], v[0:3], 0\n\t" \
  "v_mfma_f32_16x16x32_bf16 v[148:151], v[216:219], v[64:67], 0\n\t" \
  "s_waitcnt vmcnt(6)\n\t" \
  "s_barrier\n\t" \
  "ds_read_b64_tr_b16 v[216:217], v240 offset:9216\n\t" \
  "ds_read_b64_tr_b16 v[218:219], v240 offset:13824\n\t" \
  "s_waitcnt lgkmcnt(10)\n\t" \
  "v_mfma_f32_16x16x32_bf16 v[152:155], v[220:223], v[0:3], 0\n\t" \
  "v_mfma_f32_16x16x32_bf16 v[156:159], v[220:223], v[64:67], 0\n\t" \
  "s_mul_i32 s51, s68, 5\n\t" \
  "s_cmp_lg_u32 s60, 5\n\t" \
  "s_cbranch_scc1 L_p8_5%=\n\t" \
  "s_add_u32 m0, s67, 46080\n\t" \
  "s_nop 0\n\t" \
  "buffer_load_dwordx4 %[wvo8], s[36:39], s51 offen lds\n\t" \
  "L_p8_5%=:\n\t" \
  "s_add_u32 m0, s66, 46080\n\t" \
  "s_nop 0\n\t" \
  "buffer_load_dwordx4 %[wvo0], s[36:39], s51 offen lds\n\t" \
  "ds_read_b64_tr_b16 v[220:221], v240 offset:9248\n\t" \
  "ds_read_b64_tr_b16 v[222:223], v240 offset:13856\n\t" \
  "s_waitcnt lgkmcnt(10)\n\t" \
  "v_mfma_f32_16x16x32_bf16 v[160:163], v[224:227], v[0:3], 0\n\t" \
  "v_mfma_f32_16x16x32_bf16 v[164:167], v[224:227], v[64:67], 0\n\t" \
  "ds_read_b64_tr_b16 v[224:225], v240 offset:9280\n\t" \
  "ds_read_b64_tr_b16 v[226:227], v240 offset:13888\n\t" \
  "s_waitcnt lgkmcnt(10)\n\t" \
  "v_mfma_f32_16x16x32_bf16 v[168:171], v[228:231], v[0:3], 0\n\t" \
  "v_mfma_f32_16x16x32_bf16 v[172:175], v[228:231], v[64:67], 0\n\t" \
  "s_mov_b32 s52, 4096\n\t" \
  "buffer_load_dwordx4 v[16:19], %[avo], s[40:43], s52 offen offset:0\n\t" \
  "ds_read_b64_tr_b16 v[228:229], v240 offset:9312\n\t" \
  "ds_read_b64_tr_b16 v[230:231], v240 offset:13920\n\t" \
  "s_waitcnt lgkmcnt(10)\n\t" \
  "v_mfma_f32_16x16x32_bf16 v[176:179], v[232:235], v[0:3], 0\n\t" \
  "v_mfma_f32_16x16x32_bf16 v[180:183], v[232:235], v[64:67], 0\n\t" \
  "ds_read_b64_tr_b16 v[232:233], v240 offset:9344\n\t" \
  "ds_read_b64_tr_b16 v[234:235], v240 offset:13952\n\t" \
  "s_waitcnt lgkmcnt(10)\n\t" \
  "v_mfma_f32_16x16x32_bf16 v[184:187], v[236:239], v[0:3], 0\n\t" \
  "v_mfma_f32_16x16x32_bf16 v[188:191], v[236:239], v[64:67], 0\n\t" \
  "s_add_u32 s52, s63, 4096\n\t" \
  "buffer_load_dwordx4 v[80:83], %[avo], s[40:43], s52 offen offset:0\n\t" \
  "ds_read_b64_tr_b16 v[236:237], v240 offset:9376\n\t" \
  "ds_read_b64_tr_b16 v[238:239], v240 offset:13984\n\t" \
  "s_waitcnt lgkmcnt(10)\n\t" \
  "v_mfma_f32_16x16x32_bf16 v[144:147], v[216:219], v[4:7], v[144:147]\n\t" \
  "v_mfma_f32_16x16x32_bf16 v[148:151], v[216:219], v[68:71], v[148:151]\n\t" \
  "ds_read_b64_tr_b16 v[216:217], v240 offset:18432\n\t" \
  "ds_read_b64_tr_b16 v[218:219], v240 offset:23040\n\t" \
  "s_waitcnt lgkmcnt(10)\n\t" \
  "v_mfma_f32_16x16x32_bf16 v[152:155], v[220:223], v[4:7], v[152:155]\n\t" \
  "v_mfma_f32_16x16x32_bf16 v[156:159], v[220:223], v[68:71], v[156:159]\n\t" \
  "s_mul_i32 s51, s68, 6\n\t" \
  "s_cmp_lg_u32 s60, 6\n\t" \
  "s_cbranch_scc1 L_p8_6%=\n\t" \
  "s_add_u32 m0, s67, 55296\n\t" \
  "s_nop 0\n\t" \
  "buffer_load_dwordx4 %[wvo8], s[36:39], s51 offen lds\n\t" \
  "L_p8_6%=:\n\t" \
  "s_add_u32 m0, s66, 55296\n\t" \
  "s_nop 0\n\t" \
  "buffer_load_dwordx4 %[wvo0], s[36:39], s51 offen lds\n\t" \
  "ds_read_b64_tr_b16 v[220:221], v240 offset:18464\n\t" \
  "ds_read_b64_tr_b16 v[222:223], v240 offset:23072\n\t" \
  "s_waitcnt lgkmcnt(10)\n\t" \
  "v_mfma_f32_16x16x32_bf16 v[160:163], v[224:227], v[4:7], v[160:163]\n\t" \
  "v_mfma_f32_16x16x32_bf16 v[164:167], v[224:227], v[68:71], v[164:167]\n\t" \
  "ds_read_b64_tr_b16 v[224:225], v240 offset:18496\n\t" \
  "ds_read_b64_tr_b16 v[226:227], v240 offset:23104\n\t" \
  "s_waitcnt lgkmcnt(10)\n\t" \
  "v_mfma_f32_16x16x32_bf16 v[168:171], v[228:231], v[4:7], v[168:171]\n\t" \
  "v_mfma_f32_16x16x32_bf16 v[172:175], v[228:231], v[68:71], v[172:175]\n\t" \
  "s_mov_b32 s52, 4096\n\t" \
  "buffer_load_dwordx4 v[20:23], %[avo], s[40:43], s52 offen offset:1024\n\t" \
  "ds_read_b64_tr_b16 v[228:229], v240 offset:18528\n\t" \
  "ds_read_b64_tr_b16 v[230:231], v240 offset:23136\n\t" \
  "s_waitcnt lgkmcnt(10)\n\t" \
  "v_mfma_f32_16x16x32_bf16 v[176:179], v[232:235], v[4:7], v[176:179]\n\t" \
  "v_mfma_f32_16x16x32_bf16 v[180:183], v[232:235], v[68:71], v[180:183]\n\t" \
  "ds_read_b64_tr_b16 v[232:233], v240 offset:18560\n\t" \
  "ds_read_b64_tr_b16 v[234:235], v240 offset:23168\n\t" \
  "s_waitcnt lgkmcnt(10)\n\t" \
  "v_mfma_f32_16x16x32_bf16 v[184:187], v[236:239], v[4:7], v[184:187]\n\t" \
  "v_mfma_f32_16x16x32_bf16 v[188:191], v[236:239], v[68:71], v[188:191]\n\t" \
  "s_add_u32 s52, s63, 4096\n\t" \
  "buffer_load_dwordx4 v[84:87], %[avo], s[40:43], s52 offen offset:1024\n\t" \
  "ds_read_b64_tr_b16 v[236:237], v240 offset:18592\n\t" \
  "ds_read_b64_tr_b16 v[238:239], v240 offset:23200\n\t" \
  "s_waitcnt vmcnt(10)\n\t" \
  "s_waitcnt lgkmcnt(10)\n\t" \
  "v_mfma_f32_16x16x32_bf16 v[144:147], v[216:219], v[8:11], v[144:147]\n\t" \
  "v_mfma_f32_16x16x32_bf16 v[148:151], v[216:219], v[72:75], v[148:151]\n\t" \
  "s_waitcnt vmcnt(5)\n\t" \
  "s_barrier\n\t" \
  "ds_read_b64_tr_b16 v[216:217], v240 offset:27648\n\t" \
  "ds_read_b64_tr_b16 v[218:219], v240 offset:32256\n\t" \
  "s_waitcnt lgkmcnt(10)\n\t" \
  "v_mfma_f32_16x16x32_bf16 v[152:155], v[220:223], v[8:11], v[152:155]\n\t" \
  "v_mfma_f32_16x16x32_bf16 v[156:159], v[220:223], v[72:75], v[156:159]\n\t" \
  "s_mul_i32 s51, s68, 7\n\t" \
  "s_cmp_lg_u32 s60, 7\n\t" \
  "s_cbranch_scc1 L_p8_7%=\n\t" \
  "s_add_u32 m0, s67, 64512\n\t" \
  "s_nop 0\n\t" \
  "buffer_load_dwordx4 %[wvo8], s[36:39], s51 offen lds\n\t" \
  "L_p8_7%=:\n\t" \
  "s_add_u32 m0, s66, 64512\n\t" \
  "s_nop 0\n\t" \
  "buffer_load_dwordx4 %[wvo0], s[36:39], s51 offen lds\n\t" \
  "ds_read_b64_tr_b16 v[220:221], v240 offset:27680\n\t" \
  "ds_read_b64_tr_b16 v[222:223], v240 offset:32288\n\t" \
  "s_waitcnt lgkmcnt(10)\n\t" \
  "v_mfma_f32_16x16x32_bf16 v[160:163], v[224:227], v[8:11], v[160:163]\n\t" \
  "v_mfma_f32_16x16x32_bf16 v[164:167], v[224:227], v[72:75], v[164:167]\n\t" \
  "ds_read_b64_tr_b16 v[224:225], v240 offset:27712\n\t" \
  "ds_read_b64_tr_b16 v[226:227], v240 offset:32320\n\t" \
  "s_waitcnt lgkmcnt(10)\n\t" \
  "v_mfma_f32_16x16x32_bf16 v[168:171], v[228:231], v[8:11], v[168:171]\n\t" \
  "v_mfma_f32_16x16x32_bf16 v[172:175], v[228:231], v[72:75], v[172:175]\n\t" \
  "s_mov_b32 s52, 4096\n\t" \
  "buffer_load_dwordx4 v[24:27], %[avo], s[40:43], s52 offen offset:2048\n\t" \
  "ds_read_b64_tr_b16 v[228:229], v240 offset:27744\n\t" \
  "ds_read_b64_tr_b16 v[230:231], v240 offset:32352\n\t" \
  "s_waitcnt lgkmcnt(10)\n\t" \
  "v_mfma_f32_16x16x32_bf16 v[176:179], v[232:235], v[8:11], v[176:179]\n\t" \
  "v_mfma_f32_16x16x32_bf16 v[180:183], v[232:235], v[72:75], v[180:183]\n\t" \
  "ds_read_b64_tr_b16 v[232:233], v240 offset:27776\n\t" \
  "ds_read_b64_tr_b16 v[234:235], v240 offset:32384\n\t" \
  "s_waitcnt lgkmcnt(10)\n\t" \
  "v_mfma_f32_16x16x32_bf16 v[184:187], v[236:239], v[8:11], v[184:187]\n\t" \
  "v_mfma_f32_16x16x32_bf16 v[188:191], v[236:239], v[72:75], v[188:191]\n\t" \
  "s_add_u32 s52, s63, 4096\n\t" \
  "buffer_load_dwordx4 v[88:91], %[avo], s[40:43], s52 offen offset:2048\n\t" \
  "ds_read_b64_tr_b16 v[236:237], v240 offset:27808\n\t" \
  "ds_read_b64_tr_b16 v[238:239], v240 offset:32416\n\t" \
  "s_waitcnt lgkmcnt(10)\n\t" \
  "v_mfma_f32_16x16x32_bf16 v[144:147], v[216:219], v[12:15], v[144:147]\n\t" \
  "v_mfma_f32_16x16x32_bf16 v[148:151], v[216:219], v[76:79], v[148:151]\n\t" \
  "ds_read_b64_tr_b16 v[216:217], v240 offset:36864\n\t" \
  "ds_read_b64_tr_b16 v[218:219], v240 offset:41472\n\t" \
  "s_waitcnt lgkmcnt(10)\n\t" \
  "v_mfma_f32_16x16x32_bf16 v[152:155], v[220:223], v[12:15], v[152:155]\n\t" \
  "v_mfma_f32_16x16x32_bf16 v[156:159], v[220:223], v[76:79], v[156:159]\n\t" \
  "s_mul_i32 s51, s68, 8\n\t" \
  "s_cmp_lg_u32 s60, 0\n\t" \
  "s_cbranch_scc1 L_p8_8%=\n\t" \
  "s_add_u32 m0, s67, 73728\n\t" \
  "s_nop 0\n\t" \
  "buffer_load_dwordx4 %[wvo8], s[36:39], s51 offen lds\n\t" \
  "L_p8_8%=:\n\t" \
  "s_add_u32 m0, s66, 73728\n\t" \
  "s_nop 0\n\t" \
  "buffer_load_dwordx4 %[wvo0], s[36:39], s51 offen lds\n\t" \
  "ds_read_b64_tr_b16 v[220:221], v240 offset:36896\n\t" \
  "ds_read_b64_tr_b16 v[222:223], v240 offset:41504\n\t" \
  "s_waitcnt lgkmcnt(10)\n\t" \
  "v_mfma_f32_16x16x32_bf16 v[160:163], v[224:227], v[12:15], v[160:163]\n\t" \
  "v_mfma_f32_16x16x32_bf16 v[164:167], v[224:227], v[76:79], v[164:167]\n\t" \
  "ds_read_b64_tr_b16 v[224:225], v240 offset:36928\n\t" \
  "ds_read_b64_tr_b16 v[226:227], v240 offset:41536\n\t" \
  "s_waitcnt lgkmcnt(10)\n\t" \
  "v_mfma_f32_16x16x32_bf16 v[168:171], v[228:231], v[12:15], v[168:171]\n\t" \
  "v_mfma_f32_16x16x32_bf16 v[172:175], v[228:231], v[76:79], v[172:175]\n\t" \
  "s_mov_b32 s52, 4096\n\t" \
  "buffer_load_dwordx4 v[28:31], %[avo], s[40:43], s52 offen offset:3072\n\t" \
  "ds_read_b64_tr_b16 v[228:229], v240 offset:36960\n\t" \
  "ds_read_b64_tr_b16 v[230:231], v240 offset:41568\n\t" \
  "s_waitcnt lgkmcnt(10)\n\t" \
  "v_mfma_f32_16x16x32_bf16 v[176:179], v[232:235], v[12:15], v[176:179]\n\t" \
  "v_mfma_f32_16x16x32_bf16 v[180:183], v[232:235], v[76:79], v[180:183]\n\t" \
  "ds_read_b64_tr_b16 v[232:233], v240 offset:36992\n\t" \
  "ds_read_b64_tr_b16 v[234:235], v240 offset:41600\n\t" \
  "s_waitcnt lgkmcnt(10)\n\t" \
  "v_mfma_f32_16x16x32_bf16 v[184:187], v[236:239], v[12:15], v[184:187]\n\t" \
  "v_mfma_f32_16x16x32_bf16 v[188:191], v[236:239], v[76:79], v[188:191]\n\t" \
  "s_add_u32 s52, s63, 4096\n\t" \
  "buffer_load_dwordx4 v[92:95], %[avo], s[40:43], s52 offen offset:3072\n\t" \
  "ds_read_b64_tr_b16 v[236:237], v240 offset:37024\n\t" \
  "ds_read_b64_tr_b16 v[238:239], v240 offset:41632\n\t" \
  "s_waitcnt vmcnt(9)\n\t" \
  "s_waitcnt lgkmcnt(10)\n\t" \
  "v_mfma_f32_16x16x32_bf16 v[144:147], v[216:219], v[16:19], v[144:147]\n\t" \
  "v_mfma_f32_16x16x32_bf16 v[148:151], v[216:219], v[80:83], v[148:151]\n\t" \
  "ds_read_b64_tr_b16 v[216:217], v240 offset:46080\n\t" \
  "ds_read_b64_tr_b16 v[218:219], v240 offset:50688\n\t" \
  "s_waitcnt lgkmcnt(10)\n\t" \
  "v_mfma_f32_16x16x32_bf16 v[152:155], v[220:223], v[16:19], v[152:155]\n\t" \
  "v_mfma_f32_16x16x32_bf16 v[156:159], v[220:223], v[80:83], v[156:159]\n\t" \
  "s_mul_i32 s51, s68, 9\n\t" \
  "s_cmp_lg_u32 s60, 1\n\t" \
  "s_cbranch_scc1 L_p8_9%=\n\t" \
  "s_add_u32 m0, s67, 82944\n\t" \
  "s_nop 0\n\t" \
  "buffer_load_dwordx4 %[wvo8], s[36:39], s51 offen lds\n\t" \
  "L_p8_9%=:\n\t" \
  "s_add_u32 m0, s66, 82944\n\t" \
  "s_nop 0\n\t" \
  "buffer_load_dwordx4 %[wvo0], s[36:39], s51 offen lds\n\t" \
  "ds_read_b64_tr_b16 v[220:221], v240 offset:46112\n\t" \
  "ds_read_b64_tr_b16 v[222:223], v240 offset:50720\n\t" \
  "s_waitcnt lgkmcnt(10)\n\t" \
  "v_mfma_f32_16x16x32_bf16 v[160:163], v[224:227], v[16:19], v[160:163]\n\t" \
  "v_mfma_f32_16x16x32_bf16 v[164:167], v[224:227], v[80:83], v[164:167]\n\t" \
  "ds_read_b64_tr_b16 v[224:225], v240 offset:46144\n\t" \
  "ds_read_b64_tr_b16 v[226:227], v240 offset:50752\n\t" \
  "s_waitcnt lgkmcnt(10)\n\t" \
  "v_mfma_f32_16x16x32_bf16 v[168:171], v[228:231], v[16:19], v[168:171]\n\t" \
  "v_mfma_f32_16x16x32_bf16 v[172:175], v[228:231], v[80:83], v[172:175]\n\t" \
  "s_mov_b32 s52, 8192\n\t" \
  "buffer_load_dwordx4 v[32:35], %[avo], s[40:43], s52 offen offset:0\n\t" \
  "ds_read_b64_tr_b16 v[228:229], v240 offset:46176\n\t" \
  "ds_read_b64_tr_b16 v[230:231], v240 offset:50784\n\t" \
  "s_waitcnt lgkmcnt(10)\n\t" \
  "v_mfma_f32_16x16x32_bf16 v[176:179], v[232:235], v[16:19], v[176:179]\n\t" \
  "v_mfma_f32_16x16x32_bf16 v[180:183], v[232:235], v[80:83], v[180:183]\n\t" \
  "ds_read_b64_tr_b16 v[232:233], v240 offset:46208\n\t" \
  "ds_read_b64_tr_b16 v[234:235], v240 offset:50816\n\t" \
  "s_waitcnt lgkmcnt(10)\n\t" \
  "v_mfma_f32_16x16x32_bf16 v[184:187], v[236:239], v[16:19], v[184:187]\n\t" \
  "v_mfma_f32_16x16x32_bf16 v[188:191], v[236:239], v[80:83], v[188:191]\n\t" \
  "s_add_u32 s52, s63, 8192\n\t" \
  "buffer_load_dwordx4 v[96:99], %[avo], s[40:43], s52 offen offset:0\n\t" \
  "ds_read_b64_tr_b16 v[236:237], v240 offset:46240\n\t" \
  "ds_read_b64_tr_b16 v[238:239], v240 offset:50848\n\t" \
  "s_waitcnt vmcnt(9)\n\t" \
  "s_waitcnt lgkmcnt(10)\n\t" \
  "v_mfma_f32_16x16x32_bf16 v[144:147], v[216:219], v[20:23], v[144:147]\n\t" \
  "v_mfma_f32_16x16x32_bf16 v[148:151], v[216:219], v[84:87], v[148:151]\n\t" \
  "s_waitcnt vmcnt(5)\n\t" \
  "s_barrier\n\t" \
  "ds_read_b64_tr_b16 v[216:217], v240 offset:55296\n\t" \
  "ds_read_b64_tr_b16 v[218:219], v240 offset:59904\n\t" \
  "s_waitcnt lgkmcnt(10)\n\t" \
  "v_mfma_f32_16x16x32_bf16 v[152:155], v[220:223], v[20:23], v[152:155]\n\t" \
  "v_mfma_f32_16x16x32_bf16 v[156:159], v[220:223], v[84:87], v[156:159]\n\t" \
  "s_mul_i32 s51, s68, 10\n\t" \
  "s_cmp_lg_u32 s60, 2\n\t" \
  "s_cbranch_scc1 L_p8_10%=\n\t" \
  "s_add_u32 m0, s67, 92160\n\t" \
  "s_nop 0\n\t" \
  "buffer_load_dwordx4 %[wvo8], s[36:39], s51 offen lds\n\t" \
  "L_p8_10%=:\n\t" \
  "s_add_u32 m0, s66, 92160\n\t" \
  "s_nop 0\n\t" \
  "buffer_load_dwordx4 %[wvo0], s[36:39], s51 offen lds\n\t" \
  "ds_read_b64_tr_b16 v[220:221], v240 offset:55328\n\t" \
  "ds_read_b64_tr_b16 v[222:223], v240 offset:59936\n\t" \
  "s_waitcnt lgkmcnt(10)\n\t" \
  "v_mfma_f32_16x16x32_bf16 v[160:163], v[224:227], v[20:23], v[160:163]\n\t" \
  "v_mfma_f32_16x16x32_bf16 v[164:167], v[224:227], v[84:87], v[164:167]\n\t" \
  "ds_read_b64_tr_b16 v[224:225], v240 offset:55360\n\t" \
  "ds_read_b64_tr_b16 v[226:227], v240 offset:59968\n\t" \
  "s_waitcnt lgkmcnt(10)\n\t" \
  "v_mfma_f32_16x16x32_bf16 v[168:171], v[228:231], v[20:23], v[168:171]\n\t" \
  "v_mfma_f32_16x16x32_bf16 v[172:175], v[228:231], v[84:87], v[172:175]\n\t" \
  "s_mov_b32 s52, 8192\n\t" \
  "buffer_load_dwordx4 v[36:39], %[avo], s[40:43], s52 offen offset:1024\n\t" \
  "ds_read_b64_tr_b16 v[228:229], v240 offset:55392\n\t" \
  "ds_read_b64_tr_b16 v[230:231], v240 offset:60000\n\t" \
  "s_waitcnt lgkmcnt(10)\n\t" \
  "v_mfma_f32_16x16x32_bf16 v[176:179], v[232:235], v[20:23], v[176:179]\n\t" \
  "v_mfma_f32_16x16x32_bf16 v[180:183], v[232:235], v[84:87], v[180:183]\n\t" \
  "ds_read_b64_tr_b16 v[232:233], v240 offset:55424\n\t" \
  "ds_read_b64_tr_b16 v[234:235], v240 offset:60032\n\t" \
  "s_waitcnt lgkmcnt(10)\n\t" \
  "v_mfma_f32_16x16x32_bf16 v[184:187], v[236:239], v[20:23], v[184:187]\n\t" \
  "v_mfma_f32_16x16x32_bf16 v[188:191], v[236:239], v[84:87], v[188:191]\n\t" \
  "s_add_u32 s52, s63, 8192\n\t" \
  "buffer_load_dwordx4 v[100:103], %[avo], s[40:43], s52 offen offset:1024\n\t" \
  "ds_read_b64_tr_b16 v[236:237], v240 offset:55456\n\t" \
  "ds_read_b64_tr_b16 v[238:239], v240 offset:60064\n\t" \
  "s_waitcnt lgkmcnt(10)\n\t" \
  "v_mfma_f32_16x16x32_bf16 v[144:147], v[216:219], v[24:27], v[144:147]\n\t" \
  "v_mfma_f32_16x16x32_bf16 v[148:151], v[216:219], v[88:91], v[148:151]\n\t" \
  "v_add_u32_e32 v240, 64512, v240\n\t" \
  "ds_read_b64_tr_b16 v[216:217], v240 offset:0\n\t" \
  "ds_read_b64_tr_b16 v[218:219], v240 offset:4608\n\t" \
  "s_waitcnt lgkmcnt(10)\n\t" \
  "v_mfma_f32_16x16x32_bf16 v[152:155], v[220:223], v[24:27], v[152:155]\n\t" \
  "v_mfma_f32_16x16x32_bf16 v[156:159], v[220:223], v[88:91], v[156:159]\n\t" \
  "s_mul_i32 s51, s68, 11\n\t" \
  "s_cmp_lg_u32 s60, 3\n\t" \
  "s_cbranch_scc1 L_p8_11%=\n\t" \
  "s_add_u32 m0, s67, 101376\n\t" \
  "s_nop 0\n\t" \
  "buffer_load_dwordx4 %[wvo8], s[36:39], s51 offen lds\n\t" \
  "L_p8_11%=:\n\t" \
  "s_add_u32 m0, s66, 101376\n\t" \
  "s_nop 0\n\t" \
  "buffer_load_dwordx4 %[wvo0], s[36:39], s51 offen lds\n\t" \
  "ds_read_b64_tr_b16 v[220:221], v240 offset:32\n\t" \
  "ds_read_b64_tr_b16 v[222:223], v240 offset:4640\n\t" \
  "s_waitcnt lgkmcnt(10)\n\t" \
  "v_mfma_f32_16x16x32_bf16 v[160:163], v[224:227], v[24:27], v[160:163]\n\t" \
  "v_mfma_f32_16x16x32_bf16 v[164:167], v[224:227], v[88:91], v[164:167]\n\t" \
  "ds_read_b64_tr_b16 v[224:225], v240 offset:64\n\t" \
  "ds_read_b64_tr_b16 v[226:227], v240 offset:4672\n\t" \
  "s_waitcnt lgkmcnt(10)\n\t" \
  "v_mfma_f32_16x16x32_bf16 v[168:171], v[228:231], v[24:27], v[168:171]\n\t" \
  "v_mfma_f32_16x16x32_bf16 v[172:175], v[228:231], v[88:91], v[172:175]\n\t" \
  "s_mov_b32 s52, 8192\n\t" \
  "buffer_load_dwordx4 v[40:43], %[avo], s[40:43], s52 offen offset:2048\n\t" \
  "ds_read_b64_tr_b16 v[228:229], v240 offset:96\n\t" \
  "ds_read_b64_tr_b16 v[230:231], v240 offset:4704\n\t" \
  "s_waitcnt lgkmcnt(10)\n\t" \
  "v_mfma_f32_16x16x32_bf16 v[176:179], v[232:235], v[24:27], v[176:179]\n\t" \
  "v_mfma_f32_16x16x32_bf16 v[180:183], v[232:235], v[88:91], v[180:183]\n\t" \
  "ds_read_b64_tr_b16 v[232:233], v240 offset:128\n\t" \
  "ds_read_b64_tr_b16 v[234:235], v240 offset:4736\n\t" \
  "s_waitcnt lgkmcnt(10)\n\t" \
  "v_mfma_f32_16x16x32_bf16 v[184:187], v[236:239], v[24:27], v[184:187]\n\t" \
  "v_mfma_f32_16x16x32_bf16 v[188:191], v[236:239], v[88:91], v[188:191]\n\t" \
  "s_add_u32 s52, s63, 8192\n\t" \
  "buffer_load_dwordx4 v[104:107], %[avo], s[40:43], s52 offen offset:2048\n\t" \
  "ds_read_b64_tr_b16 v[236:237], v240 offset:160\n\t" \
  "ds_read_b64_tr_b16 v[238:239], v240 offset:4768\n\t" \
  "s_waitcnt vmcnt(9)\n\t" \
  "s_waitcnt lgkmcnt(10)\n\t" \
  "v_mfma_f32_16x16x32_bf16 v[144:147], v[216:219], v[28:31], v[144:147]\n\t" \
  "v_mfma_f32_16x16x32_bf16 v[148:151], v[216:219], v[92:95], v[148:151]\n\t" \
  "ds_read_b64_tr_b16 v[216:217], v240 offset:9216\n\t" \
  "ds_read_b64_tr_b16 v[218:219], v240 offset:13824\n\t" \
  "s_waitcnt lgkmcnt(10)\n\t" \
  "v_mfma_f32_16x16x32_bf16 v[152:155], v[220:223], v[28:31], v[152:155]\n\t" \
  "v_mfma_f32_16x16x32_bf16 v[156:159], v[220:223], v[92:95], v[156:159]\n\t" \
  "s_mul_i32 s51, s68, 12\n\t" \
  "s_cmp_lg_u32 s60, 4\n\t" \
  "s_cbranch_scc1 L_p8_12%=\n\t" \
  "s_add_u32 m0, s67, 110592\n\t" \
  "s_nop 0\n\t" \
  "buffer_load_dwordx4 %[wvo8], s[36:39], s51 offen lds\n\t" \
  "L_p8_12%=:\n\t" \
  "s_add_u32 m0, s66, 110592\n\t" \
  "s_nop 0\n\t" \
  "buffer_load_dwordx4 %[wvo0], s[36:39], s51 offen lds\n\t" \
  "ds_read_b64_tr_b16 v[220:221], v240 offset:9248\n\t" \
  "ds_read_b64_tr_b16 v[222:223], v240 offset:13856\n\t" \
  "s_waitcnt lgkmcnt(10)\n\t" \
  "v_mfma_f32_16x16x32_bf16 v[160:163], v[224:227], v[28:31], v[160:163]\n\t" \
  "v_mfma_f32_16x16x32_bf16 v[164:167], v[224:227], v[92:95], v[164:167]\n\t" \
  "ds_read_b64_tr_b16 v[224:225], v240 offset:9280\n\t" \
  "ds_read_b64_tr_b16 v[226:227], v240 offset:13888\n\t" \
  "s_waitcnt lgkmcnt(10)\n\t" \
  "v_mfma_f32_16x16x32_bf16 v[168:171], v[228:231], v[28:31], v[168:171]\n\t" \
  "v_mfma_f32_16x16x32_bf16 v[172:175], v[228:231], v[92:95], v[172:175]\n\t" \
  "s_mov_b32 s52, 8192\n\t" \
  "buffer_load_dwordx4 v[44:47], %[avo], s[40:43], s52 offen offset:3072\n\t" \
  "ds_read_b64_tr_b16 v[228:229], v240 offset:9312\n\t" \
  "ds_read_b64_tr_b16 v[230:231], v240 offset:13920\n\t" \
  "s_waitcnt lgkmcnt(10)\n\t" \
  "v_mfma_f32_16x16x32_bf16 v[176:179], v[232:235], v[28:31], v[176:179]\n\t" \
  "v_mfma_f32_16x16x32_bf16 v[180:183], v[232:235], v[92:95], v[180:183]\n\t" \
  "ds_read_b64_tr_b16 v[232:233], v240 offset:9344\n\t" \
  "ds_read_b64_tr_b16 v[234:235], v240 offset:13952\n\t" \
  "s_waitcnt lgkmcnt(10)\n\t" \
  "v_mfma_f32_16x16x32_bf16 v[184:187], v[236:239], v[28:31], v[184:187]\n\t" \
  "v_mfma_f32_16x16x32_bf16 v[188:191], v[236:239], v[92:95], v[188:191]\n\t" \
  "s_add_u32 s52, s63, 8192\n\t" \
  "buffer_load_dwordx4 v[108:111], %[avo], s[40:43], s52 offen offset:3072\n\t" \
  "ds_read_b64_tr_b16 v[236:237], v240 offset:9376\n\t" \
  "ds_read_b64_tr_b16 v[238:239], v240 offset:13984\n\t" \
  "s_waitcnt vmcnt(9)\n\t" \
  "s_waitcnt lgkmcnt(10)\n\t" \
  "v_mfma_f32_16x16x32_bf16 v[144:147], v[216:219], v[32:35], v[144:147]\n\t" \
  "v_mfma_f32_16x16x32_bf16 v[148:151], v[216:219], v[96:99], v[148:151]\n\t" \
  "s_waitcnt vmcnt(5)\n\t" \
  "s_barrier\n\t" \
  "ds_read_b64_tr_b16 v[216:217], v240 offset:18432\n\t" \
  "ds_read_b64_tr_b16 v[218:219], v240 offset:23040\n\t" \
  "s_waitcnt lgkmcnt(10)\n\t" \
  "v_mfma_f32_16x16x32_bf16 v[152:155], v[220:223], v[32:35], v[152:155]\n\t" \
  "v_mfma_f32_16x16x32_bf16 v[156:159], v[220:223], v[96:99], v[156:159]\n\t" \
  "s_mul_i32 s51, s68, 13\n\t" \
  "s_cmp_lg_u32 s60, 5\n\t" \
  "s_cbranch_scc1 L_p8_13%=\n\t" \
  "s_add_u32 m0, s67, 119808\n\t" \
  "s_nop 0\n\t" \
  "buffer_load_dwordx4 %[wvo8], s[36:39], s51 offen lds\n\t" \
  "L_p8_13%=:\n\t" \
  "s_add_u32 m0, s66, 119808\n\t" \
  "s_nop 0\n\t" \
  "buffer_load_dwordx4 %[wvo0], s[36:39], s51 offen lds\n\t" \
  "ds_read_b64_tr_b16 v[220:221], v240 offset:18464\n\t" \
  "ds_read_b64_tr_b16 v[222:223], v240 offset:23072\n\t" \
  "s_waitcnt lgkmcnt(10)\n\t" \
  "v_mfma_f32_16x16x32_bf16 v[160:163], v[224:227], v[32:35], v[160:163]\n\t" \
  "v_mfma_f32_16x16x32_bf16 v[164:167], v[224:227], v[96:99], v[164:167]\n\t" \
  "ds_read_b64_tr_b16 v[224:225], v240 offset:18496\n\t" \
  "ds_read_b64_tr_b16 v[226:227], v240 offset:23104\n\t" \
  "s_waitcnt lgkmcnt(10)\n\t" \
  "v_mfma_f32_16x16x32_bf16 v[168:171], v[228:231], v[32:35], v[168:171]\n\t" \
  "v_mfma_f32_16x16x32_bf16 v[172:175], v[228:231], v[96:99], v[172:175]\n\t" \
  "s_mov_b32 s52, 12288\n\t" \
  "buffer_load_dwordx4 v[48:51], %[avo], s[40:43], s52 offen offset:0\n\t" \
  "ds_read_b64_tr_b16 v[228:229], v240 offset:18528\n\t" \
  "ds_read_b64_tr_b16 v[230:231], v240 offset:23136\n\t" \
  "s_waitcnt lgkmcnt(10)\n\t" \
  "v_mfma_f32_16x16x32_bf16 v[176:179], v[232:235], v[32:35], v[176:179]\n\t" \
  "v_mfma_f32_16x16x32_bf16 v[180:183], v[232:235], v[96:99], v[180:183]\n\t" \
  "ds_read_b64_tr_b16 v[232:233], v240 offset:18560\n\t" \
  "ds_read_b64_tr_b16 v[234:235], v240 offset:23168\n\t" \
  "s_waitcnt lgkmcnt(10)\n\t" \
  "v_mfma_f32_16x16x32_bf16 v[184:187], v[236:239], v[32:35], v[184:187]\n\t" \
  "v_mfma_f32_16x16x32_bf16 v[188:191], v[236:239], v[96:99], v[188:191]\n\t" \
  "s_add_u32 s52, s63, 12288\n\t" \
  "buffer_load_dwordx4 v[112:115], %[avo], s[40:43], s52 offen offset:0\n\t" \
  "ds_read_b64_tr_b16 v[236:237], v240 offset:18592\n\t" \
  "ds_read_b64_tr_b16 v[238:239], v240 offset:23200\n\t" \
  "s_waitcnt lgkmcnt(10)\n\t" \
  "v_mfma_f32_16x16x32_bf16 v[144:147], v[216:219], v[36:39], v[144:147]\n\t" \
  "v_mfma_f32_16x16x32_bf16 v[148:151], v[216:219], v[100:103], v[148:151]\n\t" \
  "ds_read_b64_tr_b16 v[216:217], v240 offset:27648\n\t" \
  "ds_read_b64_tr_b16 v[218:219], v240 offset:32256\n\t" \
  "s_waitcnt lgkmcnt(10)\n\t" \
  "v_mfma_f32_16x16x32_bf16 v[152:155], v[220:223], v[36:39], v[152:155]\n\t" \
  "v_mfma_f32_16x16x32_bf16 v[156:159], v[220:223], v[100:103], v[156:159]\n\t" \
  "s_mul_i32 s51, s68, 14\n\t" \
  "s_cmp_lg_u32 s60, 6\n\t" \
  "s_cbranch_scc1 L_p8_14%=\n\t" \
  "s_add_u32 m0, s67, 129024\n\t" \
  "s_nop 0\n\t" \
  "buffer_load_dwordx4 %[wvo8], s[36:39], s51 offen lds\n\t" \
  "L_p8_14%=:\n\t" \
  "s_add_u32 m0, s66, 129024\n\t" \
  "s_nop 0\n\t" \
  "buffer_load_dwordx4 %[wvo0], s[36:39], s51 offen lds\n\t" \
  "ds_read_b64_tr_b16 v[220:221], v240 offset:27680\n\t" \
  "ds_read_b64_tr_b16 v[222:223], v240 offset:32288\n\t" \
  "s_waitcnt lgkmcnt(10)\n\t" \
  "v_mfma_f32_16x16x32_bf16 v[160:163], v[224:227], v[36:39], v[160:163]\n\t" \
  "v_mfma_f32_16x16x32_bf16 v[164:167], v[224:227], v[100:103], v[164:167]\n\t" \
  "ds_read_b64_tr_b16 v[224:225], v240 offset:27712\n\t" \
  "ds_read_b64_tr_b16 v[226:227], v240 offset:32320\n\t" \
  "s_waitcnt lgkmcnt(10)\n\t" \
  "v_mfma_f32_16x16x32_bf16 v[168:171], v[228:231], v[36:39], v[168:171]\n\t" \
  "v_mfma_f32_16x16x32_bf16 v[172:175], v[228:231], v[100:103], v[172:175]\n\t" \
  "s_mov_b32 s52, 12288\n\t" \
  "buffer_load_dwordx4 v[52:55], %[avo], s[40:43], s52 offen offset:1024\n\t" \
  "ds_read_b64_tr_b16 v[228:229], v240 offset:27744\n\t" \
  "ds_read_b64_tr_b16 v[230:231], v240 offset:32352\n\t" \
  "s_waitcnt lgkmcnt(10)\n\t" \
  "v_mfma_f32_16x16x32_bf16 v[176:179], v[232:235], v[36:39], v[176:179]\n\t" \
  "v_mfma_f32_16x16x32_bf16 v[180:183], v[232:235], v[100:103], v[180:183]\n\t" \
  "ds_read_b64_tr_b16 v[232:233], v240 offset:27776\n\t" \
  "ds_read_b64_tr_b16 v[234:235], v240 offset:32384\n\t" \
  "s_waitcnt lgkmcnt(10)\n\t" \
  "v_mfma_f32_16x16x32_bf16 v[184:187], v[236:239], v[36:39], v[184:187]\n\t" \
  "v_mfma_f32_16x16x32_bf16 v[188:191], v[236:239], v[100:103], v[188:191]\n\t" \
  "s_add_u32 s52, s63, 12288\n\t" \
  "buffer_load_dwordx4 v[116:119], %[avo], s[40:43], s52 offen offset:1024\n\t" \
  "ds_read_b64_tr_b16 v[236:237], v240 offset:27808\n\t" \
  "ds_read_b64_tr_b16 v[238:239], v240 offset:32416\n\t" \
  "s_waitcnt vmcnt(9)\n\t" \
  "s_waitcnt lgkmcnt(10)\n\t" \
  "v_mfma_f32_16x16x32_bf16 v[144:147], v[216:219], v[40:43], v[144:147]\n\t" \
  "v_mfma_f32_16x16x32_bf16 v[148:151], v[216:219], v[104:107], v[148:151]\n\t" \
  "ds_read_b64_tr_b16 v[216:217], v240 offset:36864\n\t" \
  "ds_read_b64_tr_b16 v[218:219], v240 offset:41472\n\t" \
  "s_waitcnt lgkmcnt(10)\n\t" \
  "v_mfma_f32_16x16x32_bf16 v[152:155], v[220:223], v[40:43], v[152:155]\n\t" \
  "v_mfma_f32_16x16x32_bf16 v[156:159], v[220:223], v[104:107], v[156:159]\n\t" \
  "s_mul_i32 s51, s68, 15\n\t" \
  "s_cmp_lg_u32 s60, 7\n\t" \
  "s_cbranch_scc1 L_p8_15%=\n\t" \
  "s_add_u32 m0, s67, 138240\n\t" \
  "s_nop 0\n\t" \
  "buffer_load_dwordx4 %[wvo8], s[36:39], s51 offen lds\n\t" \
  "L_p8_15%=:\n\t" \
  "s_add_u32 m0, s66, 138240\n\t" \
  "s_nop 0\n\t" \
  "buffer_load_dwordx4 %[wvo0], s[36:39], s51 offen lds\n\t" \
  "ds_read_b64_tr_b16 v[220:221], v240 offset:36896\n\t" \
  "ds_read_b64_tr_b16 v[222:223], v240 offset:41504\n\t" \
  "s_waitcnt lgkmcnt(10)\n\t" \
  "v_mfma_f32_16x16x32_bf16 v[160:163], v[224:227], v[40:43], v[160:163]\n\t" \
  "v_mfma_f32_16x16x32_bf16 v[164:167], v[224:227], v[104:107], v[164:167]\n\t" \
  "ds_read_b64_tr_b16 v[224:225], v240 offset:36928\n\t" \
  "ds_read_b64_tr_b16 v[226:227], v240 offset:41536\n\t" \
  "s_waitcnt lgkmcnt(10)\n\t" \
  "v_mfma_f32_16x16x32_bf16 v[168:171], v[228:231], v[40:43], v[168:171]\n\t" \
  "v_mfma_f32_16x16x32_bf16 v[172:175], v[228:231], v[104:107], v[172:175]\n\t" \
  "s_mov_b32 s52, 12288\n\t" \
  "buffer_load_dwordx4 v[56:59], %[avo], s[40:43], s52 offen offset:2048\n\t" \
  "ds_read_b64_tr_b16 v[228:229], v240 offset:36960\n\t" \
  "ds_read_b64_tr_b16 v[230:231], v240 offset:41568\n\t" \
  "s_waitcnt lgkmcnt(10)\n\t" \
  "v_mfma_f32_16x16x32_bf16 v[176:179], v[232:235], v[40:43], v[176:179]\n\t" \
  "v_mfma_f32_16x16x32_bf16 v[180:183], v[232:235], v[104:107], v[180:183]\n\t" \
  "ds_read_b64_tr_b16 v[232:233], v240 offset:36992\n\t" \
  "ds_read_b64_tr_b16 v[234:235], v240 offset:41600\n\t" \
  "s_waitcnt lgkmcnt(10)\n\t" \
  "v_mfma_f32_16x16x32_bf16 v[184:187], v[236:239], v[40:43], v[184:187]\n\t" \
  "v_mfma_f32_16x16x32_bf16 v[188:191], v[236:239], v[104:107], v[188:191]\n\t" \
  "s_add_u32 s52, s63, 12288\n\t" \
  "buffer_load_dwordx4 v[120:123], %[avo], s[40:43], s52 offen offset:2048\n\t" \
  "ds_read_b64_tr_b16 v[236:237], v240 offset:37024\n\t" \
  "ds_read_b64_tr_b16 v[238:239], v240 offset:41632\n\t" \
  "s_waitcnt vmcnt(9)\n\t" \
  "s_waitcnt lgkmcnt(10)\n\t" \
  "v_mfma_f32_16x16x32_bf16 v[144:147], v[216:219], v[44:47], v[144:147]\n\t" \
  "v_mfma_f32_16x16x32_bf16 v[148:151], v[216:219], v[108:111], v[148:151]\n\t" \
  "s_waitcnt vmcnt(5)\n\t" \
  "s_barrier\n\t" \
  "ds_read_b64_tr_b16 v[216:217], v240 offset:46080\n\t" \
  "ds_read_b64_tr_b16 v[218:219], v240 offset:50688\n\t" \
  "s_waitcnt lgkmcnt(10)\n\t" \
  "v_mfma_f32_16x16x32_bf16 v[152:155], v[220:223], v[44:47], v[152:155]\n\t" \
  "v_mfma_f32_16x16x32_bf16 v[156:159], v[220:223], v[108:111], v[156:159]\n\t" \
  "s_mov_b32 s52, 12288\n\t" \
  "buffer_load_dwordx4 v[60:63], %[avo], s[40:43], s52 offen offset:3072\n\t" \
  "ds_read_b64_tr_b16 v[220:221], v240 offset:46112\n\t" \
  "ds_read_b64_tr_b16 v[222:223], v240 offset:50720\n\t" \
  "s_waitcnt lgkmcnt(10)\n\t" \
  "v_mfma_f32_16x16x32_bf16 v[160:163], v[224:227], v[44:47], v[160:163]\n\t" \
  "v_mfma_f32_16x16x32_bf16 v[164:167], v[224:227], v[108:111], v[164:167]\n\t" \
  "ds_read_b64_tr_b16 v[224:225], v240 offset:46144\n\t" \
  "ds_read_b64_tr_b16 v[226:227], v240 offset:50752\n\t" \
  "s_waitcnt lgkmcnt(10)\n\t" \
  "v_mfma_f32_16x16x32_bf16 v[168:171], v[228:231], v[44:47], v[168:171]\n\t" \
  "v_mfma_f32_16x16x32_bf16 v[172:175], v[228:231], v[108:111], v[172:175]\n\t" \
  "s_add_u32 s52, s63, 12288\n\t" \
  "buffer_load_dwordx4 v[124:127], %[avo], s[40:43], s52 offen offset:3072\n\t" \
  "ds_read_b64_tr_b16 v[228:229], v240 offset:46176\n\t" \
  "ds_read_b64_tr_b16 v[230:231], v240 offset:50784\n\t" \
  "s_waitcnt lgkmcnt(10)\n\t" \
  "v_mfma_f32_16x16x32_bf16 v[176:179], v[232:235], v[44:47], v[176:179]\n\t" \
  "v_mfma_f32_16x16x32_bf16 v[180:183], v[232:235], v[108:111], v[180:183]\n\t" \
  "ds_read_b64_tr_b16 v[232:233], v240 offset:46208\n\t" \
  "ds_read_b64_tr_b16 v[234:235], v240 offset:50816\n\t" \
  "s_waitcnt lgkmcnt(10)\n\t" \
  "v_mfma_f32_16x16x32_bf16 v[184:187], v[236:239], v[44:47], v[184:187]\n\t" \
  "v_mfma_f32_16x16x32_bf16 v[188:191], v[236:239], v[108:111], v[188:191]\n\t" \
  "ds_read_b64_tr_b16 v[236:237], v240 offset:46240\n\t" \
  "ds_read_b64_tr_b16 v[238:239], v240 offset:50848\n\t" \
  "s_waitcnt lgkmcnt(10)\n\t" \
  "v_mfma_f32_16x16x32_bf16 v[144:147], v[216:219], v[48:51], v[144:147]\n\t" \
  "v_mfma_f32_16x16x32_bf16 v[148:151], v[216:219], v[112:115], v[148:151]\n\t" \
  "ds_read_b64_tr_b16 v[216:217], v240 offset:55296\n\t" \
  "ds_read_b64_tr_b16 v[218:219], v240 offset:59904\n\t" \
  "s_waitcnt lgkmcnt(10)\n\t" \
  "v_mfma_f32_16x16x32_bf16 v[152:155], v[220:223], v[48:51], v[152:155]\n\t" \
  "v_mfma_f32_16x16x32_bf16 v[156:159], v[220:223], v[112:115], v[156:159]\n\t" \
  "ds_read_b64_tr_b16 v[220:221], v240 offset:55328\n\t" \
  "ds_read_b64_tr_b16 v[222:223], v240 offset:59936\n\t" \
  "s_waitcnt lgkmcnt(10)\n\t" \
  "v_mfma_f32_16x16x32_bf16 v[160:163], v[224:227], v[48:51], v[160:163]\n\t" \
  "v_mfma_f32_16x16x32_bf16 v[164:167], v[224:227], v[112:115], v[164:167]\n\t" \
  "ds_read_b64_tr_b16 v[224:225], v240 offset:55360\n\t" \
  "ds_read_b64_tr_b16 v[226:227], v240 offset:59968\n\t" \
  "s_waitcnt lgkmcnt(10)\n\t" \
  "v_mfma_f32_16x16x32_bf16 v[168:171], v[228:231], v[48:51], v[168:171]\n\t" \
  "v_mfma_f32_16x16x32_bf16 v[172:175], v[228:231], v[112:115], v[172:175]\n\t" \
  "ds_read_b64_tr_b16 v[228:229], v240 offset:55392\n\t" \
  "ds_read_b64_tr_b16 v[230:231], v240 offset:60000\n\t" \
  "s_waitcnt lgkmcnt(10)\n\t" \
  "v_mfma_f32_16x16x32_bf16 v[176:179], v[232:235], v[48:51], v[176:179]\n\t" \
  "v_mfma_f32_16x16x32_bf16 v[180:183], v[232:235], v[112:115], v[180:183]\n\t" \
  "ds_read_b64_tr_b16 v[232:233], v240 offset:55424\n\t" \
  "ds_read_b64_tr_b16 v[234:235], v240 offset:60032\n\t" \
  "s_waitcnt lgkmcnt(10)\n\t" \
  "v_mfma_f32_16x16x32_bf16 v[184:187], v[236:239], v[48:51], v[184:187]\n\t" \
  "v_mfma_f32_16x16x32_bf16 v[188:191], v[236:239], v[112:115], v[188:191]\n\t" \
  "ds_read_b64_tr_b16 v[236:237], v240 offset:55456\n\t" \
  "ds_read_b64_tr_b16 v[238:239], v240 offset:60064\n\t" \
  "s_waitcnt vmcnt(5)\n\t" \
  "s_waitcnt lgkmcnt(10)\n\t" \
  "v_mfma_f32_16x16x32_bf16 v[144:147], v[216:219], v[52:55], v[144:147]\n\t" \
  "v_mfma_f32_16x16x32_bf16 v[148:151], v[216:219], v[116:119], v[148:151]\n\t" \
  "v_add_u32_e32 v240, 64512, v240\n\t" \
  "ds_read_b64_tr_b16 v[216:217], v240 offset:0\n\t" \
  "ds_read_b64_tr_b16 v[218:219], v240 offset:4608\n\t" \
  "s_waitcnt lgkmcnt(10)\n\t" \
  "v_mfma_f32_16x16x32_bf16 v[152:155], v[220:223], v[52:55], v[152:155]\n\t" \
  "v_mfma_f32_16x16x32_bf16 v[156:159], v[220:223], v[116:119], v[156:159]\n\t" \
  "ds_read_b64_tr_b16 v[220:221], v240 offset:32\n\t" \
  "ds_read_b64_tr_b16 v[222:223], v240 offset:4640\n\t" \
  "s_waitcnt lgkmcnt(10)\n\t" \
  "v_mfma_f32_16x16x32_bf16 v[160:163], v[224:227], v[52:55], v[160:163]\n\t" \
  "v_mfma_f32_16x16x32_bf16 v[164:167], v[224:227], v[116:119], v[164:167]\n\t" \
  "ds_read_b64_tr_b16 v[224:225], v240 offset:64\n\t" \
  "ds_read_b64_tr_b16 v[226:227], v240 offset:4672\n\t" \
  "s_waitcnt lgkmcnt(10)\n\t" \
  "v_mfma_f32_16x16x32_bf16 v[168:171], v[228:231], v[52:55], v[168:171]\n\t" \
  "v_mfma_f32_16x16x32_bf16 v[172:175], v[228:231], v[116:119], v[172:175]\n\t" \
  "ds_read_b64_tr_b16 v[228:229], v240 offset:96\n\t" \
  "ds_read_b64_tr_b16 v[230:231], v240 offset:4704\n\t" \
  "s_waitcnt lgkmcnt(10)\n\t" \
  "v_mfma_f32_16x16x32_bf16 v[176:179], v[232:235], v[52:55], v[176:179]\n\t" \
  "v_mfma_f32_16x16x32_bf16 v[180:183], v[232:235], v[116:119], v[180:183]\n\t" \
  "ds_read_b64_tr_b16 v[232:233], v240 offset:128\n\t" \
  "ds_read_b64_tr_b16 v[234:235], v240 offset:4736\n\t" \
  "s_waitcnt lgkmcnt(10)\n\t" \
  "v_mfma_f32_16x16x32_bf16 v[184:187], v[236:239], v[52:55], v[184:187]\n\t" \
  "v_mfma_f32_16x16x32_bf16 v[188:191], v[236:239], v[116:119], v[188:191]\n\t" \
  "ds_read_b64_tr_b16 v[236:237], v240 offset:160\n\t" \
  "ds_read_b64_tr_b16 v[238:239], v240 offset:4768\n\t" \
  "s_waitcnt vmcnt(2)\n\t" \
  "s_waitcnt lgkmcnt(10)\n\t" \
  "v_mfma_f32_16x16x32_bf16 v[144:147], v[216:219], v[56:59], v[144:147]\n\t" \
  "v_mfma_f32_16x16x32_bf16 v[148:151], v[216:219], v[120:123], v[148:151]\n\t" \
  "s_barrier\n\t" \
  "ds_read_b64_tr_b16 v[216:217], v240 offset:9216\n\t" \
  "ds_read_b64_tr_b16 v[218:219], v240 offset:13824\n\t" \
  "s_waitcnt lgkmcnt(10)\n\t" \
  "v_mfma_f32_16x16x32_bf16 v[152:155], v[220:223], v[56:59], v[152:155]\n\t" \
  "v_mfma_f32_16x16x32_bf16 v[156:159], v[220:223], v[120:123], v[156:159]\n\t" \
  "ds_read_b64_tr_b16 v[220:221], v240 offset:9248\n\t" \
  "ds_read_b64_tr_b16 v[222:223], v240 offset:13856\n\t" \
  "s_waitcnt lgkmcnt(10)\n\t" \
  "v_mfma_f32_16x16x32_bf16 v[160:163], v[224:227], v[56:59], v[160:163]\n\t" \
  "v_mfma_f32_16x16x32_bf16 v[164:167], v[224:227], v[120:123], v[164:167]\n\t" \
  "ds_read_b64_tr_b16 v[224:225], v240 offset:9280\n\t" \
  "ds_read_b64_tr_b16 v[226:227], v240 offset:13888\n\t" \
  "s_waitcnt lgkmcnt(10)\n\t" \
  "v_mfma_f32_16x16x32_bf16 v[168:171], v[228:231], v[56:59], v[168:171]\n\t" \
  "v_mfma_f32_16x16x32_bf16 v[172:175], v[228:231], v[120:123], v[172:175]\n\t" \
  "ds_read_b64_tr_b16 v[228:229], v240 offset:9312\n\t" \
  "ds_read_b64_tr_b16 v[230:231], v240 offset:13920\n\t" \
  "s_waitcnt lgkmcnt(10)\n\t" \
  "v_mfma_f32_16x16x32_bf16 v[176:179], v[232:235], v[56:59], v[176:179]\n\t" \
  "v_mfma_f32_16x16x32_bf16 v[180:183], v[232:235], v[120:123], v[180:183]\n\t" \
  "ds_read_b64_tr_b16 v[232:233], v240 offset:9344\n\t" \
  "ds_read_b64_tr_b16 v[234:235], v240 offset:13952\n\t" \
  "s_waitcnt lgkmcnt(10)\n\t" \
  "v_mfma_f32_16x16x32_bf16 v[184:187], v[236:239], v[56:59], v[184:187]\n\t" \
  "v_mfma_f32_16x16x32_bf16 v[188:191], v[236:239], v[120:123], v[188:191]\n\t" \
  "ds_read_b64_tr_b16 v[236:237], v240 offset:9376\n\t" \
  "ds_read_b64_tr_b16 v[238:239], v240 offset:13984\n\t" \
  "s_waitcnt vmcnt(0)\n\t" \
  "s_waitcnt lgkmcnt(10)\n\t" \
  "v_mfma_f32_16x16x32_bf16 v[144:147], v[216:219], v[60:63], v[144:147]\n\t" \
  "v_mfma_f32_16x16x32_bf16 v[148:151], v[216:219], v[124:127], v[148:151]\n\t" \
  "v_add_u32_e32 v240, 4294838272, v240\n\t" \
  "ds_read_b64_tr_b16 v[216:217], v240 offset:192\n\t" \
  "ds_read_b64_tr_b16 v[218:219], v240 offset:4800\n\t" \
  "s_waitcnt lgkmcnt(10)\n\t" \
  "v_mfma_f32_16x16x32_bf16 v[152:155], v[220:223], v[60:63], v[152:155]\n\t" \
  "v_mfma_f32_16x16x32_bf16 v[156:159], v[220:223], v[124:127], v[156:159]\n\t" \
  "ds_read_b64_tr_b16 v[220:221], v240 offset:224\n\t" \
  "ds_read_b64_tr_b16 v[222:223], v240 offset:4832\n\t" \
  "s_waitcnt lgkmcnt(10)\n\t" \
  "v_mfma_f32_16x16x32_bf16 v[160:163], v[224:227], v[60:63], v[160:163]\n\t" \
  "v_mfma_f32_16x16x32_bf16 v[164:167], v[224:227], v[124:127], v[164:167]\n\t" \
  "ds_read_b64_tr_b16 v[224:225], v241 offset:0\n\t" \
  "ds_read_b64_tr_b16 v[226:227], v241 offset:4608\n\t" \
  "s_waitcnt lgkmcnt(10)\n\t" \
  "v_mfma_f32_16x16x32_bf16 v[168:171], v[228:231], v[60:63], v[168:171]\n\t" \
  "v_mfma_f32_16x16x32_bf16 v[172:175], v[228:231], v[124:127], v[172:175]\n\t" \
  "ds_read_b64_tr_b16 v[228:229], v240 offset:9408\n\t" \
  "ds_read_b64_tr_b16 v[230:231], v240 offset:14016\n\t" \
  "s_waitcnt lgkmcnt(10)\n\t" \
  "v_mfma_f32_16x16x32_bf16 v[176:179], v[232:235], v[60:63], v[176:179]\n\t" \
  "v_mfma_f32_16x16x32_bf16 v[180:183], v[232:235], v[124:127], v[180:183]\n\t" \
  "ds_read_b64_tr_b16 v[232:233], v240 offset:9440\n\t" \
  "ds_read_b64_tr_b16 v[234:235], v240 offset:14048\n\t" \
  "s_waitcnt lgkmcnt(10)\n\t" \
  "v_mfma_f32_16x16x32_bf16 v[184:187], v[236:239], v[60:63], v[184:187]\n\t" \
  "v_mfma_f32_16x16x32_bf16 v[188:191], v[236:239], v[124:127], v[188:191]\n\t" \
  "ds_read_b64_tr_b16 v[236:237], v241 offset:9216\n\t" \
  "ds_read_b64_tr_b16 v[238:239], v241 offset:13824\n\t" \
  "s_waitcnt lgkmcnt(10)\n\t" \
  "v_mfma_f32_16x16x32_bf16 v[192:195], v[216:219], v[0:3], 0\n\t" \
  "v_mfma_f32_16x16x32_bf16 v[196:199], v[216:219], v[64:67], 0\n\t" \
  "ds_read_b64_tr_b16 v[216:217], v240 offset:18624\n\t" \
  "ds_read_b64_tr_b16 v[218:219], v240 offset:23232\n\t" \
  "s_waitcnt lgkmcnt(10)\n\t" \
  "v_mfma_f32_16x16x32_bf16 v[200:203], v[220:223], v[0:3], 0\n\t" \
  "v_mfma_f32_16x16x32_bf16 v[204:207], v[220:223], v[64:67], 0\n\t" \
  "ds_read_b64_tr_b16 v[220:221], v240 offset:18656\n\t" \
  "ds_read_b64_tr_b16 v[222:223], v240 offset:23264\n\t" \
  "s_waitcnt lgkmcnt(10)\n\t" \
  "v_mfma_f32_16x16x32_bf16 v[208:211], v[224:227], v[0:3], 0\n\t" \
  "v_mfma_f32_16x16x32_bf16 v[212:215], v[224:227], v[64:67], 0\n\t" \
  "ds_read_b64_tr_b16 v[224:225], v241 offset:18432\n\t" \
  "ds_read_b64_tr_b16 v[226:227], v241 offset:23040\n\t" \
  "s_waitcnt lgkmcnt(10)\n\t" \
  "v_mfma_f32_16x16x32_bf16 v[192:195], v[228:231], v[4:7], v[192:195]\n\t" \
  "v_mfma_f32_16x16x32_bf16 v[196:199], v[228:231], v[68:71], v[196:199]\n\t" \
  "ds_read_b64_tr_b16 v[228:229], v240 offset:27840\n\t" \
  "ds_read_b64_tr_b16 v[230:231], v240 offset:32448\n\t" \
  "s_waitcnt lgkmcnt(10)\n\t" \
  "v_mfma_f32_16x16x32_bf16 v[200:203], v[232:235], v[4:7], v[200:203]\n\t" \
  "v_mfma_f32_16x16x32_bf16 v[204:207], v[232:235], v[68:71], v[204:207]\n\t" \
  "ds_read_b64_tr_b16 v[232:233], v240 offset:27872\n\t" \
  "ds_read_b64_tr_b16 v[234:235], v240 offset:32480\n\t" \
  "s_waitcnt lgkmcnt(10)\n\t" \
  "v_mfma_f32_16x16x32_bf16 v[208:211], v[236:239], v[4:7], v[208:211]\n\t" \
  "v_mfma_f32_16x16x32_bf16 v[212:215], v[236:239], v[68:71], v[212:215]\n\t" \
  "ds_read_b64_tr_b16 v[236:237], v241 offset:27648\n\t" \
  "ds_read_b64_tr_b16 v[238:239], v241 offset:32256\n\t" \
  "s_waitcnt lgkmcnt(10)\n\t" \
  "v_mfma_f32_16x16x32_bf16 v[192:195], v[216:219], v[8:11], v[192:195]\n\t" \
  "v_mfma_f32_16x16x32_bf16 v[196:199], v[216:219], v[72:75], v[196:199]\n\t" \
  "ds_read_b64_tr_b16 v[216:217], v240 offset:37056\n\t" \
  "ds_read_b64_tr_b16 v[218:219], v240 offset:41664\n\t" \
  "s_waitcnt lgkmcnt(10)\n\t" \
  "v_mfma_f32_16x16x32_bf16 v[200:203], v[220:223], v[8:11], v[200:203]\n\t" \
  "v_mfma_f32_16x16x32_bf16 v[204:207], v[220:223], v[72:75], v[204:207]\n\t" \
  "ds_read_b64_tr_b16 v[220:221], v240 offset:37088\n\t" \
  "ds_read_b64_tr_b16 v[222:223], v240 offset:41696\n\t" \
  "v_cvt_pk_bf16_f32 v144, v144, v145\n\t" \
  "v_cvt_pk_bf16_f32 v145, v146, v147\n\t" \
  "v_cvt_pk_bf16_f32 v146, v152, v153\n\t" \
  "v_cvt_pk_bf16_f32 v147, v154, v155\n\t" \
  "buffer_store_dwordx4 v[144:147], %[avo], s[44:47], s69 offen offset:0 sc1\n\t" \
  "s_waitcnt lgkmcnt(10)\n\t" \
  "v_mfma_f32_16x16x32_bf16 v[208:211], v[224:227], v[8:11], v[208:211]\n\t" \
  "v_mfma_f32_16x16x32_bf16 v[212:215], v[224:227], v[72:75], v[212:215]\n\t" \
  "ds_read_b64_tr_b16 v[224:225], v241 offset:36864\n\t" \
  "ds_read_b64_tr_b16 v[226:227], v241 offset:41472\n\t" \
  "s_waitcnt lgkmcnt(10)\n\t" \
  "v_mfma_f32_16x16x32_bf16 v[192:195], v[228:231], v[12:15], v[192:195]\n\t" \
  "v_mfma_f32_16x16x32_bf16 v[196:199], v[228:231], v[76:79], v[196:199]\n\t" \
  "ds_read_b64_tr_b16 v[228:229], v240 offset:46272\n\t" \
  "ds_read_b64_tr_b16 v[230:231], v240 offset:50880\n\t" \
  "s_waitcnt lgkmcnt(10)\n\t" \
  "v_mfma_f32_16x16x32_bf16 v[200:203], v[232:235], v[12:15], v[200:203]\n\t" \
  "v_mfma_f32_16x16x32_bf16 v[204:207], v[232:235], v[76:79], v[204:207]\n\t" \
  "ds_read_b64_tr_b16 v[232:233], v240 offset:46304\n\t" \
  "ds_read_b64_tr_b16 v[234:235], v240 offset:50912\n\t" \
  "s_waitcnt lgkmcnt(10)\n\t" \
  "v_mfma_f32_16x16x32_bf16 v[208:211], v[236:239], v[12:15], v[208:211]\n\t" \
  "v_mfma_f32_16x16x32_bf16 v[212:215], v[236:239], v[76:79], v[212:215]\n\t" \
  "ds_read_b64_tr_b16 v[236:237], v241 offset:46080\n\t" \
  "ds_read_b64_tr_b16 v[238:239], v241 offset:50688\n\t" \
  "s_waitcnt lgkmcnt(10)\n\t" \
  "v_mfma_f32_16x16x32_bf16 v[192:195], v[216:219], v[16:19], v[192:195]\n\t" \
  "v_mfma_f32_16x16x32_bf16 v[196:199], v[216:219], v[80:83], v[196:199]\n\t" \
  "ds_read_b64_tr_b16 v[216:217], v240 offset:55488\n\t" \
  "ds_read_b64_tr_b16 v[218:219], v240 offset:60096\n\t" \
  "s_waitcnt lgkmcnt(10)\n\t" \
  "v_mfma_f32_16x16x32_bf16 v[200:203], v[220:223], v[16:19], v[200:203]\n\t" \
  "v_mfma_f32_16x16x32_bf16 v[204:207], v[220:223], v[80:83], v[204:207]\n\t" \
  "ds_read_b64_tr_b16 v[220:221], v240 offset:55520\n\t" \
  "ds_read_b64_tr_b16 v[222:223], v240 offset:60128\n\t" \
  "s_waitcnt lgkmcnt(10)\n\t" \
  "v_mfma_f32_16x16x32_bf16 v[208:211], v[224:227], v[16:19], v[208:211]\n\t" \
  "v_mfma_f32_16x16x32_bf16 v[212:215], v[224:227], v[80:83], v[212:215]\n\t" \
  "ds_read_b64_tr_b16 v[224:225], v241 offset:55296\n\t" \
  "ds_read_b64_tr_b16 v[226:227], v241 offset:59904\n\t" \
  "v_cvt_pk_bf16_f32 v148, v148, v149\n\t" \
  "v_cvt_pk_bf16_f32 v149, v150, v151\n\t" \
  "v_cvt_pk_bf16_f32 v150, v156, v157\n\t" \
  "v_cvt_pk_bf16_f32 v151, v158, v159\n\t" \
  "buffer_store_dwordx4 v[148:151], %[avo], s[44:47], s71 offen offset:0 sc1\n\t" \
  "s_waitcnt lgkmcnt(10)\n\t" \
  "v_mfma_f32_16x16x32_bf16 v[192:195], v[228:231], v[20:23], v[192:195]\n\t" \
  "v_mfma_f32_16x16x32_bf16 v[196:199], v[228:231], v[84:87], v[196:199]\n\t" \
  "v_add_u32_e32 v240, 64512, v240\n\t" \
  "ds_read_b64_tr_b16 v[228:229], v240 offset:192\n\t" \
  "ds_read_b64_tr_b16 v[230:231], v240 offset:4800\n\t" \
  "s_waitcnt lgkmcnt(10)\n\t" \
  "v_mfma_f32_16x16x32_bf16 v[200:203], v[232:235], v[20:23], v[200:203]\n\t" \
  "v_mfma_f32_16x16x32_bf16 v[204:207], v[232:235], v[84:87], v[204:207]\n\t" \
  "ds_read_b64_tr_b16 v[232:233], v240 offset:224\n\t" \
  "ds_read_b64_tr_b16 v[234:235], v240 offset:4832\n\t" \
  "s_waitcnt lgkmcnt(10)\n\t" \
  "v_mfma_f32_16x16x32_bf16 v[208:211], v[236:239], v[20:23], v[208:211]\n\t" \
  "v_mfma_f32_16x16x32_bf16 v[212:215], v[236:239], v[84:87], v[212:215]\n\t" \
  "v_add_u32_e32 v241, 64512, v241\n\t" \
  "ds_read_b64_tr_b16 v[236:237], v241 offset:0\n\t" \
  "ds_read_b64_tr_b16 v[238:239], v241 offset:4608\n\t" \
  "s_waitcnt lgkmcnt(10)\n\t" \
  "v_mfma_f32_16x16x32_bf16 v[192:195], v[216:219], v[24:27], v[192:195]\n\t" \
  "v_mfma_f32_16x16x32_bf16 v[196:199], v[216:219], v[88:91], v[196:199]\n\t" \
  "ds_read_b64_tr_b16 v[216:217], v240 offset:9408\n\t" \
  "ds_read_b64_tr_b16 v[218:219], v240 offset:14016\n\t" \
  "s_waitcnt lgkmcnt(10)\n\t" \
  "v_mfma_f32_16x16x32_bf16 v[200:203], v[220:223], v[24:27], v[200:203]\n\t" \
  "v_mfma_f32_16x16x32_bf16 v[204:207], v[220:223], v[88:91], v[204:207]\n\t" \
  "ds_read_b64_tr_b16 v[220:221], v240 offset:9440\n\t" \
  "ds_read_b64_tr_b16 v[222:223], v240 offset:14048\n\t" \
  "s_waitcnt lgkmcnt(10)\n\t" \
  "v_mfma_f32_16x16x32_bf16 v[208:211], v[224:227], v[24:27], v[208:211]\n\t" \
  "v_mfma_f32_16x16x32_bf16 v[212:215], v[224:227], v[88:91], v[212:215]\n\t" \
  "ds_read_b64_tr_b16 v[224:225], v241 offset:9216\n\t" \
  "ds_read_b64_tr_b16 v[226:227], v241 offset:13824\n\t" \
  "s_waitcnt lgkmcnt(10)\n\t" \
  "v_mfma_f32_16x16x32_bf16 v[192:195], v[228:231], v[28:31], v[192:195]\n\t" \
  "v_mfma_f32_16x16x32_bf16 v[196:199], v[228:231], v[92:95], v[196:199]\n\t" \
  "ds_read_b64_tr_b16 v[228:229], v240 offset:18624\n\t" \
  "ds_read_b64_tr_b16 v[230:231], v240 offset:23232\n\t" \
  "v_cvt_pk_bf16_f32 v160, v160, v161\n\t" \
  "v_cvt_pk_bf16_f32 v161, v162, v163\n\t" \
  "v_cvt_pk_bf16_f32 v162, v168, v169\n\t" \
  "v_cvt_pk_bf16_f32 v163, v170, v171\n\t" \
  "buffer_store_dwordx4 v[160:163], %[avo], s[44:47], s69 offen offset:1024 sc1\n\t" \
  "s_waitcnt lgkmcnt(10)\n\t" \
  "v_mfma_f32_16x16x32_bf16 v[200:203], v[232:235], v[28:31], v[200:203]\n\t" \
  "v_mfma_f32_16x16x32_bf16 v[204:207], v[232:235], v[92:95], v[204:207]\n\t" \
  "ds_read_b64_tr_b16 v[232:233], v240 offset:18656\n\t" \
  "ds_read_b64_tr_b16 v[234:235], v240 offset:23264\n\t" \
  "s_waitcnt lgkmcnt(10)\n\t" \
  "v_mfma_f32_16x16x32_bf16 v[208:211], v[236:239], v[28:31], v[208:211]\n\t" \
  "v_mfma_f32_16x16x32_bf16 v[212:215], v[236:239], v[92:95], v[212:215]\n\t" \
  "ds_read_b64_tr_b16 v[236:237], v241 offset:18432\n\t" \
  "ds_read_b64_tr_b16 v[238:239], v241 offset:23040\n\t" \
  "s_waitcnt lgkmcnt(10)\n\t" \
  "v_mfma_f32_16x16x32_bf16 v[192:195], v[216:219], v[32:35], v[192:195]\n\t" \
  "v_mfma_f32_16x16x32_bf16 v[196:199], v[216:219], v[96:99], v[196:199]\n\t" \
  "ds_read_b64_tr_b16 v[216:217], v240 offset:27840\n\t" \
  "ds_read_b64_tr_b16 v[218:219], v240 offset:32448\n\t" \
  "s_waitcnt lgkmcnt(10)\n\t" \
  "v_mfma_f32_16x16x32_bf16 v[200:203], v[220:223], v[32:35], v[200:203]\n\t" \
  "v_mfma_f32_16x16x32_bf16 v[204:207], v[220:223], v[96:99], v[204:207]\n\t" \
  "ds_read_b64_tr_b16 v[220:221], v240 offset:27872\n\t" \
  "ds_read_b64_tr_b16 v[222:223], v240 offset:32480\n\t" \
  "s_waitcnt lgkmcnt(10)\n\t" \
  "v_mfma_f32_16x16x32_bf16 v[208:211], v[224:227], v[32:35], v[208:211]\n\t" \
  "v_mfma_f32_16x16x32_bf16 v[212:215], v[224:227], v[96:99], v[212:215]\n\t" \
  "ds_read_b64_tr_b16 v[224:225], v241 offset:27648\n\t" \
  "ds_read_b64_tr_b16 v[226:227], v241 offset:32256\n\t" \
  "s_waitcnt lgkmcnt(10)\n\t" \
  "v_mfma_f32_16x16x32_bf16 v[192:195], v[228:231], v[36:39], v[192:195]\n\t" \
  "v_mfma_f32_16x16x32_bf16 v[196:199], v[228:231], v[100:103], v[196:199]\n\t" \
  "ds_read_b64_tr_b16 v[228:229], v240 offset:37056\n\t" \
  "ds_read_b64_tr_b16 v[230:231], v240 offset:41664\n\t" \
  "s_waitcnt lgkmcnt(10)\n\t" \
  "v_mfma_f32_16x16x32_bf16 v[200:203], v[232:235], v[36:39], v[200:203]\n\t" \
  "v_mfma_f32_16x16x32_bf16 v[204:207], v[232:235], v[100:103], v[204:207]\n\t" \
  "ds_read_b64_tr_b16 v[232:233], v240 offset:37088\n\t" \
  "ds_read_b64_tr_b16 v[234:235], v240 offset:41696\n\t" \
  "v_cvt_pk_bf16_f32 v164, v164, v165\n\t" \
  "v_cvt_pk_bf16_f32 v165, v166, v167\n\t" \
  "v_cvt_pk_bf16_f32 v166, v172, v173\n\t" \
  "v_cvt_pk_bf16_f32 v167, v174, v175\n\t" \
  "buffer_store_dwordx4 v[164:167], %[avo], s[44:47], s71 offen offset:1024 sc1\n\t" \
  "s_waitcnt lgkmcnt(10)\n\t" \
  "v_mfma_f32_16x16x32_bf16 v[208:211], v[236:239], v[36:39], v[208:211]\n\t" \
  "v_mfma_f32_16x16x32_bf16 v[212:215], v[236:239], v[100:103], v[212:215]\n\t" \
  "ds_read_b64_tr_b16 v[236:237], v241 offset:36864\n\t" \
  "ds_read_b64_tr_b16 v[238:239], v241 offset:41472\n\t" \
  "s_waitcnt lgkmcnt(10)\n\t" \
  "v_mfma_f32_16x16x32_bf16 v[192:195], v[216:219], v[40:43], v[192:195]\n\t" \
  "v_mfma_f32_16x16x32_bf16 v[196:199], v[216:219], v[104:107], v[196:199]\n\t" \
  "ds_read_b64_tr_b16 v[216:217], v240 offset:46272\n\t" \
  "ds_read_b64_tr_b16 v[218:219], v240 offset:50880\n\t" \
  "s_waitcnt lgkmcnt(10)\n\t" \
  "v_mfma_f32_16x16x32_bf16 v[200:203], v[220:223], v[40:43], v[200:203]\n\t" \
  "v_mfma_f32_16x16x32_bf16 v[204:207], v[220:223], v[104:107], v[204:207]\n\t" \
  "ds_read_b64_tr_b16 v[220:221], v240 offset:46304\n\t" \
  "ds_read_b64_tr_b16 v[222:223], v240 offset:50912\n\t" \
  "s_waitcnt lgkmcnt(10)\n\t" \
  "v_mfma_f32_16x16x32_bf16 v[208:211], v[224:227], v[40:43], v[208:211]\n\t" \
  "v_mfma_f32_16x16x32_bf16 v[212:215], v[224:227], v[104:107], v[212:215]\n\t" \
  "ds_read_b64_tr_b16 v[224:225], v241 offset:46080\n\t" \
  "ds_read_b64_tr_b16 v[226:227], v241 offset:50688\n\t" \
  "s_waitcnt lgkmcnt(10)\n\t" \
  "v_mfma_f32_16x16x32_bf16 v[192:195], v[228:231], v[44:47], v[192:195]\n\t" \
  "v_mfma_f32_16x16x32_bf16 v[196:199], v[228:231], v[108:111], v[196:199]\n\t" \
  "ds_read_b64_tr_b16 v[228:229], v240 offset:55488\n\t" \
  "ds_read_b64_tr_b16 v[230:231], v240 offset:60096\n\t" \
  "s_waitcnt lgkmcnt(10)\n\t" \
  "v_mfma_f32_16x16x32_bf16 v[200:203], v[232:235], v[44:47], v[200:203]\n\t" \
  "v_mfma_f32_16x16x32_bf16 v[204:207], v[232:235], v[108:111], v[204:207]\n\t" \
  "ds_read_b64_tr_b16 v[232:233], v240 offset:55520\n\t" \
  "ds_read_b64_tr_b16 v[234:235], v240 offset:60128\n\t" \
  "s_waitcnt lgkmcnt(10)\n\t" \
  "v_mfma_f32_16x16x32_bf16 v[208:211], v[236:239], v[44:47], v[208:211]\n\t" \
  "v_mfma_f32_16x16x32_bf16 v[212:215], v[236:239], v[108:111], v[212:215]\n\t" \
  "ds_read_b64_tr_b16 v[236:237], v241 offset:55296\n\t" \
  "ds_read_b64_tr_b16 v[238:239], v241 offset:59904\n\t" \
  "v_cvt_pk_bf16_f32 v176, v176, v177\n\t" \
  "v_cvt_pk_bf16_f32 v177, v178, v179\n\t" \
  "v_cvt_pk_bf16_f32 v178, v184, v185\n\t" \
  "v_cvt_pk_bf16_f32 v179, v186, v187\n\t" \
  "buffer_store_dwordx4 v[176:179], %[avo], s[44:47], s69 offen offset:2048 sc1\n\t" \
  "s_waitcnt lgkmcnt(10)\n\t" \
  "v_mfma_f32_16x16x32_bf16 v[192:195], v[216:219], v[48:51], v[192:195]\n\t" \
  "v_mfma_f32_16x16x32_bf16 v[196:199], v[216:219], v[112:115], v[196:199]\n\t" \
  "v_add_u32_e32 v240, 64512, v240\n\t" \
  "ds_read_b64_tr_b16 v[216:217], v240 offset:192\n\t" \
  "ds_read_b64_tr_b16 v[218:219], v240 offset:4800\n\t" \
  "s_waitcnt lgkmcnt(10)\n\t" \
  "v_mfma_f32_16x16x32_bf16 v[200:203], v[220:223], v[48:51], v[200:203]\n\t" \
  "v_mfma_f32_16x16x32_bf16 v[204:207], v[220:223], v[112:115], v[204:207]\n\t" \
  "ds_read_b64_tr_b16 v[220:221], v240 offset:224\n\t" \
  "ds_read_b64_tr_b16 v[222:223], v240 offset:4832\n\t" \
  "s_waitcnt lgkmcnt(10)\n\t" \
  "v_mfma_f32_16x16x32_bf16 v[208:211], v[224:227], v[48:51], v[208:211]\n\t" \
  "v_mfma_f32_16x16x32_bf16 v[212:215], v[224:227], v[112:115], v[212:215]\n\t" \
  "v_add_u32_e32 v241, 64512, v241\n\t" \
  "ds_read_b64_tr_b16 v[224:225], v241 offset:0\n\t" \
  "ds_read_b64_tr_b16 v[226:227], v241 offset:4608\n\t" \
  "s_waitcnt lgkmcnt(10)\n\t" \
  "v_mfma_f32_16x16x32_bf16 v[192:195], v[228:231], v[52:55], v[192:195]\n\t" \
  "v_mfma_f32_16x16x32_bf16 v[196:199], v[228:231], v[116:119], v[196:199]\n\t" \
  "ds_read_b64_tr_b16 v[228:229], v240 offset:9408\n\t" \
  "ds_read_b64_tr_b16 v[230:231], v240 offset:14016\n\t" \
  "s_waitcnt lgkmcnt(10)\n\t" \
  "v_mfma_f32_16x16x32_bf16 v[200:203], v[232:235], v[52:55], v[200:203]\n\t" \
  "v_mfma_f32_16x16x32_bf16 v[204:207], v[232:235], v[116:119], v[204:207]\n\t" \
  "ds_read_b64_tr_b16 v[232:233], v240 offset:9440\n\t" \
  "ds_read_b64_tr_b16 v[234:235], v240 offset:14048\n\t" \
  "s_waitcnt lgkmcnt(10)\n\t" \
  "v_mfma_f32_16x16x32_bf16 v[208:211], v[236:239], v[52:55], v[208:211]\n\t" \
  "v_mfma_f32_16x16x32_bf16 v[212:215], v[236:239], v[116:119], v[212:215]\n\t" \
  "ds_read_b64_tr_b16 v[236:237], v241 offset:9216\n\t" \
  "ds_read_b64_tr_b16 v[238:239], v241 offset:13824\n\t" \
  "s_waitcnt lgkmcnt(10)\n\t" \
  "v_mfma_f32_16x16x32_bf16 v[192:195], v[216:219], v[56:59], v[192:195]\n\t" \
  "v_mfma_f32_16x16x32_bf16 v[196:199], v[216:219], v[120:123], v[196:199]\n\t" \
  "v_cvt_pk_bf16_f32 v180, v180, v181\n\t" \
  "v_cvt_pk_bf16_f32 v181, v182, v183\n\t" \
  "v_cvt_pk_bf16_f32 v182, v188, v189\n\t" \
  "v_cvt_pk_bf16_f32 v183, v190, v191\n\t" \
  "buffer_store_dwordx4 v[180:183], %[avo], s[44:47], s71 offen offset:2048 sc1\n\t" \
  "s_waitcnt lgkmcnt(8)\n\t" \
  "v_mfma_f32_16x16x32_bf16 v[200:203], v[220:223], v[56:59], v[200:203]\n\t" \
  "v_mfma_f32_16x16x32_bf16 v[204:207], v[220:223], v[120:123], v[204:207]\n\t" \
  "s_waitcnt lgkmcnt(6)\n\t" \
  "v_mfma_f32_16x16x32_bf16 v[208:211], v[224:227], v[56:59], v[208:211]\n\t" \
  "v_mfma_f32_16x16x32_bf16 v[212:215], v[224:227], v[120:123], v[212:215]\n\t" \
  "s_waitcnt lgkmcnt(4)\n\t" \
  "v_mfma_f32_16x16x32_bf16 v[192:195], v[228:231], v[60:63], v[192:195]\n\t" \
  "v_mfma_f32_16x16x32_bf16 v[196:199], v[228:231], v[124:127], v[196:199]\n\t" \
  "s_waitcnt lgkmcnt(2)\n\t" \
  "v_mfma_f32_16x16x32_bf16 v[200:203], v[232:235], v[60:63], v[200:203]\n\t" \
  "v_mfma_f32_16x16x32_bf16 v[204:207], v[232:235], v[124:127], v[204:207]\n\t" \
  "s_waitcnt lgkmcnt(0)\n\t" \
  "v_mfma_f32_16x16x32_bf16 v[208:211], v[236:239], v[60:63], v[208:211]\n\t" \
  "v_mfma_f32_16x16x32_bf16 v[212:215], v[236:239], v[124:127], v[212:215]\n\t" \
  "s_nop 7\n\t" \
  "v_cvt_pk_bf16_f32 v192, v192, v193\n\t" \
  "v_cvt_pk_bf16_f32 v193, v194, v195\n\t" \
  "v_cvt_pk_bf16_f32 v194, v200, v201\n\t" \
  "v_cvt_pk_bf16_f32 v195, v202, v203\n\t" \
  "buffer_store_dwordx4 v[192:195], %[avo], s[44:47], s69 offen offset:3072 sc1\n\t" \
  "v_cvt_pk_bf16_f32 v196, v196, v197\n\t" \
  "v_cvt_pk_bf16_f32 v197, v198, v199\n\t" \
  "v_cvt_pk_bf16_f32 v198, v204, v205\n\t" \
  "v_cvt_pk_bf16_f32 v199, v206, v207\n\t" \
  "buffer_store_dwordx4 v[196:199], %[avo], s[44:47], s71 offen offset:3072 sc1\n\t" \
  "v_cvt_pk_bf16_f32 v208, v208, v209\n\t" \
  "v_cvt_pk_bf16_f32 v209, v210, v211\n\t" \
  "buffer_store_dwordx2 v[208:209], %[avo], s[44:47], s70 offen sc1\n\t" \
  "v_cvt_pk_bf16_f32 v212, v212, v213\n\t" \
  "v_cvt_pk_bf16_f32 v213, v214, v215\n\t" \
  "buffer_store_dwordx2 v[212:213], %[avo], s[44:47], s72 offen sc1\n\t" \
  ""
#define GEMM_P16_CLOB "memory","vcc","scc","v0","v1","v2","v3","v4","v5","v6","v7","v8","v9","v10","v11","v12","v13","v14","v15","v16","v17","v18","v19","v20","v21","v22","v23","v24","v25","v26","v27","v28","v29","v30","v31","v32","v33","v34","v35","v36","v37","v38","v39","v40","v41","v42","v43","v44","v45","v46","v47","v48","v49","v50","v51","v52","v53","v54","v55","v56","v57","v58","v59","v60","v61","v62","v63","v64","v65","v66","v67","v68","v69","v70","v71","v72","v73","v74","v75","v76","v77","v78","v79","v80","v81","v82","v83","v84","v85","v86","v87","v88","v89","v90","v91","v92","v93","v94","v95","v96","v97","v98","v99","v100","v101","v102","v103","v104","v105","v106","v107","v108","v109","v110","v111","v112","v113","v114","v115","v116","v117","v118","v119","v120","v121","v122","v123","v124","v125","v126","v127","v128","v129","v130","v131","v132","v133","v134","v135","v136","v137","v138","v139","v140","v141","v142","v143","v144","v145","v146","v147","v148","v149","v150","v151","v152","v153","v154","v155","v156","v157","v158","v159","v160","v161","v162","v163","v164","v165","v166","v167","v168","v169","v170","v171","v172","v173","v174","v175","v176","v177","v178","v179","v180","v181","v182","v183","v184","v185","v186","v187","v188","v189","v190","v191","v192","v193","v194","v195","v196","v197","v198","v199","v200","v201","v202","v203","v204","v205","v206","v207","v208","v209","v210","v211","v212","v213","v214","v215","v216","v217","v218","v219","v220","v221","v222","v223","v224","v225","v226","v227","v228","v229","v230","v231","v232","v233","v234","v235","v236","v237","v238","v239","v240","v241","s36","s37","s38","s39","s40","s41","s42","s43","s44","s45","s46","s47","s48","s49","s50","s51","s52","s53","s54","s55","s56","s57","s58","s59","s60","s61","s62","s63","s64","s65","s66","s67","s68","s69","s70","s71","s72","s73"
#define GEMM_QKV_TEXT \
  "s_mov_b32 s77, %[wid]\n\t" \
  "s_mov_b32 s78, %[lds0]\n\t" \
  "s_mov_b64 s[36:37], %[wk]\n\t" \
  "s_and_b32 s37, s37, 0xffff\n\t" \
  "s_mov_b32 s38, 0x7fffffff\n\t" \
  "s_mov_b32 s39, 0x20000\n\t" \
  "s_mov_b64 s[48:49], %[ckv]\n\t" \
  "s_and_b32 s49, s49, 0xffff\n\t" \
  "s_mov_b32 s50, 0x7fffffff\n\t" \
  "s_mov_b32 s51, 0x20000\n\t" \
  "s_lshl_b32 s68, s77, 10\n\t" \
  "s_add_u32 s76, s78, s68\n\t" \
  "s_lshr_b32 s68, s77, 2\n\t" \
  "s_lshl_b32 s79, s68, 7\n\t" \
  "s_lshl_b32 s80, s68, 18\n\t" \
  "s_cmp_ge_u32 s77, 4\n\t" \
  "s_cselect_b64 vcc, -1, 0\n\t" \
  "s_add_u32 s71, s79, 0\n\t" \
  "s_add_u32 m0, s76, 0\n\t" \
  "s_nop 0\n\t" \
  "buffer_load_dwordx4 %[wvo], s[36:39], s71 offen lds\n\t" \
  "s_add_u32 s71, s79, 262144\n\t" \
  "s_add_u32 m0, s76, 8192\n\t" \
  "s_nop 0\n\t" \
  "buffer_load_dwordx4 %[wvo], s[36:39], s71 offen lds\n\t" \
  "s_mov_b32 s72, 0\n\t" \
  "buffer_load_dwordx4 v[0:3], %[avo], s[48:51], s72 offen offset:0\n\t" \
  "s_mov_b32 s72, 16384\n\t" \
  "buffer_load_dwordx4 v[72:75], %[avo], s[48:51], s72 offen offset:0\n\t" \
  "s_add_u32 s71, s79, 524288\n\t" \
  "s_add_u32 m0, s76, 16384\n\t" \
  "s_nop 0\n\t" \
  "buffer_load_dwordx4 %[wvo], s[36:39], s71 offen lds\n\t" \
  "s_add_u32 s71, s79, 786432\n\t" \
  "s_add_u32 m0, s76, 24576\n\t" \
  "s_nop 0\n\t" \
  "buffer_load_dwordx4 %[wvo], s[36:39], s71 offen lds\n\t" \
  "s_mov_b32 s72, 0\n\t" \
  "buffer_load_dwordx4 v[4:7], %[avo], s[48:51], s72 offen offset:1024\n\t" \
  "s_mov_b32 s72, 16384\n\t" \
  "buffer_load_dwordx4 v[76:79], %[avo], s[48:51], s72 offen offset:1024\n\t" \
  "s_mov_b32 s72, 0\n\t" \
  "buffer_load_dwordx4 v[8:11], %[avo], s[48:51], s72 offen offset:2048\n\t" \
  "s_mov_b32 s72, 16384\n\t" \
  "buffer_load_dwordx4 v[80:83], %[avo], s[48:51], s72 offen offset:2048\n\t" \
  "s_mov_b32 s72, 0\n\t" \
  "buffer_load_dwordx4 v[12:15], %[avo], s[48:51], s72 offen offset:3072\n\t" \
  "s_mov_b32 s72, 16384\n\t" \
  "buffer_load_dwordx4 v[84:87], %[avo], s[48:51], s72 offen offset:3072\n\t" \
  "s_add_u32 s71, s79, 1048576\n\t" \
  "s_add_u32 m0, s76, 32768\n\t" \
  "s_nop 0\n\t" \
  "buffer_load_dwordx4 %[wvo], s[36:39], s71 offen lds\n\t" \
  "s_add_u32 s71, s79, 1310720\n\t" \
  "s_add_u32 m0, s76, 40960\n\t" \
  "s_nop 0\n\t" \
  "buffer_load_dwordx4 %[wvo], s[36:39], s71 offen lds\n\t" \
  "s_add_u32 s71, s79, 1572864\n\t" \
  "s_add_u32 m0, s76, 49152\n\t" \
  "s_nop 0\n\t" \
  "buffer_load_dwordx4 %[wvo], s[36:39], s71 offen lds\n\t" \
  "s_add_u32 s71, s79, 1835008\n\t" \
  "s_add_u32 m0, s76, 57344\n\t" \
  "s_nop 0\n\t" \
  "buffer_load_dwordx4 %[wvo], s[36:39], s71 offen lds\n\t" \
  "s_mov_b64 s[40:41], %[wv]\n\t" \
  "s_and_b32 s41, s41, 0xffff\n\t" \
  "s_mov_b32 s42, 0x7fffffff\n\t" \
  "s_mov_b32 s43, 0x20000\n\t" \
  "s_mov_b64 s[44:45], %[wq]\n\t" \
  "s_and_b32 s45, s45, 0xffff\n\t" \
  "s_mov_b32 s46, 0x7fffffff\n\t" \
  "s_mov_b32 s47, 0x20000\n\t" \
  "s_mov_b64 s[52:53], %[ln1]\n\t" \
  "s_and_b32 s53, s53, 0xffff\n\t" \
  "s_mov_b32 s54, 0x7fffffff\n\t" \
  "s_mov_b32 s55, 0x20000\n\t" \
  "s_mov_b64 s[56:57], %[ko]\n\t" \
  "s_and_b32 s57, s57, 0xffff\n\t" \
  "s_mov_b32 s58, 0x7fffffff\n\t" \
  "s_mov_b32 s59, 0x20000\n\t" \
  "s_mov_b64 s[60:61], %[vo]\n\t" \
  "s_and_b32 s61, s61, 0xffff\n\t" \
  "s_mov_b32 s62, 0x7fffffff\n\t" \
  "s_mov_b32 s63, 0x20000\n\t" \
  "s_mov_b64 s[64:65], %[qo]\n\t" \
  "s_and_b32 s65, s65, 0xffff\n\t" \
  "s_mov_b32 s66, 0x7fffffff\n\t" \
  "s_mov_b32 s67, 0x20000\n\t" \
  "s_mov_b32 s81, 4096\n\t" \
  "s_mov_b32 s82, 1040356334\n\t" \
  "v_xor_b32_e32 v240, 0, %[trx]\n\t" \
  "v_lshl_add_u32 v236, v240, 5, %[trb]\n\t" \
  "v_xor_b32_e32 v240, 1, %[trx]\n\t" \
  "v_lshl_add_u32 v237, v240, 5, %[trb]\n\t" \
  "v_xor_b32_e32 v240, 2, %[trx]\n\t" \
  "v_lshl_add_u32 v238, v240, 5, %[trb]\n\t" \
  "v_xor_b32_e32 v240, 3, %[trx]\n\t" \
  "v_lshl_add_u32 v239, v240, 5, %[trb]\n\t" \
  "s_waitcnt vmcnt(10)\n\t" \
  "s_barrier\n\t" \
  "ds_read_b64_tr_b16 v[208:209], v236 offset:0\n\t" \
  "ds_read_b64_tr_b16 v[210:211], v236 offset:2048\n\t" \
  "ds_read_b64_tr_b16 v[212:213], v237 offset:0\n\t" \
  "ds_read_b64_tr_b16 v[214:215], v237 offset:2048\n\t" \
  "ds_read_b64_tr_b16 v[216:217], v238 offset:0\n\t" \
  "ds_read_b64_tr_b16 v[218:219], v238 offset:2048\n\t" \
  "ds_read_b64_tr_b16 v[220:221], v239 offset:0\n\t" \
  "ds_read_b64_tr_b16 v[222:223], v239 offset:2048\n\t" \
  "ds_read_b64_tr_b16 v[224:225], v236 offset:4096\n\t" \
  "ds_read_b64_tr_b16 v[226:227], v236 offset:6144\n\t" \
  "ds_read_b64_tr_b16 v[228:229], v237 offset:4096\n\t" \
  "ds_read_b64_tr_b16 v[230:231], v237 offset:6144\n\t" \
  "s_waitcnt lgkmcnt(10)\n\t" \
  "v_mfma_f32_16x16x32_bf16 v[144:147], v[208:211], v[0:3], 0\n\t" \
  "v_mfma_f32_16x16x32_bf16 v[148:151], v[208:211], v[72:75], 0\n\t" \
  "ds_read_b64_tr_b16 v[208:209], v238 offset:4096\n\t" \
  "ds_read_b64_tr_b16 v[210:211], v238 offset:6144\n\t" \
  "s_waitcnt lgkmcnt(10)\n\t" \
  "v_mfma_f32_16x16x32_bf16 v[152:155], v[212:215], v[0:3], 0\n\t" \
  "v_mfma_f32_16x16x32_bf16 v[156:159], v[212:215], v[72:75], 0\n\t" \
  "s_mov_b32 s72, 4096\n\t" \
  "buffer_load_dwordx4 v[16:19], %[avo], s[48:51], s72 offen offset:0\n\t" \
  "s_add_u32 s71, s79, 2097152\n\t" \
  "s_add_u32 m0, s76, 65536\n\t" \
  "s_nop 0\n\t" \
  "buffer_load_dwordx4 %[wvo], s[36:39], s71 offen lds\n\t" \
  "ds_read_b64_tr_b16 v[212:213], v239 offset:4096\n\t" \
  "ds_read_b64_tr_b16 v[214:215], v239 offset:6144\n\t" \
  "s_waitcnt lgkmcnt(10)\n\t" \
  "v_mfma_f32_16x16x32_bf16 v[160:163], v[216:219], v[0:3], 0\n\t" \
  "v_mfma_f32_16x16x32_bf16 v[164:167], v[216:219], v[72:75], 0\n\t" \
  "ds_read_b64_tr_b16 v[216:217], v236 offset:8192\n\t" \
  "ds_read_b64_tr_b16 v[218:219], v236 offset:10240\n\t" \
  "s_waitcnt lgkmcnt(10)\n\t" \
  "v_mfma_f32_16x16x32_bf16 v[168:171], v[220:223], v[0:3], 0\n\t" \
  "v_mfma_f32_16x16x32_bf16 v[172:175], v[220:223], v[72:75], 0\n\t" \
  "ds_read_b64_tr_b16 v[220:221], v237 offset:8192\n\t" \
  "ds_read_b64_tr_b16 v[222:223], v237 offset:10240\n\t" \
  "s_waitcnt lgkmcnt(10)\n\t" \
  "v_mfma_f32_16x16x32_bf16 v[176:179], v[224:227], v[0:3], 0\n\t" \
  "v_mfma_f32_16x16x32_bf16 v[180:183], v[224:227], v[72:75], 0\n\t" \
  "ds_read_b64_tr_b16 v[224:225], v238 offset:8192\n\t" \
  "ds_read_b64_tr_b16 v[226:227], v238 offset:10240\n\t" \
  "s_waitcnt lgkmcnt(10)\n\t" \
  "v_mfma_f32_16x16x32_bf16 v[184:187], v[228:231], v[0:3], 0\n\t" \
  "v_mfma_f32_16x16x32_bf16 v[188:191], v[228:231], v[72:75], 0\n\t" \
  "s_mov_b32 s72, 20480\n\t" \
  "buffer_load_dwordx4 v[88:91], %[avo], s[48:51], s72 offen offset:0\n\t" \
  "s_add_u32 s71, s79, 2359296\n\t" \
  "s_add_u32 m0, s76, 73728\n\t" \
  "s_nop 0\n\t" \
  "buffer_load_dwordx4 %[wvo], s[36:39], s71 offen lds\n\t" \
  "ds_read_b64_tr_b16 v[228:229], v239 offset:8192\n\t" \
  "ds_read_b64_tr_b16 v[230:231], v239 offset:10240\n\t" \
  "s_waitcnt lgkmcnt(10)\n\t" \
  "v_mfma_f32_16x16x32_bf16 v[192:195], v[208:211], v[0:3], 0\n\t" \
  "v_mfma_f32_16x16x32_bf16 v[196:199], v[208:211], v[72:75], 0\n\t" \
  "ds_read_b64_tr_b16 v[208:209], v236 offset:12288\n\t" \
  "ds_read_b64_tr_b16 v[210:211], v236 offset:14336\n\t" \
  "s_waitcnt lgkmcnt(10)\n\t" \
  "v_mfma_f32_16x16x32_bf16 v[200:203], v[212:215], v[0:3], 0\n\t" \
  "v_mfma_f32_16x16x32_bf16 v[204:207], v[212:215], v[72:75], 0\n\t" \
  "ds_read_b64_tr_b16 v[212:213], v237 offset:12288\n\t" \
  "ds_read_b64_tr_b16 v[214:215], v237 offset:14336\n\t" \
  "s_waitcnt vmcnt(12)\n\t" \
  "s_waitcnt lgkmcnt(10)\n\t" \
  "v_mfma_f32_16x16x32_bf16 v[144:147], v[216:219], v[4:7], v[144:147]\n\t" \
  "v_mfma_f32_16x16x32_bf16 v[148:151], v[216:219], v[76:79], v[148:151]\n\t" \
  "ds_read_b64_tr_b16 v[216:217], v238 offset:12288\n\t" \
  "ds_read_b64_tr_b16 v[218:219], v238 offset:14336\n\t" \
  "s_waitcnt lgkmcnt(10)\n\t" \
  "v_mfma_f32_16x16x32_bf16 v[152:155], v[220:223], v[4:7], v[152:155]\n\t" \
  "v_mfma_f32_16x16x32_bf16 v[156:159], v[220:223], v[76:79], v[156:159]\n\t" \
  "s_mov_b32 s72, 4096\n\t" \
  "buffer_load_dwordx4 v[20:23], %[avo], s[48:51], s72 offen offset:1024\n\t" \
  "s_add_u32 s71, s79, 2621440\n\t" \
  "s_add_u32 m0, s76, 81920\n\t" \
  "s_nop 0\n\t" \
  "buffer_load_dwordx4 %[wvo], s[36:39], s71 offen lds\n\t" \
  "ds_read_b64_tr_b16 v[220:221], v239 offset:12288\n\t" \
  "ds_read_b64_tr_b16 v[222:223], v239 offset:14336\n\t" \
  "s_waitcnt lgkmcnt(10)\n\t" \
  "v_mfma_f32_16x16x32_bf16 v[160:163], v[224:227], v[4:7], v[160:163]\n\t" \
  "v_mfma_f32_16x16x32_bf16 v[164:167], v[224:227], v[76:79], v[164:167]\n\t" \
  "ds_read_b64_tr_b16 v[224:225], v236 offset:16384\n\t" \
  "ds_read_b64_tr_b16 v[226:227], v236 offset:18432\n\t" \
  "s_waitcnt lgkmcnt(10)\n\t" \
  "v_mfma_f32_16x16x32_bf16 v[168:171], v[228:231], v[4:7], v[168:171]\n\t" \
  "v_mfma_f32_16x16x32_bf16 v[172:175], v[228:231], v[76:79], v[172:175]\n\t" \
  "ds_read_b64_tr_b16 v[228:229], v237 offset:16384\n\t" \
  "ds_read_b64_tr_b16 v[230:231], v237 offset:18432\n\t" \
  "s_waitcnt lgkmcnt(10)\n\t" \
  "v_mfma_f32_16x16x32_bf16 v[176:179], v[208:211], v[4:7], v[176:179]\n\t" \
  "v_mfma_f32_16x16x32_bf16 v[180:183], v[208:211], v[76:79], v[180:183]\n\t" \
  "ds_read_b64_tr_b16 v[208:209], v238 offset:16384\n\t" \
  "ds_read_b64_tr_b16 v[210:211], v238 offset:18432\n\t" \
  "s_waitcnt lgkmcnt(10)\n\t" \
  "v_mfma_f32_16x16x32_bf16 v[184:187], v[212:215], v[4:7], v[184:187]\n\t" \
  "v_mfma_f32_16x16x32_bf16 v[188:191], v[212:215], v[76:79], v[188:191]\n\t" \
  "s_mov_b32 s72, 20480\n\t" \
  "buffer_load_dwordx4 v[92:95], %[avo], s[48:51], s72 offen offset:1024\n\t" \
  "s_add_u32 s71, s79, 2883584\n\t" \
  "s_add_u32 m0, s76, 90112\n\t" \
  "s_nop 0\n\t" \
  "buffer_load_dwordx4 %[wvo], s[36:39], s71 offen lds\n\t" \
  "ds_read_b64_tr_b16 v[212:213], v239 offset:16384\n\t" \
  "ds_read_b64_tr_b16 v[214:215], v239 offset:18432\n\t" \
  "s_waitcnt lgkmcnt(10)\n\t" \
  "v_mfma_f32_16x16x32_bf16 v[192:195], v[216:219], v[4:7], v[192:195]\n\t" \
  "v_mfma_f32_16x16x32_bf16 v[196:199], v[216:219], v[76:79], v[196:199]\n\t" \
  "ds_read_b64_tr_b16 v[216:217], v236 offset:20480\n\t" \
  "ds_read_b64_tr_b16 v[218:219], v236 offset:22528\n\t" \
  "s_waitcnt lgkmcnt(10)\n\t" \
  "v_mfma_f32_16x16x32_bf16 v[200:203], v[220:223], v[4:7], v[200:203]\n\t" \
  "v_mfma_f32_16x16x32_bf16 v[204:207], v[220:223], v[76:79], v[204:207]\n\t" \
  "ds_read_b64_tr_b16 v[220:221], v237 offset:20480\n\t" \
  "ds_read_b64_tr_b16 v[222:223], v237 offset:22528\n\t" \
  "s_waitcnt vmcnt(14)\n\t" \
  "s_waitcnt lgkmcnt(10)\n\t" \
  "v_mfma_f32_16x16x32_bf16 v[144:147], v[224:227], v[8:11], v[144:147]\n\t" \
  "v_mfma_f32_16x16x32_bf16 v[148:151], v[224:227], v[80:83], v[148:151]\n\t" \
  "ds_read_b64_tr_b16 v[224:225], v238 offset:20480\n\t" \
  "ds_read_b64_tr_b16 v[226:227], v238 offset:22528\n\t" \
  "s_waitcnt lgkmcnt(10)\n\t" \
  "v_mfma_f32_16x16x32_bf16 v[152:155], v[228:231], v[8:11], v[152:155]\n\t" \
  "v_mfma_f32_16x16x32_bf16 v[156:159], v[228:231], v[80:83], v[156:159]\n\t" \
  "s_mov_b32 s72, 4096\n\t" \
  "buffer_load_dwordx4 v[24:27], %[avo], s[48:51], s72 offen offset:2048\n\t" \
  "ds_read_b64_tr_b16 v[228:229], v239 offset:20480\n\t" \
  "ds_read_b64_tr_b16 v[230:231], v239 offset:22528\n\t" \
  "s_waitcnt lgkmcnt(10)\n\t" \
  "v_mfma_f32_16x16x32_bf16 v[160:163], v[208:211], v[8:11], v[160:163]\n\t" \
  "v_mfma_f32_16x16x32_bf16 v[164:167], v[208:211], v[80:83], v[164:167]\n\t" \
  "ds_read_b64_tr_b16 v[208:209], v236 offset:24576\n\t" \
  "ds_read_b64_tr_b16 v[210:211], v236 offset:26624\n\t" \
  "s_waitcnt lgkmcnt(10)\n\t" \
  "v_mfma_f32_16x16x32_bf16 v[168:171], v[212:215], v[8:11], v[168:171]\n\t" \
  "v_mfma_f32_16x16x32_bf16 v[172:175], v[212:215], v[80:83], v[172:175]\n\t" \
  "ds_read_b64_tr_b16 v[212:213], v237 offset:24576\n\t" \
  "ds_read_b64_tr_b16 v[214:215], v237 offset:26624\n\t" \
  "s_waitcnt lgkmcnt(10)\n\t" \
  "v_mfma_f32_16x16x32_bf16 v[176:179], v[216:219], v[8:11], v[176:179]\n\t" \
  "v_mfma_f32_16x16x32_bf16 v[180:183], v[216:219], v[80:83], v[180:183]\n\t" \
  "ds_read_b64_tr_b16 v[216:217], v238 offset:24576\n\t" \
  "ds_read_b64_tr_b16 v[218:219], v238 offset:26624\n\t" \
  "s_waitcnt lgkmcnt(10)\n\t" \
  "v_mfma_f32_16x16x32_bf16 v[184:187], v[220:223], v[8:11], v[184:187]\n\t" \
  "v_mfma_f32_16x16x32_bf16 v[188:191], v[220:223], v[80:83], v[188:191]\n\t" \
  "s_mov_b32 s72, 20480\n\t" \
  "buffer_load_dwordx4 v[96:99], %[avo], s[48:51], s72 offen offset:2048\n\t" \
  "ds_read_b64_tr_b16 v[220:221], v239 offset:24576\n\t" \
  "ds_read_b64_tr_b16 v[222:223], v239 offset:26624\n\t" \
  "s_waitcnt lgkmcnt(10)\n\t" \
  "v_mfma_f32_16x16x32_bf16 v[192:195], v[224:227], v[8:11], v[192:195]\n\t" \
  "v_mfma_f32_16x16x32_bf16 v[196:199], v[224:227], v[80:83], v[196:199]\n\t" \
  "ds_read_b64_tr_b16 v[224:225], v236 offset:28672\n\t" \
  "ds_read_b64_tr_b16 v[226:227], v236 offset:30720\n\t" \
  "s_waitcnt lgkmcnt(10)\n\t" \
  "v_mfma_f32_16x16x32_bf16 v[200:203], v[228:231], v[8:11], v[200:203]\n\t" \
  "v_mfma_f32_16x16x32_bf16 v[204:207], v[228:231], v[80:83], v[204:207]\n\t" \
  "ds_read_b64_tr_b16 v[228:229], v237 offset:28672\n\t" \
  "ds_read_b64_tr_b16 v[230:231], v237 offset:30720\n\t" \
  "s_waitcnt vmcnt(14)\n\t" \
  "s_waitcnt lgkmcnt(10)\n\t" \
  "v_mfma_f32_16x16x32_bf16 v[144:147], v[208:211], v[12:15], v[144:147]\n\t" \
  "v_mfma_f32_16x16x32_bf16 v[148:151], v[208:211], v[84:87], v[148:151]\n\t" \
  "ds_read_b64_tr_b16 v[208:209], v238 offset:28672\n\t" \
  "ds_read_b64_tr_b16 v[210:211], v238 offset:30720\n\t" \
  "s_waitcnt lgkmcnt(10)\n\t" \
  "v_mfma_f32_16x16x32_bf16 v[152:155], v[212:215], v[12:15], v[152:155]\n\t" \
  "v_mfma_f32_16x16x32_bf16 v[156:159], v[212:215], v[84:87], v[156:159]\n\t" \
  "s_mov_b32 s72, 4096\n\t" \
  "buffer_load_dwordx4 v[28:31], %[avo], s[48:51], s72 offen offset:3072\n\t" \
  "ds_read_b64_tr_b16 v[212:213], v239 offset:28672\n\t" \
  "ds_read_b64_tr_b16 v[214:215], v239 offset:30720\n\t" \
  "s_waitcnt lgkmcnt(10)\n\t" \
  "v_mfma_f32_16x16x32_bf16 v[160:163], v[216:219], v[12:15], v[160:163]\n\t" \
  "v_mfma_f32_16x16x32_bf16 v[164:167], v[216:219], v[84:87], v[164:167]\n\t" \
  "s_waitcnt vmcnt(11)\n\t" \
  "s_barrier\n\t" \
  "ds_read_b64_tr_b16 v[216:217], v236 offset:32768\n\t" \
  "ds_read_b64_tr_b16 v[218:219], v236 offset:34816\n\t" \
  "s_waitcnt lgkmcnt(10)\n\t" \
  "v_mfma_f32_16x16x32_bf16 v[168:171], v[220:223], v[12:15], v[168:171]\n\t" \
  "v_mfma_f32_16x16x32_bf16 v[172:175], v[220:223], v[84:87], v[172:175]\n\t" \
  "ds_read_b64_tr_b16 v[220:221], v237 offset:32768\n\t" \
  "ds_read_b64_tr_b16 v[222:223], v237 offset:34816\n\t" \
  "s_waitcnt lgkmcnt(10)\n\t" \
  "v_mfma_f32_16x16x32_bf16 v[176:179], v[224:227], v[12:15], v[176:179]\n\t" \
  "v_mfma_f32_16x16x32_bf16 v[180:183], v[224:227], v[84:87], v[180:183]\n\t" \
  "ds_read_b64_tr_b16 v[224:225], v238 offset:32768\n\t" \
  "ds_read_b64_tr_b16 v[226:227], v238 offset:34816\n\t" \
  "s_waitcnt lgkmcnt(10)\n\t" \
  "v_mfma_f32_16x16x32_bf16 v[184:187], v[228:231], v[12:15], v[184:187]\n\t" \
  "v_mfma_f32_16x16x32_bf16 v[188:191], v[228:231], v[84:87], v[188:191]\n\t" \
  "s_mov_b32 s72, 20480\n\t" \
  "buffer_load_dwordx4 v[100:103], %[avo], s[48:51], s72 offen offset:3072\n\t" \
  "s_add_u32 s71, s79, 3145728\n\t" \
  "s_add_u32 m0, s76, 98304\n\t" \
  "s_nop 0\n\t" \
  "buffer_load_dwordx4 %[wvo], s[36:39], s71 offen lds\n\t" \
  "ds_read_b64_tr_b16 v[228:229], v239 offset:32768\n\t" \
  "ds_read_b64_tr_b16 v[230:231], v239 offset:34816\n\t" \
  "s_waitcnt lgkmcnt(10)\n\t" \
  "v_mfma_f32_16x16x32_bf16 v[192:195], v[208:211], v[12:15], v[192:195]\n\t" \
  "v_mfma_f32_16x16x32_bf16 v[196:199], v[208:211], v[84:87], v[196:199]\n\t" \
  "ds_read_b64_tr_b16 v[208:209], v236 offset:36864\n\t" \
  "ds_read_b64_tr_b16 v[210:211], v236 offset:38912\n\t" \
  "s_waitcnt lgkmcnt(10)\n\t" \
  "v_mfma_f32_16x16x32_bf16 v[200:203], v[212:215], v[12:15], v[200:203]\n\t" \
  "v_mfma_f32_16x16x32_bf16 v[204:207], v[212:215], v[84:87], v[204:207]\n\t" \
  "ds_read_b64_tr_b16 v[212:213], v237 offset:36864\n\t" \
  "ds_read_b64_tr_b16 v[214:215], v237 offset:38912\n\t" \
  "s_waitcnt vmcnt(10)\n\t" \
  "s_waitcnt lgkmcnt(10)\n\t" \
  "v_mfma_f32_16x16x32_bf16 v[144:147], v[216:219], v[16:19], v[144:147]\n\t" \
  "v_mfma_f32_16x16x32_bf16 v[148:151], v[216:219], v[88:91], v[148:151]\n\t" \
  "ds_read_b64_tr_b16 v[216:217], v238 offset:36864\n\t" \
  "ds_read_b64_tr_b16 v[218:219], v238 offset:38912\n\t" \
  "s_waitcnt lgkmcnt(10)\n\t" \
  "v_mfma_f32_16x16x32_bf16 v[152:155], v[220:223], v[16:19], v[152:155]\n\t" \
  "v_mfma_f32_16x16x32_bf16 v[156:159], v[220:223], v[88:91], v[156:159]\n\t" \
  "s_mov_b32 s72, 8192\n\t" \
  "buffer_load_dwordx4 v[32:35], %[avo], s[48:51], s72 offen offset:0\n\t" \
  "s_add_u32 s71, s79, 3407872\n\t" \
  "s_add_u32 m0, s76, 106496\n\t" \
  "s_nop 0\n\t" \
  "buffer_load_dwordx4 %[wvo], s[36:39], s71 offen lds\n\t" \
  "ds_read_b64_tr_b16 v[220:221], v239 offset:36864\n\t" \
  "ds_read_b64_tr_b16 v[222:223], v239 offset:38912\n\t" \
  "s_waitcnt lgkmcnt(10)\n\t" \
  "v_mfma_f32_16x16x32_bf16 v[160:163], v[224:227], v[16:19], v[160:163]\n\t" \
  "v_mfma_f32_16x16x32_bf16 v[164:167], v[224:227], v[88:91], v[164:167]\n\t" \
  "ds_read_b64_tr_b16 v[224:225], v236 offset:40960\n\t" \
  "ds_read_b64_tr_b16 v[226:227], v236 offset:43008\n\t" \
  "s_waitcnt lgkmcnt(10)\n\t" \
  "v_mfma_f32_16x16x32_bf16 v[168:171], v[228:231], v[16:19], v[168:171]\n\t" \
  "v_mfma_f32_16x16x32_bf16 v[172:175], v[228:231], v[88:91], v[172:175]\n\t" \
  "ds_read_b64_tr_b16 v[228:229], v237 offset:40960\n\t" \
  "ds_read_b64_tr_b16 v[230:231], v237 offset:43008\n\t" \
  "s_waitcnt lgkmcnt(10)\n\t" \
  "v_mfma_f32_16x16x32_bf16 v[176:179], v[208:211], v[16:19], v[176:179]\n\t" \
  "v_mfma_f32_16x16x32_bf16 v[180:183], v[208:211], v[88:91], v[180:183]\n\t" \
  "ds_read_b64_tr_b16 v[208:209], v238 offset:40960\n\t" \
  "ds_read_b64_tr_b16 v[210:211], v238 offset:43008\n\t" \
  "s_waitcnt lgkmcnt(10)\n\t" \
  "v_mfma_f32_16x16x32_bf16 v[184:187], v[212:215], v[16:19], v[184:187]\n\t" \
  "v_mfma_f32_16x16x32_bf16 v[188:191], v[212:215], v[88:91], v[188:191]\n\t" \
  "s_mov_b32 s72, 24576\n\t" \
  "buffer_load_dwordx4 v[104:107], %[avo], s[48:51], s72 offen offset:0\n\t" \
  "s_add_u32 s71, s79, 3670016\n\t" \
  "s_add_u32 m0, s76, 114688\n\t" \
  "s_nop 0\n\t" \
  "buffer_load_dwordx4 %[wvo], s[36:39], s71 offen lds\n\t" \
  "ds_read_b64_tr_b16 v[212:213], v239 offset:40960\n\t" \
  "ds_read_b64_tr_b16 v[214:215], v239 offset:43008\n\t" \
  "s_waitcnt lgkmcnt(10)\n\t" \
  "v_mfma_f32_16x16x32_bf16 v[192:195], v[216:219], v[16:19], v[192:195]\n\t" \
  "v_mfma_f32_16x16x32_bf16 v[196:199], v[216:219], v[88:91], v[196:199]\n\t" \
  "ds_read_b64_tr_b16 v[216:217], v236 offset:45056\n\t" \
  "ds_read_b64_tr_b16 v[218:219], v236 offset:47104\n\t" \
  "s_waitcnt lgkmcnt(10)\n\t" \
  "v_mfma_f32_16x16x32_bf16 v[200:203], v[220:223], v[16:19], v[200:203]\n\t" \
  "v_mfma_f32_16x16x32_bf16 v[204:207], v[220:223], v[88:91], v[204:207]\n\t" \
  "ds_read_b64_tr_b16 v[220:221], v237 offset:45056\n\t" \
  "ds_read_b64_tr_b16 v[222:223], v237 offset:47104\n\t" \
  "s_waitcnt vmcnt(10)\n\t" \
  "s_waitcnt lgkmcnt(10)\n\t" \
  "v_mfma_f32_16x16x32_bf16 v[144:147], v[224:227], v[20:23], v[144:147]\n\t" \
  "v_mfma_f32_16x16x32_bf16 v[148:151], v[224:227], v[92:95], v[148:151]\n\t" \
  "ds_read_b64_tr_b16 v[224:225], v238 offset:45056\n\t" \
  "ds_read_b64_tr_b16 v[226:227], v238 offset:47104\n\t" \
  "s_waitcnt lgkmcnt(10)\n\t" \
  "v_mfma_f32_16x16x32_bf16 v[152:155], v[228:231], v[20:23], v[152:155]\n\t" \
  "v_mfma_f32_16x16x32_bf16 v[156:159], v[228:231], v[92:95], v[156:159]\n\t" \
  "s_mov_b32 s72, 8192\n\t" \
  "buffer_load_dwordx4 v[36:39], %[avo], s[48:51], s72 offen offset:1024\n\t" \
  "s_add_u32 s71, s79, 3932160\n\t" \
  "s_add_u32 m0, s76, 122880\n\t" \
  "s_nop 0\n\t" \
  "buffer_load_dwordx4 %[wvo], s[36:39], s71 offen lds\n\t" \
  "ds_read_b64_tr_b16 v[228:229], v239 offset:45056\n\t" \
  "ds_read_b64_tr_b16 v[230:231], v239 offset:47104\n\t" \
  "s_waitcnt lgkmcnt(10)\n\t" \
  "v_mfma_f32_16x16x32_bf16 v[160:163], v[208:211], v[20:23], v[160:163]\n\t" \
  "v_mfma_f32_16x16x32_bf16 v[164:167], v[208:211], v[92:95], v[164:167]\n\t" \
  "ds_read_b64_tr_b16 v[208:209], v236 offset:49152\n\t" \
  "ds_read_b64_tr_b16 v[210:211], v236 offset:51200\n\t" \
  "s_waitcnt lgkmcnt(10)\n\t" \
  "v_mfma_f32_16x16x32_bf16 v[168:171], v[212:215], v[20:23], v[168:171]\n\t" \
  "v_mfma_f32_16x16x32_bf16 v[172:175], v[212:215], v[92:95], v[172:175]\n\t" \
  "ds_read_b64_tr_b16 v[212:213], v237 offset:49152\n\t" \
  "ds_read_b64_tr_b16 v[214:215], v237 offset:51200\n\t" \
  "s_waitcnt lgkmcnt(10)\n\t" \
  "v_mfma_f32_16x16x32_bf16 v[176:179], v[216:219], v[20:23], v[176:179]\n\t" \
  "v_mfma_f32_16x16x32_bf16 v[180:183], v[216:219], v[92:95], v[180:183]\n\t" \
  "ds_read_b64_tr_b16 v[216:217], v238 offset:49152\n\t" \
  "ds_read_b64_tr_b16 v[218:219], v238 offset:51200\n\t" \
  "s_waitcnt lgkmcnt(10)\n\t" \
  "v_mfma_f32_16x16x32_bf16 v[184:187], v[220:223], v[20:23], v[184:187]\n\t" \
  "v_mfma_f32_16x16x32_bf16 v[188:191], v[220:223], v[92:95], v[188:191]\n\t" \
  "s_mov_b32 s72, 24576\n\t" \
  "buffer_load_dwordx4 v[108:111], %[avo], s[48:51], s72 offen offset:1024\n\t" \
  "ds_read_b64_tr_b16 v[220:221], v239 offset:49152\n\t" \
  "ds_read_b64_tr_b16 v[222:223], v239 offset:51200\n\t" \
  "s_waitcnt lgkmcnt(10)\n\t" \
  "v_mfma_f32_16x16x32_bf16 v[192:195], v[224:227], v[20:23], v[192:195]\n\t" \
  "v_mfma_f32_16x16x32_bf16 v[196:199], v[224:227], v[92:95], v[196:199]\n\t" \
  "ds_read_b64_tr_b16 v[224:225], v236 offset:53248\n\t" \
  "ds_read_b64_tr_b16 v[226:227], v236 offset:55296\n\t" \
  "s_waitcnt lgkmcnt(10)\n\t" \
  "v_mfma_f32_16x16x32_bf16 v[200:203], v[228:231], v[20:23], v[200:203]\n\t" \
  "v_mfma_f32_16x16x32_bf16 v[204:207], v[228:231], v[92:95], v[204:207]\n\t" \
  "ds_read_b64_tr_b16 v[228:229], v237 offset:53248\n\t" \
  "ds_read_b64_tr_b16 v[230:231], v237 offset:55296\n\t" \
  "s_waitcnt vmcnt(10)\n\t" \
  "s_waitcnt lgkmcnt(10)\n\t" \
  "v_mfma_f32_16x16x32_bf16 v[144:147], v[208:211], v[24:27], v[144:147]\n\t" \
  "v_mfma_f32_16x16x32_bf16 v[148:151], v[208:211], v[96:99], v[148:151]\n\t" \
  "ds_read_b64_tr_b16 v[208:209], v238 offset:53248\n\t" \
  "ds_read_b64_tr_b16 v[210:211], v238 offset:55296\n\t" \
  "s_waitcnt lgkmcnt(10)\n\t" \
  "v_mfma_f32_16x16x32_bf16 v[152:155], v[212:215], v[24:27], v[152:155]\n\t" \
  "v_mfma_f32_16x16x32_bf16 v[156:159], v[212:215], v[96:99], v[156:159]\n\t" \
  "s_mov_b32 s72, 8192\n\t" \
  "buffer_load_dwordx4 v[40:43], %[avo], s[48:51], s72 offen offset:2048\n\t" \
  "ds_read_b64_tr_b16 v[212:213], v239 offset:53248\n\t" \
  "ds_read_b64_tr_b16 v[214:215], v239 offset:55296\n\t" \
  "s_waitcnt lgkmcnt(10)\n\t" \
  "v_mfma_f32_16x16x32_bf16 v[160:163], v[216:219], v[24:27], v[160:163]\n\t" \
  "v_mfma_f32_16x16x32_bf16 v[164:167], v[216:219], v[96:99], v[164:167]\n\t" \
  "ds_read_b64_tr_b16 v[216:217], v236 offset:57344\n\t" \
  "ds_read_b64_tr_b16 v[218:219], v236 offset:59392\n\t" \
  "s_waitcnt lgkmcnt(10)\n\t" \
  "v_mfma_f32_16x16x32_bf16 v[168:171], v[220:223], v[24:27], v[168:171]\n\t" \
  "v_mfma_f32_16x16x32_bf16 v[172:175], v[220:223], v[96:99], v[172:175]\n\t" \
  "ds_read_b64_tr_b16 v[220:221], v237 offset:57344\n\t" \
  "ds_read_b64_tr_b16 v[222:223], v237 offset:59392\n\t" \
  "s_waitcnt lgkmcnt(10)\n\t" \
  "v_mfma_f32_16x16x32_bf16 v[176:179], v[224:227], v[24:27], v[176:179]\n\t" \
  "v_mfma_f32_16x16x32_bf16 v[180:183], v[224:227], v[96:99], v[180:183]\n\t" \
  "ds_read_b64_tr_b16 v[224:225], v238 offset:57344\n\t" \
  "ds_read_b64_tr_b16 v[226:227], v238 offset:59392\n\t" \
  "s_waitcnt lgkmcnt(10)\n\t" \
  "v_mfma_f32_16x16x32_bf16 v[184:187], v[228:231], v[24:27], v[184:187]\n\t" \
  "v_mfma_f32_16x16x32_bf16 v[188:191], v[228:231], v[96:99], v[188:191]\n\t" \
  "s_mov_b32 s72, 24576\n\t" \
  "buffer_load_dwordx4 v[112:115], %[avo], s[48:51], s72 offen offset:2048\n\t" \
  "ds_read_b64_tr_b16 v[228:229], v239 offset:57344\n\t" \
  "ds_read_b64_tr_b16 v[230:231], v239 offset:59392\n\t" \
  "s_waitcnt lgkmcnt(10)\n\t" \
  "v_mfma_f32_16x16x32_bf16 v[192:195], v[208:211], v[24:27], v[192:195]\n\t" \
  "v_mfma_f32_16x16x32_bf16 v[196:199], v[208:211], v[96:99], v[196:199]\n\t" \
  "ds_read_b64_tr_b16 v[208:209], v236 offset:61440\n\t" \
  "ds_read_b64_tr_b16 v[210:211], v236 offset:63488\n\t" \
  "s_waitcnt lgkmcnt(10)\n\t" \
  "v_mfma_f32_16x16x32_bf16 v[200:203], v[212:215], v[24:27], v[200:203]\n\t" \
  "v_mfma_f32_16x16x32_bf16 v[204:207], v[212:215], v[96:99], v[204:207]\n\t" \
  "ds_read_b64_tr_b16 v[212:213], v237 offset:61440\n\t" \
  "ds_read_b64_tr_b16 v[214:215], v237 offset:63488\n\t" \
  "s_waitcnt vmcnt(10)\n\t" \
  "s_waitcnt lgkmcnt(10)\n\t" \
  "v_mfma_f32_16x16x32_bf16 v[144:147], v[216:219], v[28:31], v[144:147]\n\t" \
  "v_mfma_f32_16x16x32_bf16 v[148:151], v[216:219], v[100:103], v[148:151]\n\t" \
  "ds_read_b64_tr_b16 v[216:217], v238 offset:61440\n\t" \
  "ds_read_b64_tr_b16 v[218:219], v238 offset:63488\n\t" \
  "s_waitcnt lgkmcnt(10)\n\t" \
  "v_mfma_f32_16x16x32_bf16 v[152:155], v[220:223], v[28:31], v[152:155]\n\t" \
  "v_mfma_f32_16x16x32_bf16 v[156:159], v[220:223], v[100:103], v[156:159]\n\t" \
  "s_mov_b32 s72, 8192\n\t" \
  "buffer_load_dwordx4 v[44:47], %[avo], s[48:51], s72 offen offset:3072\n\t" \
  "ds_read_b64_tr_b16 v[220:221], v239 offset:61440\n\t" \
  "ds_read_b64_tr_b16 v[222:223], v239 offset:63488\n\t" \
  "s_waitcnt lgkmcnt(10)\n\t" \
  "v_mfma_f32_16x16x32_bf16 v[160:163], v[224:227], v[28:31], v[160:163]\n\t" \
  "v_mfma_f32_16x16x32_bf16 v[164:167], v[224:227], v[100:103], v[164:167]\n\t" \
  "s_barrier\n\t" \
  "v_add_u32_e32 v236, 65536, v236\n\t" \
  "v_add_u32_e32 v237, 65536, v237\n\t" \
  "v_add_u32_e32 v238, 65536, v238\n\t" \
  "v_add_u32_e32 v239, 65536, v239\n\t" \
  "ds_read_b64_tr_b16 v[224:225], v236 offset:0\n\t" \
  "ds_read_b64_tr_b16 v[226:227], v236 offset:2048\n\t" \
  "s_waitcnt lgkmcnt(10)\n\t" \
  "v_mfma_f32_16x16x32_bf16 v[168:171], v[228:231], v[28:31], v[168:171]\n\t" \
  "v_mfma_f32_16x16x32_bf16 v[172:175], v[228:231], v[100:103], v[172:175]\n\t" \
  "ds_read_b64_tr_b16 v[228:229], v237 offset:0\n\t" \
  "ds_read_b64_tr_b16 v[230:231], v237 offset:2048\n\t" \
  "s_waitcnt lgkmcnt(10)\n\t" \
  "v_mfma_f32_16x16x32_bf16 v[176:179], v[208:211], v[28:31], v[176:179]\n\t" \
  "v_mfma_f32_16x16x32_bf16 v[180:183], v[208:211], v[100:103], v[180:183]\n\t" \
  "ds_read_b64_tr_b16 v[208:209], v238 offset:0\n\t" \
  "ds_read_b64_tr_b16 v[210:211], v238 offset:2048\n\t" \
  "s_waitcnt lgkmcnt(10)\n\t" \
  "v_mfma_f32_16x16x32_bf16 v[184:187], v[212:215], v[28:31], v[184:187]\n\t" \
  "v_mfma_f32_16x16x32_bf16 v[188:191], v[212:215], v[100:103], v[188:191]\n\t" \
  "s_mov_b32 s72, 24576\n\t" \
  "buffer_load_dwordx4 v[116:119], %[avo], s[48:51], s72 offen offset:3072\n\t" \
  "s_add_u32 s71, s79, 0\n\t" \
  "s_add_u32 m0, s76, 0\n\t" \
  "s_nop 0\n\t" \
  "buffer_load_dwordx4 %[wvo], s[40:43], s71 offen lds\n\t" \
  "ds_read_b64_tr_b16 v[212:213], v239 offset:0\n\t" \
  "ds_read_b64_tr_b16 v[214:215], v239 offset:2048\n\t" \
  "s_waitcnt lgkmcnt(10)\n\t" \
  "v_mfma_f32_16x16x32_bf16 v[192:195], v[216:219], v[28:31], v[192:195]\n\t" \
  "v_mfma_f32_16x16x32_bf16 v[196:199], v[216:219], v[100:103], v[196:199]\n\t" \
  "ds_read_b64_tr_b16 v[216:217], v236 offset:4096\n\t" \
  "ds_read_b64_tr_b16 v[218:219], v236 offset:6144\n\t" \
  "s_waitcnt lgkmcnt(10)\n\t" \
  "v_mfma_f32_16x16x32_bf16 v[200:203], v[220:223], v[28:31], v[200:203]\n\t" \
  "v_mfma_f32_16x16x32_bf16 v[204:207], v[220:223], v[100:103], v[204:207]\n\t" \
  "ds_read_b64_tr_b16 v[220:221], v237 offset:4096\n\t" \
  "ds_read_b64_tr_b16 v[222:223], v237 offset:6144\n\t" \
  "s_waitcnt vmcnt(9)\n\t" \
  "s_waitcnt lgkmcnt(10)\n\t" \
  "v_mfma_f32_16x16x32_bf16 v[144:147], v[224:227], v[32:35], v[144:147]\n\t" \
  "v_mfma_f32_16x16x32_bf16 v[148:151], v[224:227], v[104:107], v[148:151]\n\t" \
  "ds_read_b64_tr_b16 v[224:225], v238 offset:4096\n\t" \
  "ds_read_b64_tr_b16 v[226:227], v238 offset:6144\n\t" \
  "s_waitcnt lgkmcnt(10)\n\t" \
  "v_mfma_f32_16x16x32_bf16 v[152:155], v[228:231], v[32:35], v[152:155]\n\t" \
  "v_mfma_f32_16x16x32_bf16 v[156:159], v[228:231], v[104:107], v[156:159]\n\t" \
  "s_mov_b32 s72, 12288\n\t" \
  "buffer_load_dwordx4 v[48:51], %[avo], s[48:51], s72 offen offset:0\n\t" \
  "s_add_u32 s71, s79, 262144\n\t" \
  "s_add_u32 m0, s76, 8192\n\t" \
  "s_nop 0\n\t" \
  "buffer_load_dwordx4 %[wvo], s[40:43], s71 offen lds\n\t" \
  "ds_read_b64_tr_b16 v[228:229], v239 offset:4096\n\t" \
  "ds_read_b64_tr_b16 v[230:231], v239 offset:6144\n\t" \
  "s_waitcnt lgkmcnt(10)\n\t" \
  "v_mfma_f32_16x16x32_bf16 v[160:163], v[208:211], v[32:35], v[160:163]\n\t" \
  "v_mfma_f32_16x16x32_bf16 v[164:167], v[208:211], v[104:107], v[164:167]\n\t" \
  "ds_read_b64_tr_b16 v[208:209], v236 offset:8192\n\t" \
  "ds_read_b64_tr_b16 v[210:211], v236 offset:10240\n\t" \
  "s_waitcnt lgkmcnt(10)\n\t" \
  "v_mfma_f32_16x16x32_bf16 v[168:171], v[212:215], v[32:35], v[168:171]\n\t" \
  "v_mfma_f32_16x16x32_bf16 v[172:175], v[212:215], v[104:107], v[172:175]\n\t" \
  "ds_read_b64_tr_b16 v[212:213], v237 offset:8192\n\t" \
  "ds_read_b64_tr_b16 v[214:215], v237 offset:10240\n\t" \
  "s_waitcnt lgkmcnt(10)\n\t" \
  "v_mfma_f32_16x16x32_bf16 v[176:179], v[216:219], v[32:35], v[176:179]\n\t" \
  "v_mfma_f32_16x16x32_bf16 v[180:183], v[216:219], v[104:107], v[180:183]\n\t" \
  "ds_read_b64_tr_b16 v[216:217], v238 offset:8192\n\t" \
  "ds_read_b64_tr_b16 v[218:219], v238 offset:10240\n\t" \
  "s_waitcnt lgkmcnt(10)\n\t" \
  "v_mfma_f32_16x16x32_bf16 v[184:187], v[220:223], v[32:35], v[184:187]\n\t" \
  "v_mfma_f32_16x16x32_bf16 v[188:191], v[220:223], v[104:107], v[188:191]\n\t" \
  "s_mov_b32 s72, 28672\n\t" \
  "buffer_load_dwordx4 v[120:123], %[avo], s[48:51], s72 offen offset:0\n\t" \
  "s_add_u32 s71, s79, 524288\n\t" \
  "s_add_u32 m0, s76, 16384\n\t" \
  "s_nop 0\n\t" \
  "buffer_load_dwordx4 %[wvo], s[40:43], s71 offen lds\n\t" \
  "ds_read_b64_tr_b16 v[220:221], v239 offset:8192\n\t" \
  "ds_read_b64_tr_b16 v[222:223], v239 offset:10240\n\t" \
  "s_waitcnt lgkmcnt(10)\n\t" \
  "v_mfma_f32_16x16x32_bf16 v[192:195], v[224:227], v[32:35], v[192:195]\n\t" \
  "v_mfma_f32_16x16x32_bf16 v[196:199], v[224:227], v[104:107], v[196:199]\n\t" \
  "ds_read_b64_tr_b16 v[224:225], v236 offset:12288\n\t" \
  "ds_read_b64_tr_b16 v[226:227], v236 offset:14336\n\t" \
  "s_waitcnt lgkmcnt(10)\n\t" \
  "v_mfma_f32_16x16x32_bf16 v[200:203], v[228:231], v[32:35], v[200:203]\n\t" \
  "v_mfma_f32_16x16x32_bf16 v[204:207], v[228:231], v[104:107], v[204:207]\n\t" \
  "ds_read_b64_tr_b16 v[228:229], v237 offset:12288\n\t" \
  "ds_read_b64_tr_b16 v[230:231], v237 offset:14336\n\t" \
  "s_waitcnt vmcnt(9)\n\t" \
  "s_waitcnt lgkmcnt(10)\n\t" \
  "v_mfma_f32_16x16x32_bf16 v[144:147], v[208:211], v[36:39], v[144:147]\n\t" \
  "v_mfma_f32_16x16x32_bf16 v[148:151], v[208:211], v[108:111], v[148:151]\n\t" \
  "ds_read_b64_tr_b16 v[208:209], v238 offset:12288\n\t" \
  "ds_read_b64_tr_b16 v[210:211], v238 offset:14336\n\t" \
  "s_waitcnt lgkmcnt(10)\n\t" \
  "v_mfma_f32_16x16x32_bf16 v[152:155], v[212:215], v[36:39], v[152:155]\n\t" \
  "v_mfma_f32_16x16x32_bf16 v[156:159], v[212:215], v[108:111], v[156:159]\n\t" \
  "s_mov_b32 s72, 12288\n\t" \
  "buffer_load_dwordx4 v[52:55], %[avo], s[48:51], s72 offen offset:1024\n\t" \
  "s_add_u32 s71, s79, 786432\n\t" \
  "s_add_u32 m0, s76, 24576\n\t" \
  "s_nop 0\n\t" \
  "buffer_load_dwordx4 %[wvo], s[40:43], s71 offen lds\n\t" \
  "ds_read_b64_tr_b16 v[212:213], v239 offset:12288\n\t" \
  "ds_read_b64_tr_b16 v[214:215], v239 offset:14336\n\t" \
  "s_waitcnt lgkmcnt(10)\n\t" \
  "v_mfma_f32_16x16x32_bf16 v[160:163], v[216:219], v[36:39], v[160:163]\n\t" \
  "v_mfma_f32_16x16x32_bf16 v[164:167], v[216:219], v[108:111], v[164:167]\n\t" \
  "ds_read_b64_tr_b16 v[216:217], v236 offset:16384\n\t" \
  "ds_read_b64_tr_b16 v[218:219], v236 offset:18432\n\t" \
  "s_waitcnt lgkmcnt(10)\n\t" \
  "v_mfma_f32_16x16x32_bf16 v[168:171], v[220:223], v[36:39], v[168:171]\n\t" \
  "v_mfma_f32_16x16x32_bf16 v[172:175], v[220:223], v[108:111], v[172:175]\n\t" \
  "ds_read_b64_tr_b16 v[220:221], v237 offset:16384\n\t" \
  "ds_read_b64_tr_b16 v[222:223], v237 offset:18432\n\t" \
  "s_waitcnt lgkmcnt(10)\n\t" \
  "v_mfma_f32_16x16x32_bf16 v[176:179], v[224:227], v[36:39], v[176:179]\n\t" \
  "v_mfma_f32_16x16x32_bf16 v[180:183], v[224:227], v[108:111], v[180:183]\n\t" \
  "ds_read_b64_tr_b16 v[224:225], v238 offset:16384\n\t" \
  "ds_read_b64_tr_b16 v[226:227], v238 offset:18432\n\t" \
  "s_waitcnt lgkmcnt(10)\n\t" \
  "v_mfma_f32_16x16x32_bf16 v[184:187], v[228:231], v[36:39], v[184:187]\n\t" \
  "v_mfma_f32_16x16x32_bf16 v[188:191], v[228:231], v[108:111], v[188:191]\n\t" \
  "s_mov_b32 s72, 28672\n\t" \
  "buffer_load_dwordx4 v[124:127], %[avo], s[48:51], s72 offen offset:1024\n\t" \
  "ds_read_b64_tr_b16 v[228:229], v239 offset:16384\n\t" \
  "ds_read_b64_tr_b16 v[230:231], v239 offset:18432\n\t" \
  "s_waitcnt lgkmcnt(10)\n\t" \
  "v_mfma_f32_16x16x32_bf16 v[192:195], v[208:211], v[36:39], v[192:195]\n\t" \
  "v_mfma_f32_16x16x32_bf16 v[196:199], v[208:211], v[108:111], v[196:199]\n\t" \
  "ds_read_b64_tr_b16 v[208:209], v236 offset:20480\n\t" \
  "ds_read_b64_tr_b16 v[210:211], v236 offset:22528\n\t" \
  "s_waitcnt lgkmcnt(10)\n\t" \
  "v_mfma_f32_16x16x32_bf16 v[200:203], v[212:215], v[36:39], v[200:203]\n\t" \
  "v_mfma_f32_16x16x32_bf16 v[204:207], v[212:215], v[108:111], v[204:207]\n\t" \
  "ds_read_b64_tr_b16 v[212:213], v237 offset:20480\n\t" \
  "ds_read_b64_tr_b16 v[214:215], v237 offset:22528\n\t" \
  "s_waitcnt vmcnt(10)\n\t" \
  "s_waitcnt lgkmcnt(10)\n\t" \
  "v_mfma_f32_16x16x32_bf16 v[144:147], v[216:219], v[40:43], v[144:147]\n\t" \
  "v_mfma_f32_16x16x32_bf16 v[148:151], v[216:219], v[112:115], v[148:151]\n\t" \
  "ds_read_b64_tr_b16 v[216:217], v238 offset:20480\n\t" \
  "ds_read_b64_tr_b16 v[218:219], v238 offset:22528\n\t" \
  "s_waitcnt lgkmcnt(10)\n\t" \
  "v_mfma_f32_16x16x32_bf16 v[152:155], v[220:223], v[40:43], v[152:155]\n\t" \
  "v_mfma_f32_16x16x32_bf16 v[156:159], v[220:223], v[112:115], v[156:159]\n\t" \
  "s_mov_b32 s72, 12288\n\t" \
  "buffer_load_dwordx4 v[56:59], %[avo], s[48:51], s72 offen offset:2048\n\t" \
  "ds_read_b64_tr_b16 v[220:221], v239 offset:20480\n\t" \
  "ds_read_b64_tr_b16 v[222:223], v239 offset:22528\n\t" \
  "s_waitcnt lgkmcnt(10)\n\t" \
  "v_mfma_f32_16x16x32_bf16 v[160:163], v[224:227], v[40:43], v[160:163]\n\t" \
  "v_mfma_f32_16x16x32_bf16 v[164:167], v[224:227], v[112:115], v[164:167]\n\t" \
  "ds_read_b64_tr_b16 v[224:225], v236 offset:24576\n\t" \
  "ds_read_b64_tr_b16 v[226:227], v236 offset:26624\n\t" \
  "s_waitcnt lgkmcnt(10)\n\t" \
  "v_mfma_f32_16x16x32_bf16 v[168:171], v[228:231], v[40:43], v[168:171]\n\t" \
  "v_mfma_f32_16x16x32_bf16 v[172:175], v[228:231], v[112:115], v[172:175]\n\t" \
  "ds_read_b64_tr_b16 v[228:229], v237 offset:24576\n\t" \
  "ds_read_b64_tr_b16 v[230:231], v237 offset:26624\n\t" \
  "s_waitcnt lgkmcnt(10)\n\t" \
  "v_mfma_f32_16x16x32_bf16 v[176:179], v[208:211], v[40:43], v[176:179]\n\t" \
  "v_mfma_f32_16x16x32_bf16 v[180:183], v[208:211], v[112:115], v[180:183]\n\t" \
  "ds_read_b64_tr_b16 v[208:209], v238 offset:24576\n\t" \
  "ds_read_b64_tr_b16 v[210:211], v238 offset:26624\n\t" \
  "s_waitcnt lgkmcnt(10)\n\t" \
  "v_mfma_f32_16x16x32_bf16 v[184:187], v[212:215], v[40:43], v[184:187]\n\t" \
  "v_mfma_f32_16x16x32_bf16 v[188:191], v[212:215], v[112:115], v[188:191]\n\t" \
  "s_mov_b32 s72, 28672\n\t" \
  "buffer_load_dwordx4 v[128:131], %[avo], s[48:51], s72 offen offset:2048\n\t" \
  "ds_read_b64_tr_b16 v[212:213], v239 offset:24576\n\t" \
  "ds_read_b64_tr_b16 v[214:215], v239 offset:26624\n\t" \
  "s_waitcnt lgkmcnt(10)\n\t" \
  "v_mfma_f32_16x16x32_bf16 v[192:195], v[216:219], v[40:43], v[192:195]\n\t" \
  "v_mfma_f32_16x16x32_bf16 v[196:199], v[216:219], v[112:115], v[196:199]\n\t" \
  "ds_read_b64_tr_b16 v[216:217], v236 offset:28672\n\t" \
  "ds_read_b64_tr_b16 v[218:219], v236 offset:30720\n\t" \
  "s_waitcnt lgkmcnt(10)\n\t" \
  "v_mfma_f32_16x16x32_bf16 v[200:203], v[220:223], v[40:43], v[200:203]\n\t" \
  "v_mfma_f32_16x16x32_bf16 v[204:207], v[220:223], v[112:115], v[204:207]\n\t" \
  "ds_read_b64_tr_b16 v[220:221], v237 offset:28672\n\t" \
  "ds_read_b64_tr_b16 v[222:223], v237 offset:30720\n\t" \
  "s_waitcnt vmcnt(10)\n\t" \
  "s_waitcnt lgkmcnt(10)\n\t" \
  "v_mfma_f32_16x16x32_bf16 v[144:147], v[224:227], v[44:47], v[144:147]\n\t" \
  "v_mfma_f32_16x16x32_bf16 v[148:151], v[224:227], v[116:119], v[148:151]\n\t" \
  "ds_read_b64_tr_b16 v[224:225], v238 offset:28672\n\t" \
  "ds_read_b64_tr_b16 v[226:227], v238 offset:30720\n\t" \
  "s_waitcnt lgkmcnt(10)\n\t" \
  "v_mfma_f32_16x16x32_bf16 v[152:155], v[228:231], v[44:47], v[152:155]\n\t" \
  "v_mfma_f32_16x16x32_bf16 v[156:159], v[228:231], v[116:119], v[156:159]\n\t" \
  "s_mov_b32 s72, 12288\n\t" \
  "buffer_load_dwordx4 v[60:63], %[avo], s[48:51], s72 offen offset:3072\n\t" \
  "ds_read_b64_tr_b16 v[228:229], v239 offset:28672\n\t" \
  "ds_read_b64_tr_b16 v[230:231], v239 offset:30720\n\t" \
  "s_waitcnt lgkmcnt(10)\n\t" \
  "v_mfma_f32_16x16x32_bf16 v[160:163], v[208:211], v[44:47], v[160:163]\n\t" \
  "v_mfma_f32_16x16x32_bf16 v[164:167], v[208:211], v[116:119], v[164:167]\n\t" \
  "s_barrier\n\t" \
  "ds_read_b64_tr_b16 v[208:209], v236 offset:32768\n\t" \
  "ds_read_b64_tr_b16 v[210:211], v236 offset:34816\n\t" \
  "s_waitcnt lgkmcnt(10)\n\t" \
  "v_mfma_f32_16x16x32_bf16 v[168:171], v[212:215], v[44:47], v[168:171]\n\t" \
  "v_mfma_f32_16x16x32_bf16 v[172:175], v[212:215], v[116:119], v[172:175]\n\t" \
  "ds_read_b64_tr_b16 v[212:213], v237 offset:32768\n\t" \
  "ds_read_b64_tr_b16 v[214:215], v237 offset:34816\n\t" \
  "s_waitcnt lgkmcnt(10)\n\t" \
  "v_mfma_f32_16x16x32_bf16 v[176:179], v[216:219], v[44:47], v[176:179]\n\t" \
  "v_mfma_f32_16x16x32_bf16 v[180:183], v[216:219], v[116:119], v[180:183]\n\t" \
  "ds_read_b64_tr_b16 v[216:217], v238 offset:32768\n\t" \
  "ds_read_b64_tr_b16 v[218:219], v238 offset:34816\n\t" \
  "s_waitcnt lgkmcnt(10)\n\t" \
  "v_mfma_f32_16x16x32_bf16 v[184:187], v[220:223], v[44:47], v[184:187]\n\t" \
  "v_mfma_f32_16x16x32_bf16 v[188:191], v[220:223], v[116:119], v[188:191]\n\t" \
  "s_mov_b32 s72, 28672\n\t" \
  "buffer_load_dwordx4 v[132:135], %[avo], s[48:51], s72 offen offset:3072\n\t" \
  "s_add_u32 s71, s79, 1048576\n\t" \
  "s_add_u32 m0, s76, 32768\n\t" \
  "s_nop 0\n\t" \
  "buffer_load_dwordx4 %[wvo], s[40:43], s71 offen lds\n\t" \
  "ds_read_b64_tr_b16 v[220:221], v239 offset:32768\n\t" \
  "ds_read_b64_tr_b16 v[222:223], v239 offset:34816\n\t" \
  "s_waitcnt lgkmcnt(10)\n\t" \
  "v_mfma_f32_16x16x32_bf16 v[192:195], v[224:227], v[44:47], v[192:195]\n\t" \
  "v_mfma_f32_16x16x32_bf16 v[196:199], v[224:227], v[116:119], v[196:199]\n\t" \
  "ds_read_b64_tr_b16 v[224:225], v236 offset:36864\n\t" \
  "ds_read_b64_tr_b16 v[226:227], v236 offset:38912\n\t" \
  "s_waitcnt lgkmcnt(10)\n\t" \
  "v_mfma_f32_16x16x32_bf16 v[200:203], v[228:231], v[44:47], v[200:203]\n\t" \
  "v_mfma_f32_16x16x32_bf16 v[204:207], v[228:231], v[116:119], v[204:207]\n\t" \
  "ds_read_b64_tr_b16 v[228:229], v237 offset:36864\n\t" \
  "ds_read_b64_tr_b16 v[230:231], v237 offset:38912\n\t" \
  "s_waitcnt vmcnt(9)\n\t" \
  "s_waitcnt lgkmcnt(10)\n\t" \
  "v_mfma_f32_16x16x32_bf16 v[144:147], v[208:211], v[48:51], v[144:147]\n\t" \
  "v_mfma_f32_16x16x32_bf16 v[148:151], v[208:211], v[120:123], v[148:151]\n\t" \
  "ds_read_b64_tr_b16 v[208:209], v238 offset:36864\n\t" \
  "ds_read_b64_tr_b16 v[210:211], v238 offset:38912\n\t" \
  "s_waitcnt lgkmcnt(10)\n\t" \
  "v_mfma_f32_16x16x32_bf16 v[152:155], v[212:215], v[48:51], v[152:155]\n\t" \
  "v_mfma_f32_16x16x32_bf16 v[156:159], v[212:215], v[120:123], v[156:159]\n\t" \
  "s_add_u32 s71, s79, 1310720\n\t" \
  "s_add_u32 m0, s76, 40960\n\t" \
  "s_nop 0\n\t" \
  "buffer_load_dwordx4 %[wvo], s[40:43], s71 offen lds\n\t" \
  "ds_read_b64_tr_b16 v[212:213], v239 offset:36864\n\t" \
  "ds_read_b64_tr_b16 v[214:215], v239 offset:38912\n\t" \
  "s_waitcnt lgkmcnt(10)\n\t" \
  "v_mfma_f32_16x16x32_bf16 v[160:163], v[216:219], v[48:51], v[160:163]\n\t" \
  "v_mfma_f32_16x16x32_bf16 v[164:167], v[216:219], v[120:123], v[164:167]\n\t" \
  "ds_read_b64_tr_b16 v[216:217], v236 offset:40960\n\t" \
  "ds_read_b64_tr_b16 v[218:219], v236 offset:43008\n\t" \
  "s_waitcnt lgkmcnt(10)\n\t" \
  "v_mfma_f32_16x16x32_bf16 v[168:171], v[220:223], v[48:51], v[168:171]\n\t" \
  "v_mfma_f32_16x16x32_bf16 v[172:175], v[220:223], v[120:123], v[172:175]\n\t" \
  "ds_read_b64_tr_b16 v[220:221], v237 offset:40960\n\t" \
  "ds_read_b64_tr_b16 v[222:223], v237 offset:43008\n\t" \
  "s_waitcnt lgkmcnt(10)\n\t" \
  "v_mfma_f32_16x16x32_bf16 v[176:179], v[224:227], v[48:51], v[176:179]\n\t" \
  "v_mfma_f32_16x16x32_bf16 v[180:183], v[224:227], v[120:123], v[180:183]\n\t" \
  "ds_read_b64_tr_b16 v[224:225], v238 offset:40960\n\t" \
  "ds_read_b64_tr_b16 v[226:227], v238 offset:43008\n\t" \
  "s_waitcnt lgkmcnt(10)\n\t" \
  "v_mfma_f32_16x16x32_bf16 v[184:187], v[228:231], v[48:51], v[184:187]\n\t" \
  "v_mfma_f32_16x16x32_bf16 v[188:191], v[228:231], v[120:123], v[188:191]\n\t" \
  "s_add_u32 s71, s79, 1572864\n\t" \
  "s_add_u32 m0, s76, 49152\n\t" \
  "s_nop 0\n\t" \
  "buffer_load_dwordx4 %[wvo], s[40:43], s71 offen lds\n\t" \
  "ds_read_b64_tr_b16 v[228:229], v239 offset:40960\n\t" \
  "ds_read_b64_tr_b16 v[230:231], v239 offset:43008\n\t" \
  "s_waitcnt lgkmcnt(10)\n\t" \
  "v_mfma_f32_16x16x32_bf16 v[192:195], v[208:211], v[48:51], v[192:195]\n\t" \
  "v_mfma_f32_16x16x32_bf16 v[196:199], v[208:211], v[120:123], v[196:199]\n\t" \
  "ds_read_b64_tr_b16 v[208:209], v236 offset:45056\n\t" \
  "ds_read_b64_tr_b16 v[210:211], v236 offset:47104\n\t" \
  "s_waitcnt lgkmcnt(10)\n\t" \
  "v_mfma_f32_16x16x32_bf16 v[200:203], v[212:215], v[48:51], v[200:203]\n\t" \
  "v_mfma_f32_16x16x32_bf16 v[204:207], v[212:215], v[120:123], v[204:207]\n\t" \
  "ds_read_b64_tr_b16 v[212:213], v237 offset:45056\n\t" \
  "ds_read_b64_tr_b16 v[214:215], v237 offset:47104\n\t" \
  "s_waitcnt vmcnt(7)\n\t" \
  "s_waitcnt lgkmcnt(10)\n\t" \
  "v_mfma_f32_16x16x32_bf16 v[144:147], v[216:219], v[52:55], v[144:147]\n\t" \
  "v_mfma_f32_16x16x32_bf16 v[148:151], v[216:219], v[124:127], v[148:151]\n\t" \
  "ds_read_b64_tr_b16 v[216:217], v238 offset:45056\n\t" \
  "ds_read_b64_tr_b16 v[218:219], v238 offset:47104\n\t" \
  "s_waitcnt lgkmcnt(10)\n\t" \
  "v_mfma_f32_16x16x32_bf16 v[152:155], v[220:223], v[52:55], v[152:155]\n\t" \
  "v_mfma_f32_16x16x32_bf16 v[156:159], v[220:223], v[124:127], v[156:159]\n\t" \
  "s_add_u32 s71, s79, 1835008\n\t" \
  "s_add_u32 m0, s76, 57344\n\t" \
  "s_nop 0\n\t" \
  "buffer_load_dwordx4 %[wvo], s[40:43], s71 offen lds\n\t" \
  "ds_read_b64_tr_b16 v[220:221], v239 offset:45056\n\t" \
  "ds_read_b64_tr_b16 v[222:223], v239 offset:47104\n\t" \
  "s_waitcnt lgkmcnt(10)\n\t" \
  "v_mfma_f32_16x16x32_bf16 v[160:163], v[224:227], v[52:55], v[160:163]\n\t" \
  "v_mfma_f32_16x16x32_bf16 v[164:167], v[224:227], v[124:127], v[164:167]\n\t" \
  "ds_read_b64_tr_b16 v[224:225], v236 offset:49152\n\t" \
  "ds_read_b64_tr_b16 v[226:227], v236 offset:51200\n\t" \
  "s_waitcnt lgkmcnt(10)\n\t" \
  "v_mfma_f32_16x16x32_bf16 v[168:171], v[228:231], v[52:55], v[168:171]\n\t" \
  "v_mfma_f32_16x16x32_bf16 v[172:175], v[228:231], v[124:127], v[172:175]\n\t" \
  "ds_read_b64_tr_b16 v[228:229], v237 offset:49152\n\t" \
  "ds_read_b64_tr_b16 v[230:231], v237 offset:51200\n\t" \
  "s_waitcnt lgkmcnt(10)\n\t" \
  "v_mfma_f32_16x16x32_bf16 v[176:179], v[208:211], v[52:55], v[176:179]\n\t" \
  "v_mfma_f32_16x16x32_bf16 v[180:183], v[208:211], v[124:127], v[180:183]\n\t" \
  "ds_read_b64_tr_b16 v[208:209], v238 offset:49152\n\t" \
  "ds_read_b64_tr_b16 v[210:211], v238 offset:51200\n\t" \
  "s_waitcnt lgkmcnt(10)\n\t" \
  "v_mfma_f32_16x16x32_bf16 v[184:187], v[212:215], v[52:55], v[184:187]\n\t" \
  "v_mfma_f32_16x16x32_bf16 v[188:191], v[212:215], v[124:127], v[188:191]\n\t" \
  "ds_read_b64_tr_b16 v[212:213], v239 offset:49152\n\t" \
  "ds_read_b64_tr_b16 v[214:215], v239 offset:51200\n\t" \
  "s_waitcnt lgkmcnt(10)\n\t" \
  "v_mfma_f32_16x16x32_bf16 v[192:195], v[216:219], v[52:55], v[192:195]\n\t" \
  "v_mfma_f32_16x16x32_bf16 v[196:199], v[216:219], v[124:127], v[196:199]\n\t" \
  "ds_read_b64_tr_b16 v[216:217], v236 offset:53248\n\t" \
  "ds_read_b64_tr_b16 v[218:219], v236 offset:55296\n\t" \
  "s_waitcnt lgkmcnt(10)\n\t" \
  "v_mfma_f32_16x16x32_bf16 v[200:203], v[220:223], v[52:55], v[200:203]\n\t" \
  "v_mfma_f32_16x16x32_bf16 v[204:207], v[220:223], v[124:127], v[204:207]\n\t" \
  "ds_read_b64_tr_b16 v[220:221], v237 offset:53248\n\t" \
  "ds_read_b64_tr_b16 v[222:223], v237 offset:55296\n\t" \
  "s_waitcnt vmcnt(6)\n\t" \
  "s_waitcnt lgkmcnt(10)\n\t" \
  "v_mfma_f32_16x16x32_bf16 v[144:147], v[224:227], v[56:59], v[144:147]\n\t" \
  "v_mfma_f32_16x16x32_bf16 v[148:151], v[224:227], v[128:131], v[148:151]\n\t" \
  "ds_read_b64_tr_b16 v[224:225], v238 offset:53248\n\t" \
  "ds_read_b64_tr_b16 v[226:227], v238 offset:55296\n\t" \
  "s_waitcnt lgkmcnt(10)\n\t" \
  "v_mfma_f32_16x16x32_bf16 v[152:155], v[228:231], v[56:59], v[152:155]\n\t" \
  "v_mfma_f32_16x16x32_bf16 v[156:159], v[228:231], v[128:131], v[156:159]\n\t" \
  "ds_read_b64_tr_b16 v[228:229], v239 offset:53248\n\t" \
  "ds_read_b64_tr_b16 v[230:231], v239 offset:55296\n\t" \
  "s_waitcnt lgkmcnt(10)\n\t" \
  "v_mfma_f32_16x16x32_bf16 v[160:163], v[208:211], v[56:59], v[160:163]\n\t" \
  "v_mfma_f32_16x16x32_bf16 v[164:167], v[208:211], v[128:131], v[164:167]\n\t" \
  "ds_read_b64_tr_b16 v[208:209], v236 offset:57344\n\t" \
  "ds_read_b64_tr_b16 v[210:211], v236 offset:59392\n\t" \
  "s_waitcnt lgkmcnt(10)\n\t" \
  "v_mfma_f32_16x16x32_bf16 v[168:171], v[212:215], v[56:59], v[168:171]\n\t" \
  "v_mfma_f32_16x16x32_bf16 v[172:175], v[212:215], v[128:131], v[172:175]\n\t" \
  "ds_read_b64_tr_b16 v[212:213], v237 offset:57344\n\t" \
  "ds_read_b64_tr_b16 v[214:215], v237 offset:59392\n\t" \
  "s_waitcnt lgkmcnt(10)\n\t" \
  "v_mfma_f32_16x16x32_bf16 v[176:179], v[216:219], v[56:59], v[176:179]\n\t" \
  "v_mfma_f32_16x16x32_bf16 v[180:183], v[216:219], v[128:131], v[180:183]\n\t" \
  "ds_read_b64_tr_b16 v[216:217], v238 offset:57344\n\t" \
  "ds_read_b64_tr_b16 v[218:219], v238 offset:59392\n\t" \
  "s_waitcnt lgkmcnt(10)\n\t" \
  "v_mfma_f32_16x16x32_bf16 v[184:187], v[220:223], v[56:59], v[184:187]\n\t" \
  "v_mfma_f32_16x16x32_bf16 v[188:191], v[220:223], v[128:131], v[188:191]\n\t" \
  "ds_read_b64_tr_b16 v[220:221], v239 offset:57344\n\t" \
  "ds_read_b64_tr_b16 v[222:223], v239 offset:59392\n\t" \
  "s_waitcnt lgkmcnt(10)\n\t" \
  "v_mfma_f32_16x16x32_bf16 v[192:195], v[224:227], v[56:59], v[192:195]\n\t" \
  "v_mfma_f32_16x16x32_bf16 v[196:199], v[224:227], v[128:131], v[196:199]\n\t" \
  "ds_read_b64_tr_b16 v[224:225], v236 offset:61440\n\t" \
  "ds_read_b64_tr_b16 v[226:227], v236 offset:63488\n\t" \
  "s_waitcnt lgkmcnt(10)\n\t" \
  "v_mfma_f32_16x16x32_bf16 v[200:203], v[228:231], v[56:59], v[200:203]\n\t" \
  "v_mfma_f32_16x16x32_bf16 v[204:207], v[228:231], v[128:131], v[204:207]\n\t" \
  "ds_read_b64_tr_b16 v[228:229], v237 offset:61440\n\t" \
  "ds_read_b64_tr_b16 v[230:231], v237 offset:63488\n\t" \
  "s_waitcnt vmcnt(4)\n\t" \
  "s_waitcnt lgkmcnt(10)\n\t" \
  "v_mfma_f32_16x16x32_bf16 v[144:147], v[208:211], v[60:63], v[144:147]\n\t" \
  "v_mfma_f32_16x16x32_bf16 v[148:151], v[208:211], v[132:135], v[148:151]\n\t" \
  "ds_read_b64_tr_b16 v[208:209], v238 offset:61440\n\t" \
  "ds_read_b64_tr_b16 v[210:211], v238 offset:63488\n\t" \
  "s_waitcnt lgkmcnt(10)\n\t" \
  "v_mfma_f32_16x16x32_bf16 v[152:155], v[212:215], v[60:63], v[152:155]\n\t" \
  "v_mfma_f32_16x16x32_bf16 v[156:159], v[212:215], v[132:135], v[156:159]\n\t" \
  "ds_read_b64_tr_b16 v[212:213], v239 offset:61440\n\t" \
  "ds_read_b64_tr_b16 v[214:215], v239 offset:63488\n\t" \
  "s_waitcnt lgkmcnt(10)\n\t" \
  "v_mfma_f32_16x16x32_bf16 v[160:163], v[216:219], v[60:63], v[160:163]\n\t" \
  "v_mfma_f32_16x16x32_bf16 v[164:167], v[216:219], v[132:135], v[164:167]\n\t" \
  "s_barrier\n\t" \
  "v_add_u32_e32 v236, 4294901760, v236\n\t" \
  "v_add_u32_e32 v237, 4294901760, v237\n\t" \
  "v_add_u32_e32 v238, 4294901760, v238\n\t" \
  "v_add_u32_e32 v239, 4294901760, v239\n\t" \
  "ds_read_b64_tr_b16 v[216:217], v236 offset:0\n\t" \
  "ds_read_b64_tr_b16 v[218:219], v236 offset:2048\n\t" \
  "s_waitcnt lgkmcnt(10)\n\t" \
  "v_mfma_f32_16x16x32_bf16 v[168:171], v[220:223], v[60:63], v[168:171]\n\t" \
  "v_mfma_f32_16x16x32_bf16 v[172:175], v[220:223], v[132:135], v[172:175]\n\t" \
  "ds_read_b64_tr_b16 v[220:221], v237 offset:0\n\t" \
  "ds_read_b64_tr_b16 v[222:223], v237 offset:2048\n\t" \
  "s_waitcnt lgkmcnt(10)\n\t" \
  "v_mfma_f32_16x16x32_bf16 v[176:179], v[224:227], v[60:63], v[176:179]\n\t" \
  "v_mfma_f32_16x16x32_bf16 v[180:183], v[224:227], v[132:135], v[180:183]\n\t" \
  "ds_read_b64_tr_b16 v[224:225], v238 offset:0\n\t" \
  "ds_read_b64_tr_b16 v[226:227], v238 offset:2048\n\t" \
  "s_waitcnt lgkmcnt(10)\n\t" \
  "v_mfma_f32_16x16x32_bf16 v[184:187], v[228:231], v[60:63], v[184:187]\n\t" \
  "v_mfma_f32_16x16x32_bf16 v[188:191], v[228:231], v[132:135], v[188:191]\n\t" \
  "s_add_u32 s71, s79, 2097152\n\t" \
  "s_add_u32 m0, s76, 65536\n\t" \
  "s_nop 0\n\t" \
  "buffer_load_dwordx4 %[wvo], s[40:43], s71 offen lds\n\t" \
  "ds_read_b64_tr_b16 v[228:229], v239 offset:0\n\t" \
  "ds_read_b64_tr_b16 v[230:231], v239 offset:2048\n\t" \
  "s_waitcnt lgkmcnt(10)\n\t" \
  "v_mfma_f32_16x16x32_bf16 v[192:195], v[208:211], v[60:63], v[192:195]\n\t" \
  "v_mfma_f32_16x16x32_bf16 v[196:199], v[208:211], v[132:135], v[196:199]\n\t" \
  "ds_read_b64_tr_b16 v[208:209], v236 offset:4096\n\t" \
  "ds_read_b64_tr_b16 v[210:211], v236 offset:6144\n\t" \
  "s_waitcnt lgkmcnt(10)\n\t" \
  "v_mfma_f32_16x16x32_bf16 v[200:203], v[212:215], v[60:63], v[200:203]\n\t" \
  "v_mfma_f32_16x16x32_bf16 v[204:207], v[212:215], v[132:135], v[204:207]\n\t" \
  "ds_read_b64_tr_b16 v[212:213], v237 offset:4096\n\t" \
  "ds_read_b64_tr_b16 v[214:215], v237 offset:6144\n\t" \
  "s_nop 7\n\t" \
  "v_cvt_pk_bf16_f32 v64, v144, v145\n\t" \
  "v_cvt_pk_bf16_f32 v65, v146, v147\n\t" \
  "v_cvt_pk_bf16_f32 v66, v152, v153\n\t" \
  "v_cvt_pk_bf16_f32 v67, v154, v155\n\t" \
  "v_cvt_pk_bf16_f32 v68, v148, v149\n\t" \
  "v_cvt_pk_bf16_f32 v69, v150, v151\n\t" \
  "v_cvt_pk_bf16_f32 v70, v156, v157\n\t" \
  "v_cvt_pk_bf16_f32 v71, v158, v159\n\t" \
  "v_cvt_pk_bf16_f32 v136, v160, v161\n\t" \
  "v_cvt_pk_bf16_f32 v137, v162, v163\n\t" \
  "v_cvt_pk_bf16_f32 v138, v168, v169\n\t" \
  "v_cvt_pk_bf16_f32 v139, v170, v171\n\t" \
  "v_cvt_pk_bf16_f32 v140, v164, v165\n\t" \
  "v_cvt_pk_bf16_f32 v141, v166, v167\n\t" \
  "v_cvt_pk_bf16_f32 v142, v172, v173\n\t" \
  "v_cvt_pk_bf16_f32 v143, v174, v175\n\t" \
  "v_cvt_pk_bf16_f32 v240, v176, v177\n\t" \
  "v_cvt_pk_bf16_f32 v241, v178, v179\n\t" \
  "v_cvt_pk_bf16_f32 v242, v184, v185\n\t" \
  "v_cvt_pk_bf16_f32 v243, v186, v187\n\t" \
  "v_cvt_pk_bf16_f32 v244, v180, v181\n\t" \
  "v_cvt_pk_bf16_f32 v245, v182, v183\n\t" \
  "v_cvt_pk_bf16_f32 v246, v188, v189\n\t" \
  "v_cvt_pk_bf16_f32 v247, v190, v191\n\t" \
  "v_cvt_pk_bf16_f32 v192, v192, v193\n\t" \
  "v_cvt_pk_bf16_f32 v193, v194, v195\n\t" \
  "v_cvt_pk_bf16_f32 v194, v200, v201\n\t" \
  "v_cvt_pk_bf16_f32 v195, v202, v203\n\t" \
  "buffer_store_dwordx4 v[192:195], %[kvo], s[56:59], s81 offen offset:2048 sc1\n\t" \
  "v_cvt_pk_bf16_f32 v196, v196, v197\n\t" \
  "v_cvt_pk_bf16_f32 v197, v198, v199\n\t" \
  "v_cvt_pk_bf16_f32 v198, v204, v205\n\t" \
  "v_cvt_pk_bf16_f32 v199, v206, v207\n\t" \
  "buffer_store_dwordx4 v[196:199], %[kvo], s[56:59], s81 offen offset:2176 sc1\n\t" \
  "s_nop 1\n\t" \
  "s_waitcnt lgkmcnt(10)\n\t" \
  "v_mfma_f32_16x16x32_bf16 v[144:147], v[0:3], v[216:219], 0\n\t" \
  "v_mfma_f32_16x16x32_bf16 v[148:151], v[72:75], v[216:219], 0\n\t" \
  "ds_read_b64_tr_b16 v[216:217], v238 offset:4096\n\t" \
  "ds_read_b64_tr_b16 v[218:219], v238 offset:6144\n\t" \
  "s_waitcnt lgkmcnt(10)\n\t" \
  "v_mfma_f32_16x16x32_bf16 v[152:155], v[0:3], v[220:223], 0\n\t" \
  "v_mfma_f32_16x16x32_bf16 v[156:159], v[72:75], v[220:223], 0\n\t" \
  "s_add_u32 s71, s79, 2359296\n\t" \
  "s_add_u32 m0, s76, 73728\n\t" \
  "s_nop 0\n\t" \
  "buffer_load_dwordx4 %[wvo], s[40:43], s71 offen lds\n\t" \
  "ds_read_b64_tr_b16 v[220:221], v239 offset:4096\n\t" \
  "ds_read_b64_tr_b16 v[222:223], v239 offset:6144\n\t" \
  "s_waitcnt lgkmcnt(10)\n\t" \
  "v_mfma_f32_16x16x32_bf16 v[160:163], v[0:3], v[224:227], 0\n\t" \
  "v_mfma_f32_16x16x32_bf16 v[164:167], v[72:75], v[224:227], 0\n\t" \
  "ds_read_b64_tr_b16 v[224:225], v236 offset:8192\n\t" \
  "ds_read_b64_tr_b16 v[226:227], v236 offset:10240\n\t" \
  "s_waitcnt lgkmcnt(10)\n\t" \
  "v_mfma_f32_16x16x32_bf16 v[168:171], v[0:3], v[228:231], 0\n\t" \
  "v_mfma_f32_16x16x32_bf16 v[172:175], v[72:75], v[228:231], 0\n\t" \
  "buffer_store_dwordx4 v[64:67], %[kvo], s[56:59], 0 offen offset:0 sc1\n\t" \
  "ds_read_b64_tr_b16 v[228:229], v237 offset:8192\n\t" \
  "ds_read_b64_tr_b16 v[230:231], v237 offset:10240\n\t" \
  "s_waitcnt lgkmcnt(10)\n\t" \
  "v_mfma_f32_16x16x32_bf16 v[176:179], v[0:3], v[208:211], 0\n\t" \
  "v_mfma_f32_16x16x32_bf16 v[180:183], v[72:75], v[208:211], 0\n\t" \
  "ds_read_b64_tr_b16 v[208:209], v238 offset:8192\n\t" \
  "ds_read_b64_tr_b16 v[210:211], v238 offset:10240\n\t" \
  "s_waitcnt lgkmcnt(10)\n\t" \
  "v_mfma_f32_16x16x32_bf16 v[184:187], v[0:3], v[212:215], 0\n\t" \
  "v_mfma_f32_16x16x32_bf16 v[188:191], v[72:75], v[212:215], 0\n\t" \
  "s_add_u32 s71, s79, 2621440\n\t" \
  "s_add_u32 m0, s76, 81920\n\t" \
  "s_nop 0\n\t" \
  "buffer_load_dwordx4 %[wvo], s[40:43], s71 offen lds\n\t" \
  "ds_read_b64_tr_b16 v[212:213], v239 offset:8192\n\t" \
  "ds_read_b64_tr_b16 v[214:215], v239 offset:10240\n\t" \
  "s_waitcnt lgkmcnt(10)\n\t" \
  "v_mfma_f32_16x16x32_bf16 v[192:195], v[0:3], v[216:219], 0\n\t" \
  "v_mfma_f32_16x16x32_bf16 v[196:199], v[72:75], v[216:219], 0\n\t" \
  "ds_read_b64_tr_b16 v[216:217], v236 offset:12288\n\t" \
  "ds_read_b64_tr_b16 v[218:219], v236 offset:14336\n\t" \
  "s_waitcnt lgkmcnt(10)\n\t" \
  "v_mfma_f32_16x16x32_bf16 v[200:203], v[0:3], v[220:223], 0\n\t" \
  "v_mfma_f32_16x16x32_bf16 v[204:207], v[72:75], v[220:223], 0\n\t" \
  "ds_read_b64_tr_b16 v[220:221], v237 offset:12288\n\t" \
  "ds_read_b64_tr_b16 v[222:223], v237 offset:14336\n\t" \
  "s_waitcnt lgkmcnt(10)\n\t" \
  "v_mfma_f32_16x16x32_bf16 v[144:147], v[4:7], v[224:227], v[144:147]\n\t" \
  "v_mfma_f32_16x16x32_bf16 v[148:151], v[76:79], v[224:227], v[148:151]\n\t" \
  "ds_read_b64_tr_b16 v[224:225], v238 offset:12288\n\t" \
  "ds_read_b64_tr_b16 v[226:227], v238 offset:14336\n\t" \
  "s_waitcnt lgkmcnt(10)\n\t" \
  "v_mfma_f32_16x16x32_bf16 v[152:155], v[4:7], v[228:231], v[152:155]\n\t" \
  "v_mfma_f32_16x16x32_bf16 v[156:159], v[76:79], v[228:231], v[156:159]\n\t" \
  "s_mov_b32 s72, 0\n\t" \
  "buffer_load_dwordx4 v[0:3], %[avo], s[52:55], s72 offen offset:0\n\t" \
  "s_add_u32 s71, s79, 2883584\n\t" \
  "s_add_u32 m0, s76, 90112\n\t" \
  "s_nop 0\n\t" \
  "buffer_load_dwordx4 %[wvo], s[40:43], s71 offen lds\n\t" \
  "ds_read_b64_tr_b16 v[228:229], v239 offset:12288\n\t" \
  "ds_read_b64_tr_b16 v[230:231], v239 offset:14336\n\t" \
  "s_waitcnt lgkmcnt(10)\n\t" \
  "v_mfma_f32_16x16x32_bf16 v[160:163], v[4:7], v[208:211], v[160:163]\n\t" \
  "v_mfma_f32_16x16x32_bf16 v[164:167], v[76:79], v[208:211], v[164:167]\n\t" \
  "ds_read_b64_tr_b16 v[208:209], v236 offset:16384\n\t" \
  "ds_read_b64_tr_b16 v[210:211], v236 offset:18432\n\t" \
  "s_waitcnt lgkmcnt(10)\n\t" \
  "v_mfma_f32_16x16x32_bf16 v[168:171], v[4:7], v[212:215], v[168:171]\n\t" \
  "v_mfma_f32_16x16x32_bf16 v[172:175], v[76:79], v[212:215], v[172:175]\n\t" \
  "ds_read_b64_tr_b16 v[212:213], v237 offset:16384\n\t" \
  "ds_read_b64_tr_b16 v[214:215], v237 offset:18432\n\t" \
  "s_waitcnt lgkmcnt(10)\n\t" \
  "v_mfma_f32_16x16x32_bf16 v[176:179], v[4:7], v[216:219], v[176:179]\n\t" \
  "v_mfma_f32_16x16x32_bf16 v[180:183], v[76:79], v[216:219], v[180:183]\n\t" \
  "ds_read_b64_tr_b16 v[216:217], v238 offset:16384\n\t" \
  "ds_read_b64_tr_b16 v[218:219], v238 offset:18432\n\t" \
  "s_waitcnt lgkmcnt(10)\n\t" \
  "v_mfma_f32_16x16x32_bf16 v[184:187], v[4:7], v[220:223], v[184:187]\n\t" \
  "v_mfma_f32_16x16x32_bf16 v[188:191], v[76:79], v[220:223], v[188:191]\n\t" \
  "s_mov_b32 s72, 18432\n\t" \
  "buffer_load_dwordx4 v[72:75], %[avo], s[52:55], s72 offen offset:0\n\t" \
  "ds_read_b64_tr_b16 v[220:221], v239 offset:16384\n\t" \
  "ds_read_b64_tr_b16 v[222:223], v239 offset:18432\n\t" \
  "s_waitcnt lgkmcnt(10)\n\t" \
  "v_mfma_f32_16x16x32_bf16 v[192:195], v[4:7], v[224:227], v[192:195]\n\t" \
  "v_mfma_f32_16x16x32_bf16 v[196:199], v[76:79], v[224:227], v[196:199]\n\t" \
  "buffer_store_dwordx4 v[68:71], %[kvo], s[56:59], 0 offen offset:128 sc1\n\t" \
  "ds_read_b64_tr_b16 v[224:225], v236 offset:20480\n\t" \
  "ds_read_b64_tr_b16 v[226:227], v236 offset:22528\n\t" \
  "s_waitcnt lgkmcnt(10)\n\t" \
  "v_mfma_f32_16x16x32_bf16 v[200:203], v[4:7], v[228:231], v[200:203]\n\t" \
  "v_mfma_f32_16x16x32_bf16 v[204:207], v[76:79], v[228:231], v[204:207]\n\t" \
  "ds_read_b64_tr_b16 v[228:229], v237 offset:20480\n\t" \
  "ds_read_b64_tr_b16 v[230:231], v237 offset:22528\n\t" \
  "s_waitcnt lgkmcnt(10)\n\t" \
  "v_mfma_f32_16x16x32_bf16 v[144:147], v[8:11], v[208:211], v[144:147]\n\t" \
  "v_mfma_f32_16x16x32_bf16 v[148:151], v[80:83], v[208:211], v[148:151]\n\t" \
  "ds_read_b64_tr_b16 v[208:209], v238 offset:20480\n\t" \
  "ds_read_b64_tr_b16 v[210:211], v238 offset:22528\n\t" \
  "s_waitcnt lgkmcnt(10)\n\t" \
  "v_mfma_f32_16x16x32_bf16 v[152:155], v[8:11], v[212:215], v[152:155]\n\t" \
  "v_mfma_f32_16x16x32_bf16 v[156:159], v[80:83], v[212:215], v[156:159]\n\t" \
  "s_mov_b32 s72, 0\n\t" \
  "buffer_load_dwordx4 v[4:7], %[avo], s[52:55], s72 offen offset:1024\n\t" \
  "ds_read_b64_tr_b16 v[212:213], v239 offset:20480\n\t" \
  "ds_read_b64_tr_b16 v[214:215], v239 offset:22528\n\t" \
  "s_waitcnt lgkmcnt(10)\n\t" \
  "v_mfma_f32_16x16x32_bf16 v[160:163], v[8:11], v[216:219], v[160:163]\n\t" \
  "v_mfma_f32_16x16x32_bf16 v[164:167], v[80:83], v[216:219], v[164:167]\n\t" \
  "ds_read_b64_tr_b16 v[216:217], v236 offset:24576\n\t" \
  "ds_read_b64_tr_b16 v[218:219], v236 offset:26624\n\t" \
  "s_waitcnt lgkmcnt(10)\n\t" \
  "v_mfma_f32_16x16x32_bf16 v[168:171], v[8:11], v[220:223], v[168:171]\n\t" \
  "v_mfma_f32_16x16x32_bf16 v[172:175], v[80:83], v[220:223], v[172:175]\n\t" \
  "ds_read_b64_tr_b16 v[220:221], v237 offset:24576\n\t" \
  "ds_read_b64_tr_b16 v[222:223], v237 offset:26624\n\t" \
  "s_waitcnt lgkmcnt(10)\n\t" \
  "v_mfma_f32_16x16x32_bf16 v[176:179], v[8:11], v[224:227], v[176:179]\n\t" \
  "v_mfma_f32_16x16x32_bf16 v[180:183], v[80:83], v[224:227], v[180:183]\n\t" \
  "ds_read_b64_tr_b16 v[224:225], v238 offset:24576\n\t" \
  "ds_read_b64_tr_b16 v[226:227], v238 offset:26624\n\t" \
  "s_waitcnt lgkmcnt(10)\n\t" \
  "v_mfma_f32_16x16x32_bf16 v[184:187], v[8:11], v[228:231], v[184:187]\n\t" \
  "v_mfma_f32_16x16x32_bf16 v[188:191], v[80:83], v[228:231], v[188:191]\n\t" \
  "s_mov_b32 s72, 18432\n\t" \
  "buffer_load_dwordx4 v[76:79], %[avo], s[52:55], s72 offen offset:1024\n\t" \
  "ds_read_b64_tr_b16 v[228:229], v239 offset:24576\n\t" \
  "ds_read_b64_tr_b16 v[230:231], v239 offset:26624\n\t" \
  "s_waitcnt lgkmcnt(10)\n\t" \
  "v_mfma_f32_16x16x32_bf16 v[192:195], v[8:11], v[208:211], v[192:195]\n\t" \
  "v_mfma_f32_16x16x32_bf16 v[196:199], v[80:83], v[208:211], v[196:199]\n\t" \
  "ds_read_b64_tr_b16 v[208:209], v236 offset:28672\n\t" \
  "ds_read_b64_tr_b16 v[210:211], v236 offset:30720\n\t" \
  "s_waitcnt lgkmcnt(10)\n\t" \
  "v_mfma_f32_16x16x32_bf16 v[200:203], v[8:11], v[212:215], v[200:203]\n\t" \
  "v_mfma_f32_16x16x32_bf16 v[204:207], v[80:83], v[212:215], v[204:207]\n\t" \
  "ds_read_b64_tr_b16 v[212:213], v237 offset:28672\n\t" \
  "ds_read_b64_tr_b16 v[214:215], v237 offset:30720\n\t" \
  "s_waitcnt lgkmcnt(10)\n\t" \
  "v_mfma_f32_16x16x32_bf16 v[144:147], v[12:15], v[216:219], v[144:147]\n\t" \
  "v_mfma_f32_16x16x32_bf16 v[148:151], v[84:87], v[216:219], v[148:151]\n\t" \
  "ds_read_b64_tr_b16 v[216:217], v238 offset:28672\n\t" \
  "ds_read_b64_tr_b16 v[218:219], v238 offset:30720\n\t" \
  "s_waitcnt lgkmcnt(10)\n\t" \
  "v_mfma_f32_16x16x32_bf16 v[152:155], v[12:15], v[220:223], v[152:155]\n\t" \
  "v_mfma_f32_16x16x32_bf16 v[156:159], v[84:87], v[220:223], v[156:159]\n\t" \
  "s_mov_b32 s72, 0\n\t" \
  "buffer_load_dwordx4 v[8:11], %[avo], s[52:55], s72 offen offset:2048\n\t" \
  "buffer_store_dwordx4 v[136:139], %[kvo], s[56:59], 0 offen offset:2048 sc1\n\t" \
  "ds_read_b64_tr_b16 v[220:221], v239 offset:28672\n\t" \
  "ds_read_b64_tr_b16 v[222:223], v239 offset:30720\n\t" \
  "s_waitcnt lgkmcnt(10)\n\t" \
  "v_mfma_f32_16x16x32_bf16 v[160:163], v[12:15], v[224:227], v[160:163]\n\t" \
  "v_mfma_f32_16x16x32_bf16 v[164:167], v[84:87], v[224:227], v[164:167]\n\t" \
  "s_waitcnt vmcnt(14)\n\t" \
  "s_barrier\n\t" \
  "ds_read_b64_tr_b16 v[224:225], v236 offset:32768\n\t" \
  "ds_read_b64_tr_b16 v[226:227], v236 offset:34816\n\t" \
  "s_waitcnt lgkmcnt(10)\n\t" \
  "v_mfma_f32_16x16x32_bf16 v[168:171], v[12:15], v[228:231], v[168:171]\n\t" \
  "v_mfma_f32_16x16x32_bf16 v[172:175], v[84:87], v[228:231], v[172:175]\n\t" \
  "ds_read_b64_tr_b16 v[228:229], v237 offset:32768\n\t" \
  "ds_read_b64_tr_b16 v[230:231], v237 offset:34816\n\t" \
  "s_waitcnt lgkmcnt(10)\n\t" \
  "v_mfma_f32_16x16x32_bf16 v[176:179], v[12:15], v[208:211], v[176:179]\n\t" \
  "v_mfma_f32_16x16x32_bf16 v[180:183], v[84:87], v[208:211], v[180:183]\n\t" \
  "ds_read_b64_tr_b16 v[208:209], v238 offset:32768\n\t" \
  "ds_read_b64_tr_b16 v[210:211], v238 offset:34816\n\t" \
  "s_waitcnt lgkmcnt(10)\n\t" \
  "v_mfma_f32_16x16x32_bf16 v[184:187], v[12:15], v[212:215], v[184:187]\n\t" \
  "v_mfma_f32_16x16x32_bf16 v[188:191], v[84:87], v[212:215], v[188:191]\n\t" \
  "s_mov_b32 s72, 18432\n\t" \
  "buffer_load_dwordx4 v[80:83], %[avo], s[52:55], s72 offen offset:2048\n\t" \
  "s_add_u32 s71, s79, 3145728\n\t" \
  "s_add_u32 m0, s76, 98304\n\t" \
  "s_nop 0\n\t" \
  "buffer_load_dwordx4 %[wvo], s[40:43], s71 offen lds\n\t" \
  "ds_read_b64_tr_b16 v[212:213], v239 offset:32768\n\t" \
  "ds_read_b64_tr_b16 v[214:215], v239 offset:34816\n\t" \
  "s_waitcnt lgkmcnt(10)\n\t" \
  "v_mfma_f32_16x16x32_bf16 v[192:195], v[12:15], v[216:219], v[192:195]\n\t" \
  "v_mfma_f32_16x16x32_bf16 v[196:199], v[84:87], v[216:219], v[196:199]\n\t" \
  "ds_read_b64_tr_b16 v[216:217], v236 offset:36864\n\t" \
  "ds_read_b64_tr_b16 v[218:219], v236 offset:38912\n\t" \
  "s_waitcnt lgkmcnt(10)\n\t" \
  "v_mfma_f32_16x16x32_bf16 v[200:203], v[12:15], v[220:223], v[200:203]\n\t" \
  "v_mfma_f32_16x16x32_bf16 v[204:207], v[84:87], v[220:223], v[204:207]\n\t" \
  "ds_read_b64_tr_b16 v[220:221], v237 offset:36864\n\t" \
  "ds_read_b64_tr_b16 v[222:223], v237 offset:38912\n\t" \
  "s_waitcnt lgkmcnt(10)\n\t" \
  "v_mfma_f32_16x16x32_bf16 v[144:147], v[16:19], v[224:227], v[144:147]\n\t" \
  "v_mfma_f32_16x16x32_bf16 v[148:151], v[88:91], v[224:227], v[148:151]\n\t" \
  "ds_read_b64_tr_b16 v[224:225], v238 offset:36864\n\t" \
  "ds_read_b64_tr_b16 v[226:227], v238 offset:38912\n\t" \
  "s_waitcnt lgkmcnt(10)\n\t" \
  "v_mfma_f32_16x16x32_bf16 v[152:155], v[16:19], v[228:231], v[152:155]\n\t" \
  "v_mfma_f32_16x16x32_bf16 v[156:159], v[88:91], v[228:231], v[156:159]\n\t" \
  "s_mov_b32 s72, 0\n\t" \
  "buffer_load_dwordx4 v[12:15], %[avo], s[52:55], s72 offen offset:3072\n\t" \
  "s_add_u32 s71, s79, 3407872\n\t" \
  "s_add_u32 m0, s76, 106496\n\t" \
  "s_nop 0\n\t" \
  "buffer_load_dwordx4 %[wvo], s[40:43], s71 offen lds\n\t" \
  "ds_read_b64_tr_b16 v[228:229], v239 offset:36864\n\t" \
  "ds_read_b64_tr_b16 v[230:231], v239 offset:38912\n\t" \
  "s_waitcnt lgkmcnt(10)\n\t" \
  "v_mfma_f32_16x16x32_bf16 v[160:163], v[16:19], v[208:211], v[160:163]\n\t" \
  "v_mfma_f32_16x16x32_bf16 v[164:167], v[88:91], v[208:211], v[164:167]\n\t" \
  "ds_read_b64_tr_b16 v[208:209], v236 offset:40960\n\t" \
  "ds_read_b64_tr_b16 v[210:211], v236 offset:43008\n\t" \
  "s_waitcnt lgkmcnt(10)\n\t" \
  "v_mfma_f32_16x16x32_bf16 v[168:171], v[16:19], v[212:215], v[168:171]\n\t" \
  "v_mfma_f32_16x16x32_bf16 v[172:175], v[88:91], v[212:215], v[172:175]\n\t" \
  "ds_read_b64_tr_b16 v[212:213], v237 offset:40960\n\t" \
  "ds_read_b64_tr_b16 v[214:215], v237 offset:43008\n\t" \
  "s_waitcnt lgkmcnt(10)\n\t" \
  "v_mfma_f32_16x16x32_bf16 v[176:179], v[16:19], v[216:219], v[176:179]\n\t" \
  "v_mfma_f32_16x16x32_bf16 v[180:183], v[88:91], v[216:219], v[180:183]\n\t" \
  "buffer_store_dwordx4 v[140:143], %[kvo], s[56:59], 0 offen offset:2176 sc1\n\t" \
  "ds_read_b64_tr_b16 v[216:217], v238 offset:40960\n\t" \
  "ds_read_b64_tr_b16 v[218:219], v238 offset:43008\n\t" \
  "s_waitcnt lgkmcnt(10)\n\t" \
  "v_mfma_f32_16x16x32_bf16 v[184:187], v[16:19], v[220:223], v[184:187]\n\t" \
  "v_mfma_f32_16x16x32_bf16 v[188:191], v[88:91], v[220:223], v[188:191]\n\t" \
  "s_mov_b32 s72, 18432\n\t" \
  "buffer_load_dwordx4 v[84:87], %[avo], s[52:55], s72 offen offset:3072\n\t" \
  "s_add_u32 s71, s79, 3670016\n\t" \
  "s_add_u32 m0, s76, 114688\n\t" \
  "s_nop 0\n\t" \
  "buffer_load_dwordx4 %[wvo], s[40:43], s71 offen lds\n\t" \
  "ds_read_b64_tr_b16 v[220:221], v239 offset:40960\n\t" \
  "ds_read_b64_tr_b16 v[222:223], v239 offset:43008\n\t" \
  "s_waitcnt lgkmcnt(10)\n\t" \
  "v_mfma_f32_16x16x32_bf16 v[192:195], v[16:19], v[224:227], v[192:195]\n\t" \
  "v_mfma_f32_16x16x32_bf16 v[196:199], v[88:91], v[224:227], v[196:199]\n\t" \
  "ds_read_b64_tr_b16 v[224:225], v236 offset:45056\n\t" \
  "ds_read_b64_tr_b16 v[226:227], v236 offset:47104\n\t" \
  "s_waitcnt lgkmcnt(10)\n\t" \
  "v_mfma_f32_16x16x32_bf16 v[200:203], v[16:19], v[228:231], v[200:203]\n\t" \
  "v_mfma_f32_16x16x32_bf16 v[204:207], v[88:91], v[228:231], v[204:207]\n\t" \
  "ds_read_b64_tr_b16 v[228:229], v237 offset:45056\n\t" \
  "ds_read_b64_tr_b16 v[230:231], v237 offset:47104\n\t" \
  "s_waitcnt lgkmcnt(10)\n\t" \
  "v_mfma_f32_16x16x32_bf16 v[144:147], v[20:23], v[208:211], v[144:147]\n\t" \
  "v_mfma_f32_16x16x32_bf16 v[148:151], v[92:95], v[208:211], v[148:151]\n\t" \
  "ds_read_b64_tr_b16 v[208:209], v238 offset:45056\n\t" \
  "ds_read_b64_tr_b16 v[210:211], v238 offset:47104\n\t" \
  "s_waitcnt lgkmcnt(10)\n\t" \
  "v_mfma_f32_16x16x32_bf16 v[152:155], v[20:23], v[212:215], v[152:155]\n\t" \
  "v_mfma_f32_16x16x32_bf16 v[156:159], v[92:95], v[212:215], v[156:159]\n\t" \
  "s_mov_b32 s72, 4096\n\t" \
  "buffer_load_dwordx4 v[16:19], %[avo], s[52:55], s72 offen offset:0\n\t" \
  "s_add_u32 s71, s79, 3932160\n\t" \
  "s_add_u32 m0, s76, 122880\n\t" \
  "s_nop 0\n\t" \
  "buffer_load_dwordx4 %[wvo], s[40:43], s71 offen lds\n\t" \
  "ds_read_b64_tr_b16 v[212:213], v239 offset:45056\n\t" \
  "ds_read_b64_tr_b16 v[214:215], v239 offset:47104\n\t" \
  "s_waitcnt lgkmcnt(10)\n\t" \
  "v_mfma_f32_16x16x32_bf16 v[160:163], v[20:23], v[216:219], v[160:163]\n\t" \
  "v_mfma_f32_16x16x32_bf16 v[164:167], v[92:95], v[216:219], v[164:167]\n\t" \
  "ds_read_b64_tr_b16 v[216:217], v236 offset:49152\n\t" \
  "ds_read_b64_tr_b16 v[218:219], v236 offset:51200\n\t" \
  "s_waitcnt lgkmcnt(10)\n\t" \
  "v_mfma_f32_16x16x32_bf16 v[168:171], v[20:23], v[220:223], v[168:171]\n\t" \
  "v_mfma_f32_16x16x32_bf16 v[172:175], v[92:95], v[220:223], v[172:175]\n\t" \
  "ds_read_b64_tr_b16 v[220:221], v237 offset:49152\n\t" \
  "ds_read_b64_tr_b16 v[222:223], v237 offset:51200\n\t" \
  "s_waitcnt lgkmcnt(10)\n\t" \
  "v_mfma_f32_16x16x32_bf16 v[176:179], v[20:23], v[224:227], v[176:179]\n\t" \
  "v_mfma_f32_16x16x32_bf16 v[180:183], v[92:95], v[224:227], v[180:183]\n\t" \
  "ds_read_b64_tr_b16 v[224:225], v238 offset:49152\n\t" \
  "ds_read_b64_tr_b16 v[226:227], v238 offset:51200\n\t" \
  "s_waitcnt lgkmcnt(10)\n\t" \
  "v_mfma_f32_16x16x32_bf16 v[184:187], v[20:23], v[228:231], v[184:187]\n\t" \
  "v_mfma_f32_16x16x32_bf16 v[188:191], v[92:95], v[228:231], v[188:191]\n\t" \
  "s_mov_b32 s72, 22528\n\t" \
  "buffer_load_dwordx4 v[88:91], %[avo], s[52:55], s72 offen offset:0\n\t" \
  "ds_read_b64_tr_b16 v[228:229], v239 offset:49152\n\t" \
  "ds_read_b64_tr_b16 v[230:231], v239 offset:51200\n\t" \
  "s_waitcnt lgkmcnt(10)\n\t" \
  "v_mfma_f32_16x16x32_bf16 v[192:195], v[20:23], v[208:211], v[192:195]\n\t" \
  "v_mfma_f32_16x16x32_bf16 v[196:199], v[92:95], v[208:211], v[196:199]\n\t" \
  "ds_read_b64_tr_b16 v[208:209], v236 offset:53248\n\t" \
  "ds_read_b64_tr_b16 v[210:211], v236 offset:55296\n\t" \
  "s_waitcnt lgkmcnt(10)\n\t" \
  "v_mfma_f32_16x16x32_bf16 v[200:203], v[20:23], v[212:215], v[200:203]\n\t" \
  "v_mfma_f32_16x16x32_bf16 v[204:207], v[92:95], v[212:215], v[204:207]\n\t" \
  "buffer_store_dwordx4 v[240:243], %[kvo], s[56:59], s81 offen offset:0 sc1\n\t" \
  "ds_read_b64_tr_b16 v[212:213], v237 offset:53248\n\t" \
  "ds_read_b64_tr_b16 v[214:215], v237 offset:55296\n\t" \
  "s_waitcnt lgkmcnt(10)\n\t" \
  "v_mfma_f32_16x16x32_bf16 v[144:147], v[24:27], v[216:219], v[144:147]\n\t" \
  "v_mfma_f32_16x16x32_bf16 v[148:151], v[96:99], v[216:219], v[148:151]\n\t" \
  "ds_read_b64_tr_b16 v[216:217], v238 offset:53248\n\t" \
  "ds_read_b64_tr_b16 v[218:219], v238 offset:55296\n\t" \
  "s_waitcnt lgkmcnt(10)\n\t" \
  "v_mfma_f32_16x16x32_bf16 v[152:155], v[24:27], v[220:223], v[152:155]\n\t" \
  "v_mfma_f32_16x16x32_bf16 v[156:159], v[96:99], v[220:223], v[156:159]\n\t" \
  "s_mov_b32 s72, 4096\n\t" \
  "buffer_load_dwordx4 v[20:23], %[avo], s[52:55], s72 offen offset:1024\n\t" \
  "ds_read_b64_tr_b16 v[220:221], v239 offset:53248\n\t" \
  "ds_read_b64_tr_b16 v[222:223], v239 offset:55296\n\t" \
  "s_waitcnt lgkmcnt(10)\n\t" \
  "v_mfma_f32_16x16x32_bf16 v[160:163], v[24:27], v[224:227], v[160:163]\n\t" \
  "v_mfma_f32_16x16x32_bf16 v[164:167], v[96:99], v[224:227], v[164:167]\n\t" \
  "ds_read_b64_tr_b16 v[224:225], v236 offset:57344\n\t" \
  "ds_read_b64_tr_b16 v[226:227], v236 offset:59392\n\t" \
  "s_waitcnt lgkmcnt(10)\n\t" \
  "v_mfma_f32_16x16x32_bf16 v[168:171], v[24:27], v[228:231], v[168:171]\n\t" \
  "v_mfma_f32_16x16x32_bf16 v[172:175], v[96:99], v[228:231], v[172:175]\n\t" \
  "ds_read_b64_tr_b16 v[228:229], v237 offset:57344\n\t" \
  "ds_read_b64_tr_b16 v[230:231], v237 offset:59392\n\t" \
  "s_waitcnt lgkmcnt(10)\n\t" \
  "v_mfma_f32_16x16x32_bf16 v[176:179], v[24:27], v[208:211], v[176:179]\n\t" \
  "v_mfma_f32_16x16x32_bf16 v[180:183], v[96:99], v[208:211], v[180:183]\n\t" \
  "ds_read_b64_tr_b16 v[208:209], v238 offset:57344\n\t" \
  "ds_read_b64_tr_b16 v[210:211], v238 offset:59392\n\t" \
  "s_waitcnt lgkmcnt(10)\n\t" \
  "v_mfma_f32_16x16x32_bf16 v[184:187], v[24:27], v[212:215], v[184:187]\n\t" \
  "v_mfma_f32_16x16x32_bf16 v[188:191], v[96:99], v[212:215], v[188:191]\n\t" \
  "s_mov_b32 s72, 22528\n\t" \
  "buffer_load_dwordx4 v[92:95], %[avo], s[52:55], s72 offen offset:1024\n\t" \
  "ds_read_b64_tr_b16 v[212:213], v239 offset:57344\n\t" \
  "ds_read_b64_tr_b16 v[214:215], v239 offset:59392\n\t" \
  "s_waitcnt lgkmcnt(10)\n\t" \
  "v_mfma_f32_16x16x32_bf16 v[192:195], v[24:27], v[216:219], v[192:195]\n\t" \
  "v_mfma_f32_16x16x32_bf16 v[196:199], v[96:99], v[216:219], v[196:199]\n\t" \
  "ds_read_b64_tr_b16 v[216:217], v236 offset:61440\n\t" \
  "ds_read_b64_tr_b16 v[218:219], v236 offset:63488\n\t" \
  "s_waitcnt lgkmcnt(10)\n\t" \
  "v_mfma_f32_16x16x32_bf16 v[200:203], v[24:27], v[220:223], v[200:203]\n\t" \
  "v_mfma_f32_16x16x32_bf16 v[204:207], v[96:99], v[220:223], v[204:207]\n\t" \
  "ds_read_b64_tr_b16 v[220:221], v237 offset:61440\n\t" \
  "ds_read_b64_tr_b16 v[222:223], v237 offset:63488\n\t" \
  "s_waitcnt lgkmcnt(10)\n\t" \
  "v_mfma_f32_16x16x32_bf16 v[144:147], v[28:31], v[224:227], v[144:147]\n\t" \
  "v_mfma_f32_16x16x32_bf16 v[148:151], v[100:103], v[224:227], v[148:151]\n\t" \
  "ds_read_b64_tr_b16 v[224:225], v238 offset:61440\n\t" \
  "ds_read_b64_tr_b16 v[226:227], v238 offset:63488\n\t" \
  "s_waitcnt lgkmcnt(10)\n\t" \
  "v_mfma_f32_16x16x32_bf16 v[152:155], v[28:31], v[228:231], v[152:155]\n\t" \
  "v_mfma_f32_16x16x32_bf16 v[156:159], v[100:103], v[228:231], v[156:159]\n\t" \
  "s_mov_b32 s72, 4096\n\t" \
  "buffer_load_dwordx4 v[24:27], %[avo], s[52:55], s72 offen offset:2048\n\t" \
  "ds_read_b64_tr_b16 v[228:229], v239 offset:61440\n\t" \
  "ds_read_b64_tr_b16 v[230:231], v239 offset:63488\n\t" \
  "s_waitcnt lgkmcnt(10)\n\t" \
  "v_mfma_f32_16x16x32_bf16 v[160:163], v[28:31], v[208:211], v[160:163]\n\t" \
  "v_mfma_f32_16x16x32_bf16 v[164:167], v[100:103], v[208:211], v[164:167]\n\t" \
  "buffer_store_dwordx4 v[244:247], %[kvo], s[56:59], s81 offen offset:128 sc1\n\t" \
  "s_waitcnt vmcnt(21)\n\t" \
  "s_barrier\n\t" \
  "v_add_u32_e32 v236, 65536, v236\n\t" \
  "v_add_u32_e32 v237, 65536, v237\n\t" \
  "v_add_u32_e32 v238, 65536, v238\n\t" \
  "v_add_u32_e32 v239, 65536, v239\n\t" \
  "ds_read_b64_tr_b16 v[208:209], v236 offset:0\n\t" \
  "ds_read_b64_tr_b16 v[210:211], v236 offset:2048\n\t" \
  "s_waitcnt lgkmcnt(10)\n\t" \
  "v_mfma_f32_16x16x32_bf16 v[168:171], v[28:31], v[212:215], v[168:171]\n\t" \
  "v_mfma_f32_16x16x32_bf16 v[172:175], v[100:103], v[212:215], v[172:175]\n\t" \
  "ds_read_b64_tr_b16 v[212:213], v237 offset:0\n\t" \
  "ds_read_b64_tr_b16 v[214:215], v237 offset:2048\n\t" \
  "s_waitcnt lgkmcnt(10)\n\t" \
  "v_mfma_f32_16x16x32_bf16 v[176:179], v[28:31], v[216:219], v[176:179]\n\t" \
  "v_mfma_f32_16x16x32_bf16 v[180:183], v[100:103], v[216:219], v[180:183]\n\t" \
  "ds_read_b64_tr_b16 v[216:217], v238 offset:0\n\t" \
  "ds_read_b64_tr_b16 v[218:219], v238 offset:2048\n\t" \
  "s_waitcnt lgkmcnt(10)\n\t" \
  "v_mfma_f32_16x16x32_bf16 v[184:187], v[28:31], v[220:223], v[184:187]\n\t" \
  "v_mfma_f32_16x16x32_bf16 v[188:191], v[100:103], v[220:223], v[188:191]\n\t" \
  "s_mov_b32 s72, 22528\n\t" \
  "buffer_load_dwordx4 v[96:99], %[avo], s[52:55], s72 offen offset:2048\n\t" \
  "s_add_u32 s71, s80, 0\n\t" \
  "s_add_u32 m0, s76, 0\n\t" \
  "s_nop 0\n\t" \
  "buffer_load_dwordx4 %[wvo], s[44:47], s71 offen lds\n\t" \
  "ds_read_b64_tr_b16 v[220:221], v239 offset:0\n\t" \
  "ds_read_b64_tr_b16 v[222:223], v239 offset:2048\n\t" \
  "s_waitcnt lgkmcnt(10)\n\t" \
  "v_mfma_f32_16x16x32_bf16 v[192:195], v[28:31], v[224:227], v[192:195]\n\t" \
  "v_mfma_f32_16x16x32_bf16 v[196:199], v[100:103], v[224:227], v[196:199]\n\t" \
  "ds_read_b64_tr_b16 v[224:225], v236 offset:4096\n\t" \
  "ds_read_b64_tr_b16 v[226:227], v236 offset:6144\n\t" \
  "s_waitcnt lgkmcnt(10)\n\t" \
  "v_mfma_f32_16x16x32_bf16 v[200:203], v[28:31], v[228:231], v[200:203]\n\t" \
  "v_mfma_f32_16x16x32_bf16 v[204:207], v[100:103], v[228:231], v[204:207]\n\t" \
  "ds_read_b64_tr_b16 v[228:229], v237 offset:4096\n\t" \
  "ds_read_b64_tr_b16 v[230:231], v237 offset:6144\n\t" \
  "s_waitcnt lgkmcnt(10)\n\t" \
  "v_mfma_f32_16x16x32_bf16 v[144:147], v[32:35], v[208:211], v[144:147]\n\t" \
  "v_mfma_f32_16x16x32_bf16 v[148:151], v[104:107], v[208:211], v[148:151]\n\t" \
  "ds_read_b64_tr_b16 v[208:209], v238 offset:4096\n\t" \
  "ds_read_b64_tr_b16 v[210:211], v238 offset:6144\n\t" \
  "s_waitcnt lgkmcnt(10)\n\t" \
  "v_mfma_f32_16x16x32_bf16 v[152:155], v[32:35], v[212:215], v[152:155]\n\t" \
  "v_mfma_f32_16x16x32_bf16 v[156:159], v[104:107], v[212:215], v[156:159]\n\t" \
  "s_mov_b32 s72, 4096\n\t" \
  "buffer_load_dwordx4 v[28:31], %[avo], s[52:55], s72 offen offset:3072\n\t" \
  "s_add_u32 s71, s80, 524288\n\t" \
  "s_add_u32 m0, s76, 8192\n\t" \
  "s_nop 0\n\t" \
  "buffer_load_dwordx4 %[wvo], s[44:47], s71 offen lds\n\t" \
  "ds_read_b64_tr_b16 v[212:213], v239 offset:4096\n\t" \
  "ds_read_b64_tr_b16 v[214:215], v239 offset:6144\n\t" \
  "s_waitcnt lgkmcnt(10)\n\t" \
  "v_mfma_f32_16x16x32_bf16 v[160:163], v[32:35], v[216:219], v[160:163]\n\t" \
  "v_mfma_f32_16x16x32_bf16 v[164:167], v[104:107], v[216:219], v[164:167]\n\t" \
  "ds_read_b64_tr_b16 v[216:217], v236 offset:8192\n\t" \
  "ds_read_b64_tr_b16 v[218:219], v236 offset:10240\n\t" \
  "s_waitcnt lgkmcnt(10)\n\t" \
  "v_mfma_f32_16x16x32_bf16 v[168:171], v[32:35], v[220:223], v[168:171]\n\t" \
  "v_mfma_f32_16x16x32_bf16 v[172:175], v[104:107], v[220:223], v[172:175]\n\t" \
  "ds_read_b64_tr_b16 v[220:221], v237 offset:8192\n\t" \
  "ds_read_b64_tr_b16 v[222:223], v237 offset:10240\n\t" \
  "s_waitcnt lgkmcnt(10)\n\t" \
  "v_mfma_f32_16x16x32_bf16 v[176:179], v[32:35], v[224:227], v[176:179]\n\t" \
  "v_mfma_f32_16x16x32_bf16 v[180:183], v[104:107], v[224:227], v[180:183]\n\t" \
  "ds_read_b64_tr_b16 v[224:225], v238 offset:8192\n\t" \
  "ds_read_b64_tr_b16 v[226:227], v238 offset:10240\n\t" \
  "s_waitcnt lgkmcnt(10)\n\t" \
  "v_mfma_f32_16x16x32_bf16 v[184:187], v[32:35], v[228:231], v[184:187]\n\t" \
  "v_mfma_f32_16x16x32_bf16 v[188:191], v[104:107], v[228:231], v[188:191]\n\t" \
  "s_mov_b32 s72, 22528\n\t" \
  "buffer_load_dwordx4 v[100:103], %[avo], s[52:55], s72 offen offset:3072\n\t" \
  "s_add_u32 s71, s80, 1048576\n\t" \
  "s_add_u32 m0, s76, 16384\n\t" \
  "s_nop 0\n\t" \
  "buffer_load_dwordx4 %[wvo], s[44:47], s71 offen lds\n\t" \
  "ds_read_b64_tr_b16 v[228:229], v239 offset:8192\n\t" \
  "ds_read_b64_tr_b16 v[230:231], v239 offset:10240\n\t" \
  "s_waitcnt lgkmcnt(10)\n\t" \
  "v_mfma_f32_16x16x32_bf16 v[192:195], v[32:35], v[208:211], v[192:195]\n\t" \
  "v_mfma_f32_16x16x32_bf16 v[196:199], v[104:107], v[208:211], v[196:199]\n\t" \
  "ds_read_b64_tr_b16 v[208:209], v236 offset:12288\n\t" \
  "ds_read_b64_tr_b16 v[210:211], v236 offset:14336\n\t" \
  "s_waitcnt lgkmcnt(10)\n\t" \
  "v_mfma_f32_16x16x32_bf16 v[200:203], v[32:35], v[212:215], v[200:203]\n\t" \
  "v_mfma_f32_16x16x32_bf16 v[204:207], v[104:107], v[212:215], v[204:207]\n\t" \
  "ds_read_b64_tr_b16 v[212:213], v237 offset:12288\n\t" \
  "ds_read_b64_tr_b16 v[214:215], v237 offset:14336\n\t" \
  "s_waitcnt lgkmcnt(10)\n\t" \
  "v_mfma_f32_16x16x32_bf16 v[144:147], v[36:39], v[216:219], v[144:147]\n\t" \
  "v_mfma_f32_16x16x32_bf16 v[148:151], v[108:111], v[216:219], v[148:151]\n\t" \
  "ds_read_b64_tr_b16 v[216:217], v238 offset:12288\n\t" \
  "ds_read_b64_tr_b16 v[218:219], v238 offset:14336\n\t" \
  "s_waitcnt lgkmcnt(10)\n\t" \
  "v_mfma_f32_16x16x32_bf16 v[152:155], v[36:39], v[220:223], v[152:155]\n\t" \
  "v_mfma_f32_16x16x32_bf16 v[156:159], v[108:111], v[220:223], v[156:159]\n\t" \
  "s_mov_b32 s72, 8192\n\t" \
  "buffer_load_dwordx4 v[32:35], %[avo], s[52:55], s72 offen offset:0\n\t" \
  "s_add_u32 s71, s80, 1572864\n\t" \
  "s_add_u32 m0, s76, 24576\n\t" \
  "s_nop 0\n\t" \
  "buffer_load_dwordx4 %[wvo], s[44:47], s71 offen lds\n\t" \
  "ds_read_b64_tr_b16 v[220:221], v239 offset:12288\n\t" \
  "ds_read_b64_tr_b16 v[222:223], v239 offset:14336\n\t" \
  "s_waitcnt lgkmcnt(10)\n\t" \
  "v_mfma_f32_16x16x32_bf16 v[160:163], v[36:39], v[224:227], v[160:163]\n\t" \
  "v_mfma_f32_16x16x32_bf16 v[164:167], v[108:111], v[224:227], v[164:167]\n\t" \
  "ds_read_b64_tr_b16 v[224:225], v236 offset:16384\n\t" \
  "ds_read_b64_tr_b16 v[226:227], v236 offset:18432\n\t" \
  "s_waitcnt lgkmcnt(10)\n\t" \
  "v_mfma_f32_16x16x32_bf16 v[168:171], v[36:39], v[228:231], v[168:171]\n\t" \
  "v_mfma_f32_16x16x32_bf16 v[172:175], v[108:111], v[228:231], v[172:175]\n\t" \
  "ds_read_b64_tr_b16 v[228:229], v237 offset:16384\n\t" \
  "ds_read_b64_tr_b16 v[230:231], v237 offset:18432\n\t" \
  "s_waitcnt lgkmcnt(10)\n\t" \
  "v_mfma_f32_16x16x32_bf16 v[176:179], v[36:39], v[208:211], v[176:179]\n\t" \
  "v_mfma_f32_16x16x32_bf16 v[180:183], v[108:111], v[208:211], v[180:183]\n\t" \
  "ds_read_b64_tr_b16 v[208:209], v238 offset:16384\n\t" \
  "ds_read_b64_tr_b16 v[210:211], v238 offset:18432\n\t" \
  "s_waitcnt lgkmcnt(10)\n\t" \
  "v_mfma_f32_16x16x32_bf16 v[184:187], v[36:39], v[212:215], v[184:187]\n\t" \
  "v_mfma_f32_16x16x32_bf16 v[188:191], v[108:111], v[212:215], v[188:191]\n\t" \
  "s_mov_b32 s72, 26624\n\t" \
  "buffer_load_dwordx4 v[104:107], %[avo], s[52:55], s72 offen offset:0\n\t" \
  "ds_read_b64_tr_b16 v[212:213], v239 offset:16384\n\t" \
  "ds_read_b64_tr_b16 v[214:215], v239 offset:18432\n\t" \
  "s_waitcnt lgkmcnt(10)\n\t" \
  "v_mfma_f32_16x16x32_bf16 v[192:195], v[36:39], v[216:219], v[192:195]\n\t" \
  "v_mfma_f32_16x16x32_bf16 v[196:199], v[108:111], v[216:219], v[196:199]\n\t" \
  "ds_read_b64_tr_b16 v[216:217], v236 offset:20480\n\t" \
  "ds_read_b64_tr_b16 v[218:219], v236 offset:22528\n\t" \
  "s_waitcnt lgkmcnt(10)\n\t" \
  "v_mfma_f32_16x16x32_bf16 v[200:203], v[36:39], v[220:223], v[200:203]\n\t" \
  "v_mfma_f32_16x16x32_bf16 v[204:207], v[108:111], v[220:223], v[204:207]\n\t" \
  "ds_read_b64_tr_b16 v[220:221], v237 offset:20480\n\t" \
  "ds_read_b64_tr_b16 v[222:223], v237 offset:22528\n\t" \
  "s_waitcnt lgkmcnt(10)\n\t" \
  "v_mfma_f32_16x16x32_bf16 v[144:147], v[40:43], v[224:227], v[144:147]\n\t" \
  "v_mfma_f32_16x16x32_bf16 v[148:151], v[112:115], v[224:227], v[148:151]\n\t" \
  "ds_read_b64_tr_b16 v[224:225], v238 offset:20480\n\t" \
  "ds_read_b64_tr_b16 v[226:227], v238 offset:22528\n\t" \
  "s_waitcnt lgkmcnt(10)\n\t" \
  "v_mfma_f32_16x16x32_bf16 v[152:155], v[40:43], v[228:231], v[152:155]\n\t" \
  "v_mfma_f32_16x16x32_bf16 v[156:159], v[112:115], v[228:231], v[156:159]\n\t" \
  "s_mov_b32 s72, 8192\n\t" \
  "buffer_load_dwordx4 v[36:39], %[avo], s[52:55], s72 offen offset:1024\n\t" \
  "ds_read_b64_tr_b16 v[228:229], v239 offset:20480\n\t" \
  "ds_read_b64_tr_b16 v[230:231], v239 offset:22528\n\t" \
  "s_waitcnt lgkmcnt(10)\n\t" \
  "v_mfma_f32_16x16x32_bf16 v[160:163], v[40:43], v[208:211], v[160:163]\n\t" \
  "v_mfma_f32_16x16x32_bf16 v[164:167], v[112:115], v[208:211], v[164:167]\n\t" \
  "ds_read_b64_tr_b16 v[208:209], v236 offset:24576\n\t" \
  "ds_read_b64_tr_b16 v[210:211], v236 offset:26624\n\t" \
  "s_waitcnt lgkmcnt(10)\n\t" \
  "v_mfma_f32_16x16x32_bf16 v[168:171], v[40:43], v[212:215], v[168:171]\n\t" \
  "v_mfma_f32_16x16x32_bf16 v[172:175], v[112:115], v[212:215], v[172:175]\n\t" \
  "s_mov_b32 s72, 16384\n\t" \
  "buffer_load_dwordx4 v[64:67], %[avo], s[52:55], s72 offen offset:0\n\t" \
  "ds_read_b64_tr_b16 v[212:213], v237 offset:24576\n\t" \
  "ds_read_b64_tr_b16 v[214:215], v237 offset:26624\n\t" \
  "s_waitcnt lgkmcnt(10)\n\t" \
  "v_mfma_f32_16x16x32_bf16 v[176:179], v[40:43], v[216:219], v[176:179]\n\t" \
  "v_mfma_f32_16x16x32_bf16 v[180:183], v[112:115], v[216:219], v[180:183]\n\t" \
  "ds_read_b64_tr_b16 v[216:217], v238 offset:24576\n\t" \
  "ds_read_b64_tr_b16 v[218:219], v238 offset:26624\n\t" \
  "s_waitcnt lgkmcnt(10)\n\t" \
  "v_mfma_f32_16x16x32_bf16 v[184:187], v[40:43], v[220:223], v[184:187]\n\t" \
  "v_mfma_f32_16x16x32_bf16 v[188:191], v[112:115], v[220:223], v[188:191]\n\t" \
  "s_mov_b32 s72, 26624\n\t" \
  "buffer_load_dwordx4 v[108:111], %[avo], s[52:55], s72 offen offset:1024\n\t" \
  "ds_read_b64_tr_b16 v[220:221], v239 offset:24576\n\t" \
  "ds_read_b64_tr_b16 v[222:223], v239 offset:26624\n\t" \
  "s_waitcnt lgkmcnt(10)\n\t" \
  "v_mfma_f32_16x16x32_bf16 v[192:195], v[40:43], v[224:227], v[192:195]\n\t" \
  "v_mfma_f32_16x16x32_bf16 v[196:199], v[112:115], v[224:227], v[196:199]\n\t" \
  "ds_read_b64_tr_b16 v[224:225], v236 offset:28672\n\t" \
  "ds_read_b64_tr_b16 v[226:227], v236 offset:30720\n\t" \
  "s_waitcnt lgkmcnt(10)\n\t" \
  "v_mfma_f32_16x16x32_bf16 v[200:203], v[40:43], v[228:231], v[200:203]\n\t" \
  "v_mfma_f32_16x16x32_bf16 v[204:207], v[112:115], v[228:231], v[204:207]\n\t" \
  "s_mov_b32 s72, 34816\n\t" \
  "buffer_load_dwordx4 v[136:139], %[avo], s[52:55], s72 offen offset:0\n\t" \
  "ds_read_b64_tr_b16 v[228:229], v237 offset:28672\n\t" \
  "ds_read_b64_tr_b16 v[230:231], v237 offset:30720\n\t" \
  "s_waitcnt lgkmcnt(10)\n\t" \
  "v_mfma_f32_16x16x32_bf16 v[144:147], v[44:47], v[208:211], v[144:147]\n\t" \
  "v_mfma_f32_16x16x32_bf16 v[148:151], v[116:119], v[208:211], v[148:151]\n\t" \
  "ds_read_b64_tr_b16 v[208:209], v238 offset:28672\n\t" \
  "ds_read_b64_tr_b16 v[210:211], v238 offset:30720\n\t" \
  "s_waitcnt lgkmcnt(10)\n\t" \
  "v_mfma_f32_16x16x32_bf16 v[152:155], v[44:47], v[212:215], v[152:155]\n\t" \
  "v_mfma_f32_16x16x32_bf16 v[156:159], v[116:119], v[212:215], v[156:159]\n\t" \
  "s_mov_b32 s72, 8192\n\t" \
  "buffer_load_dwordx4 v[40:43], %[avo], s[52:55], s72 offen offset:2048\n\t" \
  "ds_read_b64_tr_b16 v[212:213], v239 offset:28672\n\t" \
  "ds_read_b64_tr_b16 v[214:215], v239 offset:30720\n\t" \
  "s_waitcnt lgkmcnt(10)\n\t" \
  "v_mfma_f32_16x16x32_bf16 v[160:163], v[44:47], v[216:219], v[160:163]\n\t" \
  "v_mfma_f32_16x16x32_bf16 v[164:167], v[116:119], v[216:219], v[164:167]\n\t" \
  "s_waitcnt vmcnt(20)\n\t" \
  "s_barrier\n\t" \
  "ds_read_b64_tr_b16 v[216:217], v236 offset:32768\n\t" \
  "ds_read_b64_tr_b16 v[218:219], v236 offset:34816\n\t" \
  "s_waitcnt lgkmcnt(10)\n\t" \
  "v_mfma_f32_16x16x32_bf16 v[168:171], v[44:47], v[220:223], v[168:171]\n\t" \
  "v_mfma_f32_16x16x32_bf16 v[172:175], v[116:119], v[220:223], v[172:175]\n\t" \
  "ds_read_b64_tr_b16 v[220:221], v237 offset:32768\n\t" \
  "ds_read_b64_tr_b16 v[222:223], v237 offset:34816\n\t" \
  "s_waitcnt lgkmcnt(10)\n\t" \
  "v_mfma_f32_16x16x32_bf16 v[176:179], v[44:47], v[224:227], v[176:179]\n\t" \
  "v_mfma_f32_16x16x32_bf16 v[180:183], v[116:119], v[224:227], v[180:183]\n\t" \
  "ds_read_b64_tr_b16 v[224:225], v238 offset:32768\n\t" \
  "ds_read_b64_tr_b16 v[226:227], v238 offset:34816\n\t" \
  "s_waitcnt lgkmcnt(10)\n\t" \
  "v_mfma_f32_16x16x32_bf16 v[184:187], v[44:47], v[228:231], v[184:187]\n\t" \
  "v_mfma_f32_16x16x32_bf16 v[188:191], v[116:119], v[228:231], v[188:191]\n\t" \
  "s_mov_b32 s72, 26624\n\t" \
  "buffer_load_dwordx4 v[112:115], %[avo], s[52:55], s72 offen offset:2048\n\t" \
  "s_add_u32 s71, s80, 2097152\n\t" \
  "s_add_u32 m0, s76, 32768\n\t" \
  "s_nop 0\n\t" \
  "buffer_load_dwordx4 %[wvo], s[44:47], s71 offen lds\n\t" \
  "ds_read_b64_tr_b16 v[228:229], v239 offset:32768\n\t" \
  "ds_read_b64_tr_b16 v[230:231], v239 offset:34816\n\t" \
  "s_waitcnt lgkmcnt(10)\n\t" \
  "v_mfma_f32_16x16x32_bf16 v[192:195], v[44:47], v[208:211], v[192:195]\n\t" \
  "v_mfma_f32_16x16x32_bf16 v[196:199], v[116:119], v[208:211], v[196:199]\n\t" \
  "ds_read_b64_tr_b16 v[208:209], v236 offset:36864\n\t" \
  "ds_read_b64_tr_b16 v[210:211], v236 offset:38912\n\t" \
  "s_waitcnt lgkmcnt(10)\n\t" \
  "v_mfma_f32_16x16x32_bf16 v[200:203], v[44:47], v[212:215], v[200:203]\n\t" \
  "v_mfma_f32_16x16x32_bf16 v[204:207], v[116:119], v[212:215], v[204:207]\n\t" \
  "ds_read_b64_tr_b16 v[212:213], v237 offset:36864\n\t" \
  "ds_read_b64_tr_b16 v[214:215], v237 offset:38912\n\t" \
  "s_waitcnt lgkmcnt(10)\n\t" \
  "v_mfma_f32_16x16x32_bf16 v[144:147], v[48:51], v[216:219], v[144:147]\n\t" \
  "v_mfma_f32_16x16x32_bf16 v[148:151], v[120:123], v[216:219], v[148:151]\n\t" \
  "ds_read_b64_tr_b16 v[216:217], v238 offset:36864\n\t" \
  "ds_read_b64_tr_b16 v[218:219], v238 offset:38912\n\t" \
  "s_waitcnt lgkmcnt(10)\n\t" \
  "v_mfma_f32_16x16x32_bf16 v[152:155], v[48:51], v[220:223], v[152:155]\n\t" \
  "v_mfma_f32_16x16x32_bf16 v[156:159], v[120:123], v[220:223], v[156:159]\n\t" \
  "s_mov_b32 s72, 8192\n\t" \
  "buffer_load_dwordx4 v[44:47], %[avo], s[52:55], s72 offen offset:3072\n\t" \
  "s_add_u32 s71, s80, 2621440\n\t" \
  "s_add_u32 m0, s76, 40960\n\t" \
  "s_nop 0\n\t" \
  "buffer_load_dwordx4 %[wvo], s[44:47], s71 offen lds\n\t" \
  "ds_read_b64_tr_b16 v[220:221], v239 offset:36864\n\t" \
  "ds_read_b64_tr_b16 v[222:223], v239 offset:38912\n\t" \
  "s_waitcnt lgkmcnt(10)\n\t" \
  "v_mfma_f32_16x16x32_bf16 v[160:163], v[48:51], v[224:227], v[160:163]\n\t" \
  "v_mfma_f32_16x16x32_bf16 v[164:167], v[120:123], v[224:227], v[164:167]\n\t" \
  "ds_read_b64_tr_b16 v[224:225], v236 offset:40960\n\t" \
  "ds_read_b64_tr_b16 v[226:227], v236 offset:43008\n\t" \
  "s_waitcnt lgkmcnt(10)\n\t" \
  "v_mfma_f32_16x16x32_bf16 v[168:171], v[48:51], v[228:231], v[168:171]\n\t" \
  "v_mfma_f32_16x16x32_bf16 v[172:175], v[120:123], v[228:231], v[172:175]\n\t" \
  "s_mov_b32 s72, 16384\n\t" \
  "buffer_load_dwordx4 v[68:71], %[avo], s[52:55], s72 offen offset:1024\n\t" \
  "ds_read_b64_tr_b16 v[228:229], v237 offset:40960\n\t" \
  "ds_read_b64_tr_b16 v[230:231], v237 offset:43008\n\t" \
  "s_waitcnt lgkmcnt(10)\n\t" \
  "v_mfma_f32_16x16x32_bf16 v[176:179], v[48:51], v[208:211], v[176:179]\n\t" \
  "v_mfma_f32_16x16x32_bf16 v[180:183], v[120:123], v[208:211], v[180:183]\n\t" \
  "ds_read_b64_tr_b16 v[208:209], v238 offset:40960\n\t" \
  "ds_read_b64_tr_b16 v[210:211], v238 offset:43008\n\t" \
  "s_waitcnt lgkmcnt(10)\n\t" \
  "v_mfma_f32_16x16x32_bf16 v[184:187], v[48:51], v[212:215], v[184:187]\n\t" \
  "v_mfma_f32_16x16x32_bf16 v[188:191], v[120:123], v[212:215], v[188:191]\n\t" \
  "s_mov_b32 s72, 26624\n\t" \
  "buffer_load_dwordx4 v[116:119], %[avo], s[52:55], s72 offen offset:3072\n\t" \
  "s_add_u32 s71, s80, 3145728\n\t" \
  "s_add_u32 m0, s76, 49152\n\t" \
  "s_nop 0\n\t" \
  "buffer_load_dwordx4 %[wvo], s[44:47], s71 offen lds\n\t" \
  "ds_read_b64_tr_b16 v[212:213], v239 offset:40960\n\t" \
  "ds_read_b64_tr_b16 v[214:215], v239 offset:43008\n\t" \
  "s_waitcnt lgkmcnt(10)\n\t" \
  "v_mfma_f32_16x16x32_bf16 v[192:195], v[48:51], v[216:219], v[192:195]\n\t" \
  "v_mfma_f32_16x16x32_bf16 v[196:199], v[120:123], v[216:219], v[196:199]\n\t" \
  "ds_read_b64_tr_b16 v[216:217], v236 offset:45056\n\t" \
  "ds_read_b64_tr_b16 v[218:219], v236 offset:47104\n\t" \
  "s_waitcnt lgkmcnt(10)\n\t" \
  "v_mfma_f32_16x16x32_bf16 v[200:203], v[48:51], v[220:223], v[200:203]\n\t" \
  "v_mfma_f32_16x16x32_bf16 v[204:207], v[120:123], v[220:223], v[204:207]\n\t" \
  "s_mov_b32 s72, 34816\n\t" \
  "buffer_load_dwordx4 v[140:143], %[avo], s[52:55], s72 offen offset:1024\n\t" \
  "ds_read_b64_tr_b16 v[220:221], v237 offset:45056\n\t" \
  "ds_read_b64_tr_b16 v[222:223], v237 offset:47104\n\t" \
  "s_waitcnt lgkmcnt(10)\n\t" \
  "v_mfma_f32_16x16x32_bf16 v[144:147], v[52:55], v[224:227], v[144:147]\n\t" \
  "v_mfma_f32_16x16x32_bf16 v[148:151], v[124:127], v[224:227], v[148:151]\n\t" \
  "ds_read_b64_tr_b16 v[224:225], v238 offset:45056\n\t" \
  "ds_read_b64_tr_b16 v[226:227], v238 offset:47104\n\t" \
  "s_waitcnt lgkmcnt(10)\n\t" \
  "v_mfma_f32_16x16x32_bf16 v[152:155], v[52:55], v[228:231], v[152:155]\n\t" \
  "v_mfma_f32_16x16x32_bf16 v[156:159], v[124:127], v[228:231], v[156:159]\n\t" \
  "s_mov_b32 s72, 12288\n\t" \
  "buffer_load_dwordx4 v[48:51], %[avo], s[52:55], s72 offen offset:0\n\t" \
  "s_add_u32 s71, s80, 3670016\n\t" \
  "s_add_u32 m0, s76, 57344\n\t" \
  "s_nop 0\n\t" \
  "buffer_load_dwordx4 %[wvo], s[44:47], s71 offen lds\n\t" \
  "ds_read_b64_tr_b16 v[228:229], v239 offset:45056\n\t" \
  "ds_read_b64_tr_b16 v[230:231], v239 offset:47104\n\t" \
  "s_waitcnt lgkmcnt(10)\n\t" \
  "v_mfma_f32_16x16x32_bf16 v[160:163], v[52:55], v[208:211], v[160:163]\n\t" \
  "v_mfma_f32_16x16x32_bf16 v[164:167], v[124:127], v[208:211], v[164:167]\n\t" \
  "ds_read_b64_tr_b16 v[208:209], v236 offset:49152\n\t" \
  "ds_read_b64_tr_b16 v[210:211], v236 offset:51200\n\t" \
  "s_waitcnt lgkmcnt(10)\n\t" \
  "v_mfma_f32_16x16x32_bf16 v[168:171], v[52:55], v[212:215], v[168:171]\n\t" \
  "v_mfma_f32_16x16x32_bf16 v[172:175], v[124:127], v[212:215], v[172:175]\n\t" \
  "ds_read_b64_tr_b16 v[212:213], v237 offset:49152\n\t" \
  "ds_read_b64_tr_b16 v[214:215], v237 offset:51200\n\t" \
  "s_waitcnt lgkmcnt(10)\n\t" \
  "v_mfma_f32_16x16x32_bf16 v[176:179], v[52:55], v[216:219], v[176:179]\n\t" \
  "v_mfma_f32_16x16x32_bf16 v[180:183], v[124:127], v[216:219], v[180:183]\n\t" \
  "ds_read_b64_tr_b16 v[216:217], v238 offset:49152\n\t" \
  "ds_read_b64_tr_b16 v[218:219], v238 offset:51200\n\t" \
  "s_waitcnt lgkmcnt(10)\n\t" \
  "v_mfma_f32_16x16x32_bf16 v[184:187], v[52:55], v[220:223], v[184:187]\n\t" \
  "v_mfma_f32_16x16x32_bf16 v[188:191], v[124:127], v[220:223], v[188:191]\n\t" \
  "s_mov_b32 s72, 30720\n\t" \
  "buffer_load_dwordx4 v[120:123], %[avo], s[52:55], s72 offen offset:0\n\t" \
  "ds_read_b64_tr_b16 v[220:221], v239 offset:49152\n\t" \
  "ds_read_b64_tr_b16 v[222:223], v239 offset:51200\n\t" \
  "s_waitcnt lgkmcnt(10)\n\t" \
  "v_mfma_f32_16x16x32_bf16 v[192:195], v[52:55], v[224:227], v[192:195]\n\t" \
  "v_mfma_f32_16x16x32_bf16 v[196:199], v[124:127], v[224:227], v[196:199]\n\t" \
  "ds_read_b64_tr_b16 v[224:225], v236 offset:53248\n\t" \
  "ds_read_b64_tr_b16 v[226:227], v236 offset:55296\n\t" \
  "s_waitcnt lgkmcnt(10)\n\t" \
  "v_mfma_f32_16x16x32_bf16 v[200:203], v[52:55], v[228:231], v[200:203]\n\t" \
  "v_mfma_f32_16x16x32_bf16 v[204:207], v[124:127], v[228:231], v[204:207]\n\t" \
  "ds_read_b64_tr_b16 v[228:229], v237 offset:53248\n\t" \
  "ds_read_b64_tr_b16 v[230:231], v237 offset:55296\n\t" \
  "s_waitcnt lgkmcnt(10)\n\t" \
  "v_mfma_f32_16x16x32_bf16 v[144:147], v[56:59], v[208:211], v[144:147]\n\t" \
  "v_mfma_f32_16x16x32_bf16 v[148:151], v[128:131], v[208:211], v[148:151]\n\t" \
  "ds_read_b64_tr_b16 v[208:209], v238 offset:53248\n\t" \
  "ds_read_b64_tr_b16 v[210:211], v238 offset:55296\n\t" \
  "s_waitcnt lgkmcnt(10)\n\t" \
  "v_mfma_f32_16x16x32_bf16 v[152:155], v[56:59], v[212:215], v[152:155]\n\t" \
  "v_mfma_f32_16x16x32_bf16 v[156:159], v[128:131], v[212:215], v[156:159]\n\t" \
  "s_mov_b32 s72, 12288\n\t" \
  "buffer_load_dwordx4 v[52:55], %[avo], s[52:55], s72 offen offset:1024\n\t" \
  "ds_read_b64_tr_b16 v[212:213], v239 offset:53248\n\t" \
  "ds_read_b64_tr_b16 v[214:215], v239 offset:55296\n\t" \
  "s_waitcnt lgkmcnt(10)\n\t" \
  "v_mfma_f32_16x16x32_bf16 v[160:163], v[56:59], v[216:219], v[160:163]\n\t" \
  "v_mfma_f32_16x16x32_bf16 v[164:167], v[128:131], v[216:219], v[164:167]\n\t" \
  "ds_read_b64_tr_b16 v[216:217], v236 offset:57344\n\t" \
  "ds_read_b64_tr_b16 v[218:219], v236 offset:59392\n\t" \
  "s_waitcnt lgkmcnt(10)\n\t" \
  "v_mfma_f32_16x16x32_bf16 v[168:171], v[56:59], v[220:223], v[168:171]\n\t" \
  "v_mfma_f32_16x16x32_bf16 v[172:175], v[128:131], v[220:223], v[172:175]\n\t" \
  "ds_read_b64_tr_b16 v[220:221], v237 offset:57344\n\t" \
  "ds_read_b64_tr_b16 v[222:223], v237 offset:59392\n\t" \
  "s_waitcnt lgkmcnt(10)\n\t" \
  "v_mfma_f32_16x16x32_bf16 v[176:179], v[56:59], v[224:227], v[176:179]\n\t" \
  "v_mfma_f32_16x16x32_bf16 v[180:183], v[128:131], v[224:227], v[180:183]\n\t" \
  "ds_read_b64_tr_b16 v[224:225], v238 offset:57344\n\t" \
  "ds_read_b64_tr_b16 v[226:227], v238 offset:59392\n\t" \
  "s_waitcnt lgkmcnt(10)\n\t" \
  "v_mfma_f32_16x16x32_bf16 v[184:187], v[56:59], v[228:231], v[184:187]\n\t" \
  "v_mfma_f32_16x16x32_bf16 v[188:191], v[128:131], v[228:231], v[188:191]\n\t" \
  "s_mov_b32 s72, 30720\n\t" \
  "buffer_load_dwordx4 v[124:127], %[avo], s[52:55], s72 offen offset:1024\n\t" \
  "ds_read_b64_tr_b16 v[228:229], v239 offset:57344\n\t" \
  "ds_read_b64_tr_b16 v[230:231], v239 offset:59392\n\t" \
  "s_waitcnt lgkmcnt(10)\n\t" \
  "v_mfma_f32_16x16x32_bf16 v[192:195], v[56:59], v[208:211], v[192:195]\n\t" \
  "v_mfma_f32_16x16x32_bf16 v[196:199], v[128:131], v[208:211], v[196:199]\n\t" \
  "ds_read_b64_tr_b16 v[208:209], v236 offset:61440\n\t" \
  "ds_read_b64_tr_b16 v[210:211], v236 offset:63488\n\t" \
  "s_waitcnt lgkmcnt(10)\n\t" \
  "v_mfma_f32_16x16x32_bf16 v[200:203], v[56:59], v[212:215], v[200:203]\n\t" \
  "v_mfma_f32_16x16x32_bf16 v[204:207], v[128:131], v[212:215], v[204:207]\n\t" \
  "ds_read_b64_tr_b16 v[212:213], v237 offset:61440\n\t" \
  "ds_read_b64_tr_b16 v[214:215], v237 offset:63488\n\t" \
  "s_waitcnt lgkmcnt(10)\n\t" \
  "v_mfma_f32_16x16x32_bf16 v[144:147], v[60:63], v[216:219], v[144:147]\n\t" \
  "v_mfma_f32_16x16x32_bf16 v[148:151], v[132:135], v[216:219], v[148:151]\n\t" \
  "ds_read_b64_tr_b16 v[216:217], v238 offset:61440\n\t" \
  "ds_read_b64_tr_b16 v[218:219], v238 offset:63488\n\t" \
  "s_waitcnt lgkmcnt(10)\n\t" \
  "v_mfma_f32_16x16x32_bf16 v[152:155], v[60:63], v[220:223], v[152:155]\n\t" \
  "v_mfma_f32_16x16x32_bf16 v[156:159], v[132:135], v[220:223], v[156:159]\n\t" \
  "s_mov_b32 s72, 12288\n\t" \
  "buffer_load_dwordx4 v[56:59], %[avo], s[52:55], s72 offen offset:2048\n\t" \
  "ds_read_b64_tr_b16 v[220:221], v239 offset:61440\n\t" \
  "ds_read_b64_tr_b16 v[222:223], v239 offset:63488\n\t" \
  "s_waitcnt lgkmcnt(10)\n\t" \
  "v_mfma_f32_16x16x32_bf16 v[160:163], v[60:63], v[224:227], v[160:163]\n\t" \
  "v_mfma_f32_16x16x32_bf16 v[164:167], v[132:135], v[224:227], v[164:167]\n\t" \
  "s_waitcnt vmcnt(20)\n\t" \
  "s_barrier\n\t" \
  "v_add_u32_e32 v236, 4294901760, v236\n\t" \
  "v_add_u32_e32 v237, 4294901760, v237\n\t" \
  "v_add_u32_e32 v238, 4294901760, v238\n\t" \
  "v_add_u32_e32 v239, 4294901760, v239\n\t" \
  "ds_read_b64_tr_b16 v[224:225], v236 offset:0\n\t" \
  "ds_read_b64_tr_b16 v[226:227], v236 offset:2048\n\t" \
  "s_waitcnt lgkmcnt(10)\n\t" \
  "v_mfma_f32_16x16x32_bf16 v[168:171], v[60:63], v[228:231], v[168:171]\n\t" \
  "v_mfma_f32_16x16x32_bf16 v[172:175], v[132:135], v[228:231], v[172:175]\n\t" \
  "ds_read_b64_tr_b16 v[228:229], v237 offset:0\n\t" \
  "ds_read_b64_tr_b16 v[230:231], v237 offset:2048\n\t" \
  "s_waitcnt lgkmcnt(10)\n\t" \
  "v_mfma_f32_16x16x32_bf16 v[176:179], v[60:63], v[208:211], v[176:179]\n\t" \
  "v_mfma_f32_16x16x32_bf16 v[180:183], v[132:135], v[208:211], v[180:183]\n\t" \
  "ds_read_b64_tr_b16 v[208:209], v238 offset:0\n\t" \
  "ds_read_b64_tr_b16 v[210:211], v238 offset:2048\n\t" \
  "s_waitcnt lgkmcnt(10)\n\t" \
  "v_mfma_f32_16x16x32_bf16 v[184:187], v[60:63], v[212:215], v[184:187]\n\t" \
  "v_mfma_f32_16x16x32_bf16 v[188:191], v[132:135], v[212:215], v[188:191]\n\t" \
  "s_mov_b32 s72, 30720\n\t" \
  "buffer_load_dwordx4 v[128:131], %[avo], s[52:55], s72 offen offset:2048\n\t" \
  "s_add_u32 s71, s80, 4194304\n\t" \
  "s_add_u32 m0, s76, 65536\n\t" \
  "s_nop 0\n\t" \
  "buffer_load_dwordx4 %[wvo], s[44:47], s71 offen lds\n\t" \
  "ds_read_b64_tr_b16 v[212:213], v239 offset:0\n\t" \
  "ds_read_b64_tr_b16 v[214:215], v239 offset:2048\n\t" \
  "s_waitcnt lgkmcnt(10)\n\t" \
  "v_mfma_f32_16x16x32_bf16 v[192:195], v[60:63], v[216:219], v[192:195]\n\t" \
  "v_mfma_f32_16x16x32_bf16 v[196:199], v[132:135], v[216:219], v[196:199]\n\t" \
  "ds_read_b64_tr_b16 v[216:217], v236 offset:4096\n\t" \
  "ds_read_b64_tr_b16 v[218:219], v236 offset:6144\n\t" \
  "s_waitcnt lgkmcnt(10)\n\t" \
  "v_mfma_f32_16x16x32_bf16 v[200:203], v[60:63], v[220:223], v[200:203]\n\t" \
  "v_mfma_f32_16x16x32_bf16 v[204:207], v[132:135], v[220:223], v[204:207]\n\t" \
  "ds_read_b64_tr_b16 v[220:221], v237 offset:4096\n\t" \
  "ds_read_b64_tr_b16 v[222:223], v237 offset:6144\n\t" \
  "s_nop 7\n\t" \
  "v_cvt_pk_bf16_f32 v176, v176, v177\n\t" \
  "v_cvt_pk_bf16_f32 v177, v178, v179\n\t" \
  "v_cvt_pk_bf16_f32 v178, v180, v181\n\t" \
  "v_cvt_pk_bf16_f32 v179, v182, v183\n\t" \
  "v_cvt_pk_bf16_f32 v184, v184, v185\n\t" \
  "v_cvt_pk_bf16_f32 v185, v186, v187\n\t" \
  "v_cvt_pk_bf16_f32 v186, v188, v189\n\t" \
  "v_cvt_pk_bf16_f32 v187, v190, v191\n\t" \
  "v_cvt_pk_bf16_f32 v192, v192, v193\n\t" \
  "v_cvt_pk_bf16_f32 v193, v194, v195\n\t" \
  "v_cvt_pk_bf16_f32 v194, v196, v197\n\t" \
  "v_cvt_pk_bf16_f32 v195, v198, v199\n\t" \
  "v_cvt_pk_bf16_f32 v200, v200, v201\n\t" \
  "v_cvt_pk_bf16_f32 v201, v202, v203\n\t" \
  "v_cvt_pk_bf16_f32 v202, v204, v205\n\t" \
  "v_cvt_pk_bf16_f32 v203, v206, v207\n\t" \
  "v_cvt_pk_bf16_f32 v180, v144, v145\n\t" \
  "v_cvt_pk_bf16_f32 v181, v146, v147\n\t" \
  "v_cvt_pk_bf16_f32 v182, v148, v149\n\t" \
  "v_cvt_pk_bf16_f32 v183, v150, v151\n\t" \
  "v_cvt_pk_bf16_f32 v188, v152, v153\n\t" \
  "v_cvt_pk_bf16_f32 v189, v154, v155\n\t" \
  "v_cvt_pk_bf16_f32 v190, v156, v157\n\t" \
  "v_cvt_pk_bf16_f32 v191, v158, v159\n\t" \
  "v_cvt_pk_bf16_f32 v196, v160, v161\n\t" \
  "v_cvt_pk_bf16_f32 v197, v162, v163\n\t" \
  "v_cvt_pk_bf16_f32 v198, v164, v165\n\t" \
  "v_cvt_pk_bf16_f32 v199, v166, v167\n\t" \
  "v_cvt_pk_bf16_f32 v204, v168, v169\n\t" \
  "v_cvt_pk_bf16_f32 v205, v170, v171\n\t" \
  "v_cvt_pk_bf16_f32 v206, v172, v173\n\t" \
  "v_cvt_pk_bf16_f32 v207, v174, v175\n\t" \
  "s_nop 1\n\t" \
  "s_waitcnt lgkmcnt(10)\n\t" \
  "v_mfma_f32_16x16x32_bf16 v[144:147], v[224:227], v[0:3], 0\n\t" \
  "v_mfma_f32_16x16x32_bf16 v[148:151], v[224:227], v[72:75], 0\n\t" \
  "s_mov_b32 s72, 12288\n\t" \
  "buffer_load_dwordx4 v[60:63], %[avo], s[52:55], s72 offen offset:3072\n\t" \
  "ds_read_b64_tr_b16 v[224:225], v238 offset:4096\n\t" \
  "ds_read_b64_tr_b16 v[226:227], v238 offset:6144\n\t" \
  "s_waitcnt lgkmcnt(10)\n\t" \
  "v_mfma_f32_16x16x32_bf16 v[152:155], v[228:231], v[0:3], 0\n\t" \
  "v_mfma_f32_16x16x32_bf16 v[156:159], v[228:231], v[72:75], 0\n\t" \
  "s_add_u32 s71, s80, 128\n\t" \
  "s_add_u32 m0, s76, 73728\n\t" \
  "s_nop 0\n\t" \
  "buffer_load_dwordx4 %[wvo], s[44:47], s71 offen lds\n\t" \
  "ds_read_b64_tr_b16 v[228:229], v239 offset:4096\n\t" \
  "ds_read_b64_tr_b16 v[230:231], v239 offset:6144\n\t" \
  "s_waitcnt lgkmcnt(10)\n\t" \
  "v_mfma_f32_16x16x32_bf16 v[160:163], v[208:211], v[0:3], 0\n\t" \
  "v_mfma_f32_16x16x32_bf16 v[164:167], v[208:211], v[72:75], 0\n\t" \
  "s_mov_b32 s72, 30720\n\t" \
  "buffer_load_dwordx4 v[132:135], %[avo], s[52:55], s72 offen offset:3072\n\t" \
  "ds_read_b64_tr_b16 v[208:209], v236 offset:8192\n\t" \
  "ds_read_b64_tr_b16 v[210:211], v236 offset:10240\n\t" \
  "s_waitcnt lgkmcnt(10)\n\t" \
  "v_mfma_f32_16x16x32_bf16 v[168:171], v[212:215], v[0:3], 0\n\t" \
  "v_mfma_f32_16x16x32_bf16 v[172:175], v[212:215], v[72:75], 0\n\t" \
  "buffer_store_dwordx4 v[176:179], %[vvo], s[60:63], 0 offen offset:2048 sc1\n\t" \
  "ds_read_b64_tr_b16 v[212:213], v237 offset:8192\n\t" \
  "ds_read_b64_tr_b16 v[214:215], v237 offset:10240\n\t" \
  "s_waitcnt lgkmcnt(10)\n\t" \
  "v_mfma_f32_16x16x32_bf16 v[144:147], v[216:219], v[4:7], v[144:147]\n\t" \
  "v_mfma_f32_16x16x32_bf16 v[148:151], v[216:219], v[76:79], v[148:151]\n\t" \
  "ds_read_b64_tr_b16 v[216:217], v238 offset:8192\n\t" \
  "ds_read_b64_tr_b16 v[218:219], v238 offset:10240\n\t" \
  "s_waitcnt lgkmcnt(10)\n\t" \
  "v_mfma_f32_16x16x32_bf16 v[152:155], v[220:223], v[4:7], v[152:155]\n\t" \
  "v_mfma_f32_16x16x32_bf16 v[156:159], v[220:223], v[76:79], v[156:159]\n\t" \
  "s_add_u32 s71, s80, 524416\n\t" \
  "s_add_u32 m0, s76, 81920\n\t" \
  "s_nop 0\n\t" \
  "buffer_load_dwordx4 %[wvo], s[44:47], s71 offen lds\n\t" \
  "ds_read_b64_tr_b16 v[220:221], v239 offset:8192\n\t" \
  "ds_read_b64_tr_b16 v[222:223], v239 offset:10240\n\t" \
  "s_waitcnt lgkmcnt(10)\n\t" \
  "v_mfma_f32_16x16x32_bf16 v[160:163], v[224:227], v[4:7], v[160:163]\n\t" \
  "v_mfma_f32_16x16x32_bf16 v[164:167], v[224:227], v[76:79], v[164:167]\n\t" \
  "ds_read_b64_tr_b16 v[224:225], v236 offset:12288\n\t" \
  "ds_read_b64_tr_b16 v[226:227], v236 offset:14336\n\t" \
  "s_waitcnt lgkmcnt(10)\n\t" \
  "v_mfma_f32_16x16x32_bf16 v[168:171], v[228:231], v[4:7], v[168:171]\n\t" \
  "v_mfma_f32_16x16x32_bf16 v[172:175], v[228:231], v[76:79], v[172:175]\n\t" \
  "ds_read_b64_tr_b16 v[228:229], v237 offset:12288\n\t" \
  "ds_read_b64_tr_b16 v[230:231], v237 offset:14336\n\t" \
  "s_waitcnt lgkmcnt(10)\n\t" \
  "v_mfma_f32_16x16x32_bf16 v[144:147], v[208:211], v[8:11], v[144:147]\n\t" \
  "v_mfma_f32_16x16x32_bf16 v[148:151], v[208:211], v[80:83], v[148:151]\n\t" \
  "ds_read_b64_tr_b16 v[208:209], v238 offset:12288\n\t" \
  "ds_read_b64_tr_b16 v[210:211], v238 offset:14336\n\t" \
  "s_waitcnt lgkmcnt(10)\n\t" \
  "v_mfma_f32_16x16x32_bf16 v[152:155], v[212:215], v[8:11], v[152:155]\n\t" \
  "v_mfma_f32_16x16x32_bf16 v[156:159], v[212:215], v[80:83], v[156:159]\n\t" \
  "s_add_u32 s71, s80, 1048704\n\t" \
  "s_add_u32 m0, s76, 90112\n\t" \
  "s_nop 0\n\t" \
  "buffer_load_dwordx4 %[wvo], s[44:47], s71 offen lds\n\t" \
  "ds_read_b64_tr_b16 v[212:213], v239 offset:12288\n\t" \
  "ds_read_b64_tr_b16 v[214:215], v239 offset:14336\n\t" \
  "s_waitcnt lgkmcnt(10)\n\t" \
  "v_mfma_f32_16x16x32_bf16 v[160:163], v[216:219], v[8:11], v[160:163]\n\t" \
  "v_mfma_f32_16x16x32_bf16 v[164:167], v[216:219], v[80:83], v[164:167]\n\t" \
  "ds_read_b64_tr_b16 v[216:217], v236 offset:16384\n\t" \
  "ds_read_b64_tr_b16 v[218:219], v236 offset:18432\n\t" \
  "s_waitcnt lgkmcnt(10)\n\t" \
  "v_mfma_f32_16x16x32_bf16 v[168:171], v[220:223], v[8:11], v[168:171]\n\t" \
  "v_mfma_f32_16x16x32_bf16 v[172:175], v[220:223], v[80:83], v[172:175]\n\t" \
  "buffer_store_dwordx4 v[184:187], %[vvo], s[60:63], 0 offen offset:2304 sc1\n\t" \
  "ds_read_b64_tr_b16 v[220:221], v237 offset:16384\n\t" \
  "ds_read_b64_tr_b16 v[222:223], v237 offset:18432\n\t" \
  "s_waitcnt lgkmcnt(10)\n\t" \
  "v_mfma_f32_16x16x32_bf16 v[144:147], v[224:227], v[12:15], v[144:147]\n\t" \
  "v_mfma_f32_16x16x32_bf16 v[148:151], v[224:227], v[84:87], v[148:151]\n\t" \
  "ds_read_b64_tr_b16 v[224:225], v238 offset:16384\n\t" \
  "ds_read_b64_tr_b16 v[226:227], v238 offset:18432\n\t" \
  "s_waitcnt lgkmcnt(10)\n\t" \
  "v_mfma_f32_16x16x32_bf16 v[152:155], v[228:231], v[12:15], v[152:155]\n\t" \
  "v_mfma_f32_16x16x32_bf16 v[156:159], v[228:231], v[84:87], v[156:159]\n\t" \
  "ds_read_b64_tr_b16 v[228:229], v239 offset:16384\n\t" \
  "ds_read_b64_tr_b16 v[230:231], v239 offset:18432\n\t" \
  "s_waitcnt lgkmcnt(10)\n\t" \
  "v_mfma_f32_16x16x32_bf16 v[160:163], v[208:211], v[12:15], v[160:163]\n\t" \
  "v_mfma_f32_16x16x32_bf16 v[164:167], v[208:211], v[84:87], v[164:167]\n\t" \
  "ds_read_b64_tr_b16 v[208:209], v236 offset:20480\n\t" \
  "ds_read_b64_tr_b16 v[210:211], v236 offset:22528\n\t" \
  "s_waitcnt lgkmcnt(10)\n\t" \
  "v_mfma_f32_16x16x32_bf16 v[168:171], v[212:215], v[12:15], v[168:171]\n\t" \
  "v_mfma_f32_16x16x32_bf16 v[172:175], v[212:215], v[84:87], v[172:175]\n\t" \
  "ds_read_b64_tr_b16 v[212:213], v237 offset:20480\n\t" \
  "ds_read_b64_tr_b16 v[214:215], v237 offset:22528\n\t" \
  "s_waitcnt lgkmcnt(10)\n\t" \
  "v_mfma_f32_16x16x32_bf16 v[144:147], v[216:219], v[16:19], v[144:147]\n\t" \
  "v_mfma_f32_16x16x32_bf16 v[148:151], v[216:219], v[88:91], v[148:151]\n\t" \
  "ds_read_b64_tr_b16 v[216:217], v238 offset:20480\n\t" \
  "ds_read_b64_tr_b16 v[218:219], v238 offset:22528\n\t" \
  "s_waitcnt lgkmcnt(10)\n\t" \
  "v_mfma_f32_16x16x32_bf16 v[152:155], v[220:223], v[16:19], v[152:155]\n\t" \
  "v_mfma_f32_16x16x32_bf16 v[156:159], v[220:223], v[88:91], v[156:159]\n\t" \
  "ds_read_b64_tr_b16 v[220:221], v239 offset:20480\n\t" \
  "ds_read_b64_tr_b16 v[222:223], v239 offset:22528\n\t" \
  "s_waitcnt lgkmcnt(10)\n\t" \
  "v_mfma_f32_16x16x32_bf16 v[160:163], v[224:227], v[16:19], v[160:163]\n\t" \
  "v_mfma_f32_16x16x32_bf16 v[164:167], v[224:227], v[88:91], v[164:167]\n\t" \
  "ds_read_b64_tr_b16 v[224:225], v236 offset:24576\n\t" \
  "ds_read_b64_tr_b16 v[226:227], v236 offset:26624\n\t" \
  "s_waitcnt lgkmcnt(10)\n\t" \
  "v_mfma_f32_16x16x32_bf16 v[168:171], v[228:231], v[16:19], v[168:171]\n\t" \
  "v_mfma_f32_16x16x32_bf16 v[172:175], v[228:231], v[88:91], v[172:175]\n\t" \
  "buffer_store_dwordx4 v[192:195], %[vvo], s[60:63], 0 offen offset:3072 sc1\n\t" \
  "ds_read_b64_tr_b16 v[228:229], v237 offset:24576\n\t" \
  "ds_read_b64_tr_b16 v[230:231], v237 offset:26624\n\t" \
  "s_waitcnt lgkmcnt(10)\n\t" \
  "v_mfma_f32_16x16x32_bf16 v[144:147], v[208:211], v[20:23], v[144:147]\n\t" \
  "v_mfma_f32_16x16x32_bf16 v[148:151], v[208:211], v[92:95], v[148:151]\n\t" \
  "ds_read_b64_tr_b16 v[208:209], v238 offset:24576\n\t" \
  "ds_read_b64_tr_b16 v[210:211], v238 offset:26624\n\t" \
  "s_waitcnt lgkmcnt(10)\n\t" \
  "v_mfma_f32_16x16x32_bf16 v[152:155], v[212:215], v[20:23], v[152:155]\n\t" \
  "v_mfma_f32_16x16x32_bf16 v[156:159], v[212:215], v[92:95], v[156:159]\n\t" \
  "ds_read_b64_tr_b16 v[212:213], v239 offset:24576\n\t" \
  "ds_read_b64_tr_b16 v[214:215], v239 offset:26624\n\t" \
  "s_waitcnt lgkmcnt(10)\n\t" \
  "v_mfma_f32_16x16x32_bf16 v[160:163], v[216:219], v[20:23], v[160:163]\n\t" \
  "v_mfma_f32_16x16x32_bf16 v[164:167], v[216:219], v[92:95], v[164:167]\n\t" \
  "ds_read_b64_tr_b16 v[216:217], v236 offset:28672\n\t" \
  "ds_read_b64_tr_b16 v[218:219], v236 offset:30720\n\t" \
  "s_waitcnt lgkmcnt(10)\n\t" \
  "v_mfma_f32_16x16x32_bf16 v[168:171], v[220:223], v[20:23], v[168:171]\n\t" \
  "v_mfma_f32_16x16x32_bf16 v[172:175], v[220:223], v[92:95], v[172:175]\n\t" \
  "ds_read_b64_tr_b16 v[220:221], v237 offset:28672\n\t" \
  "ds_read_b64_tr_b16 v[222:223], v237 offset:30720\n\t" \
  "s_waitcnt lgkmcnt(10)\n\t" \
  "v_mfma_f32_16x16x32_bf16 v[144:147], v[224:227], v[24:27], v[144:147]\n\t" \
  "v_mfma_f32_16x16x32_bf16 v[148:151], v[224:227], v[96:99], v[148:151]\n\t" \
  "ds_read_b64_tr_b16 v[224:225], v238 offset:28672\n\t" \
  "ds_read_b64_tr_b16 v[226:227], v238 offset:30720\n\t" \
  "s_waitcnt lgkmcnt(10)\n\t" \
  "v_mfma_f32_16x16x32_bf16 v[152:155], v[228:231], v[24:27], v[152:155]\n\t" \
  "v_mfma_f32_16x16x32_bf16 v[156:159], v[228:231], v[96:99], v[156:159]\n\t" \
  "ds_read_b64_tr_b16 v[228:229], v239 offset:28672\n\t" \
  "ds_read_b64_tr_b16 v[230:231], v239 offset:30720\n\t" \
  "s_waitcnt lgkmcnt(10)\n\t" \
  "v_mfma_f32_16x16x32_bf16 v[160:163], v[208:211], v[24:27], v[160:163]\n\t" \
  "v_mfma_f32_16x16x32_bf16 v[164:167], v[208:211], v[96:99], v[164:167]\n\t" \
  "s_waitcnt vmcnt(14)\n\t" \
  "s_barrier\n\t" \
  "ds_read_b64_tr_b16 v[208:209], v236 offset:32768\n\t" \
  "ds_read_b64_tr_b16 v[210:211], v236 offset:34816\n\t" \
  "s_waitcnt lgkmcnt(10)\n\t" \
  "v_mfma_f32_16x16x32_bf16 v[168:171], v[212:215], v[24:27], v[168:171]\n\t" \
  "v_mfma_f32_16x16x32_bf16 v[172:175], v[212:215], v[96:99], v[172:175]\n\t" \
  "buffer_store_dwordx4 v[200:203], %[vvo], s[60:63], 0 offen offset:3328 sc1\n\t" \
  "ds_read_b64_tr_b16 v[212:213], v237 offset:32768\n\t" \
  "ds_read_b64_tr_b16 v[214:215], v237 offset:34816\n\t" \
  "s_waitcnt lgkmcnt(10)\n\t" \
  "v_mfma_f32_16x16x32_bf16 v[144:147], v[216:219], v[28:31], v[144:147]\n\t" \
  "v_mfma_f32_16x16x32_bf16 v[148:151], v[216:219], v[100:103], v[148:151]\n\t" \
  "ds_read_b64_tr_b16 v[216:217], v238 offset:32768\n\t" \
  "ds_read_b64_tr_b16 v[218:219], v238 offset:34816\n\t" \
  "s_waitcnt lgkmcnt(10)\n\t" \
  "v_mfma_f32_16x16x32_bf16 v[152:155], v[220:223], v[28:31], v[152:155]\n\t" \
  "v_mfma_f32_16x16x32_bf16 v[156:159], v[220:223], v[100:103], v[156:159]\n\t" \
  "s_add_u32 s71, s80, 1572992\n\t" \
  "s_add_u32 m0, s76, 98304\n\t" \
  "s_nop 0\n\t" \
  "buffer_load_dwordx4 %[wvo], s[44:47], s71 offen lds\n\t" \
  "ds_read_b64_tr_b16 v[220:221], v239 offset:32768\n\t" \
  "ds_read_b64_tr_b16 v[222:223], v239 offset:34816\n\t" \
  "s_waitcnt lgkmcnt(10)\n\t" \
  "v_mfma_f32_16x16x32_bf16 v[160:163], v[224:227], v[28:31], v[160:163]\n\t" \
  "v_mfma_f32_16x16x32_bf16 v[164:167], v[224:227], v[100:103], v[164:167]\n\t" \
  "ds_read_b64_tr_b16 v[224:225], v236 offset:36864\n\t" \
  "ds_read_b64_tr_b16 v[226:227], v236 offset:38912\n\t" \
  "s_waitcnt lgkmcnt(10)\n\t" \
  "v_mfma_f32_16x16x32_bf16 v[168:171], v[228:231], v[28:31], v[168:171]\n\t" \
  "v_mfma_f32_16x16x32_bf16 v[172:175], v[228:231], v[100:103], v[172:175]\n\t" \
  "ds_read_b64_tr_b16 v[228:229], v237 offset:36864\n\t" \
  "ds_read_b64_tr_b16 v[230:231], v237 offset:38912\n\t" \
  "s_waitcnt lgkmcnt(10)\n\t" \
  "v_mfma_f32_16x16x32_bf16 v[144:147], v[208:211], v[32:35], v[144:147]\n\t" \
  "v_mfma_f32_16x16x32_bf16 v[148:151], v[208:211], v[104:107], v[148:151]\n\t" \
  "ds_read_b64_tr_b16 v[208:209], v238 offset:36864\n\t" \
  "ds_read_b64_tr_b16 v[210:211], v238 offset:38912\n\t" \
  "s_waitcnt lgkmcnt(10)\n\t" \
  "v_mfma_f32_16x16x32_bf16 v[152:155], v[212:215], v[32:35], v[152:155]\n\t" \
  "v_mfma_f32_16x16x32_bf16 v[156:159], v[212:215], v[104:107], v[156:159]\n\t" \
  "s_add_u32 s71, s80, 2097280\n\t" \
  "s_add_u32 m0, s76, 106496\n\t" \
  "s_nop 0\n\t" \
  "buffer_load_dwordx4 %[wvo], s[44:47], s71 offen lds\n\t" \
  "ds_read_b64_tr_b16 v[212:213], v239 offset:36864\n\t" \
  "ds_read_b64_tr_b16 v[214:215], v239 offset:38912\n\t" \
  "s_waitcnt lgkmcnt(10)\n\t" \
  "v_mfma_f32_16x16x32_bf16 v[160:163], v[216:219], v[32:35], v[160:163]\n\t" \
  "v_mfma_f32_16x16x32_bf16 v[164:167], v[216:219], v[104:107], v[164:167]\n\t" \
  "ds_read_b64_tr_b16 v[216:217], v236 offset:40960\n\t" \
  "ds_read_b64_tr_b16 v[218:219], v236 offset:43008\n\t" \
  "s_waitcnt lgkmcnt(10)\n\t" \
  "v_mfma_f32_16x16x32_bf16 v[168:171], v[220:223], v[32:35], v[168:171]\n\t" \
  "v_mfma_f32_16x16x32_bf16 v[172:175], v[220:223], v[104:107], v[172:175]\n\t" \
  "buffer_store_dwordx4 v[180:183], %[vvo], s[60:63], 0 offen offset:0 sc1\n\t" \
  "ds_read_b64_tr_b16 v[220:221], v237 offset:40960\n\t" \
  "ds_read_b64_tr_b16 v[222:223], v237 offset:43008\n\t" \
  "s_waitcnt lgkmcnt(10)\n\t" \
  "v_mfma_f32_16x16x32_bf16 v[144:147], v[224:227], v[36:39], v[144:147]\n\t" \
  "v_mfma_f32_16x16x32_bf16 v[148:151], v[224:227], v[108:111], v[148:151]\n\t" \
  "ds_read_b64_tr_b16 v[224:225], v238 offset:40960\n\t" \
  "ds_read_b64_tr_b16 v[226:227], v238 offset:43008\n\t" \
  "s_waitcnt lgkmcnt(10)\n\t" \
  "v_mfma_f32_16x16x32_bf16 v[152:155], v[228:231], v[36:39], v[152:155]\n\t" \
  "v_mfma_f32_16x16x32_bf16 v[156:159], v[228:231], v[108:111], v[156:159]\n\t" \
  "s_add_u32 s71, s80, 2621568\n\t" \
  "s_add_u32 m0, s76, 114688\n\t" \
  "s_nop 0\n\t" \
  "buffer_load_dwordx4 %[wvo], s[44:47], s71 offen lds\n\t" \
  "ds_read_b64_tr_b16 v[228:229], v239 offset:40960\n\t" \
  "ds_read_b64_tr_b16 v[230:231], v239 offset:43008\n\t" \
  "s_waitcnt lgkmcnt(10)\n\t" \
  "v_mfma_f32_16x16x32_bf16 v[160:163], v[208:211], v[36:39], v[160:163]\n\t" \
  "v_mfma_f32_16x16x32_bf16 v[164:167], v[208:211], v[108:111], v[164:167]\n\t" \
  "ds_read_b64_tr_b16 v[208:209], v236 offset:45056\n\t" \
  "ds_read_b64_tr_b16 v[210:211], v236 offset:47104\n\t" \
  "s_waitcnt lgkmcnt(10)\n\t" \
  "v_mfma_f32_16x16x32_bf16 v[168:171], v[212:215], v[36:39], v[168:171]\n\t" \
  "v_mfma_f32_16x16x32_bf16 v[172:175], v[212:215], v[108:111], v[172:175]\n\t" \
  "ds_read_b64_tr_b16 v[212:213], v237 offset:45056\n\t" \
  "ds_read_b64_tr_b16 v[214:215], v237 offset:47104\n\t" \
  "s_waitcnt lgkmcnt(10)\n\t" \
  "v_mfma_f32_16x16x32_bf16 v[144:147], v[216:219], v[40:43], v[144:147]\n\t" \
  "v_mfma_f32_16x16x32_bf16 v[148:151], v[216:219], v[112:115], v[148:151]\n\t" \
  "ds_read_b64_tr_b16 v[216:217], v238 offset:45056\n\t" \
  "ds_read_b64_tr_b16 v[218:219], v238 offset:47104\n\t" \
  "s_waitcnt lgkmcnt(10)\n\t" \
  "v_mfma_f32_16x16x32_bf16 v[152:155], v[220:223], v[40:43], v[152:155]\n\t" \
  "v_mfma_f32_16x16x32_bf16 v[156:159], v[220:223], v[112:115], v[156:159]\n\t" \
  "s_add_u32 s71, s80, 3145856\n\t" \
  "s_add_u32 m0, s76, 122880\n\t" \
  "s_nop 0\n\t" \
  "buffer_load_dwordx4 %[wvo], s[44:47], s71 offen lds\n\t" \
  "ds_read_b64_tr_b16 v[220:221], v239 offset:45056\n\t" \
  "ds_read_b64_tr_b16 v[222:223], v239 offset:47104\n\t" \
  "s_waitcnt lgkmcnt(10)\n\t" \
  "v_mfma_f32_16x16x32_bf16 v[160:163], v[224:227], v[40:43], v[160:163]\n\t" \
  "v_mfma_f32_16x16x32_bf16 v[164:167], v[224:227], v[112:115], v[164:167]\n\t" \
  "ds_read_b64_tr_b16 v[224:225], v236 offset:49152\n\t" \
  "ds_read_b64_tr_b16 v[226:227], v236 offset:51200\n\t" \
  "s_waitcnt lgkmcnt(10)\n\t" \
  "v_mfma_f32_16x16x32_bf16 v[168:171], v[228:231], v[40:43], v[168:171]\n\t" \
  "v_mfma_f32_16x16x32_bf16 v[172:175], v[228:231], v[112:115], v[172:175]\n\t" \
  "buffer_store_dwordx4 v[188:191], %[vvo], s[60:63], 0 offen offset:256 sc1\n\t" \
  "ds_read_b64_tr_b16 v[228:229], v237 offset:49152\n\t" \
  "ds_read_b64_tr_b16 v[230:231], v237 offset:51200\n\t" \
  "s_waitcnt lgkmcnt(10)\n\t" \
  "v_mfma_f32_16x16x32_bf16 v[144:147], v[208:211], v[44:47], v[144:147]\n\t" \
  "v_mfma_f32_16x16x32_bf16 v[148:151], v[208:211], v[116:119], v[148:151]\n\t" \
  "ds_read_b64_tr_b16 v[208:209], v238 offset:49152\n\t" \
  "ds_read_b64_tr_b16 v[210:211], v238 offset:51200\n\t" \
  "s_waitcnt lgkmcnt(10)\n\t" \
  "v_mfma_f32_16x16x32_bf16 v[152:155], v[212:215], v[44:47], v[152:155]\n\t" \
  "v_mfma_f32_16x16x32_bf16 v[156:159], v[212:215], v[116:119], v[156:159]\n\t" \
  "ds_read_b64_tr_b16 v[212:213], v239 offset:49152\n\t" \
  "ds_read_b64_tr_b16 v[214:215], v239 offset:51200\n\t" \
  "s_waitcnt lgkmcnt(10)\n\t" \
  "v_mfma_f32_16x16x32_bf16 v[160:163], v[216:219], v[44:47], v[160:163]\n\t" \
  "v_mfma_f32_16x16x32_bf16 v[164:167], v[216:219], v[116:119], v[164:167]\n\t" \
  "ds_read_b64_tr_b16 v[216:217], v236 offset:53248\n\t" \
  "ds_read_b64_tr_b16 v[218:219], v236 offset:55296\n\t" \
  "s_waitcnt lgkmcnt(10)\n\t" \
  "v_mfma_f32_16x16x32_bf16 v[168:171], v[220:223], v[44:47], v[168:171]\n\t" \
  "v_mfma_f32_16x16x32_bf16 v[172:175], v[220:223], v[116:119], v[172:175]\n\t" \
  "ds_read_b64_tr_b16 v[220:221], v237 offset:53248\n\t" \
  "ds_read_b64_tr_b16 v[222:223], v237 offset:55296\n\t" \
  "s_waitcnt vmcnt(20)\n\t" \
  "s_waitcnt lgkmcnt(10)\n\t" \
  "v_mfma_f32_16x16x32_bf16 v[144:147], v[224:227], v[48:51], v[144:147]\n\t" \
  "v_mfma_f32_16x16x32_bf16 v[148:151], v[224:227], v[120:123], v[148:151]\n\t" \
  "ds_read_b64_tr_b16 v[224:225], v238 offset:53248\n\t" \
  "ds_read_b64_tr_b16 v[226:227], v238 offset:55296\n\t" \
  "s_waitcnt lgkmcnt(10)\n\t" \
  "v_mfma_f32_16x16x32_bf16 v[152:155], v[228:231], v[48:51], v[152:155]\n\t" \
  "v_mfma_f32_16x16x32_bf16 v[156:159], v[228:231], v[120:123], v[156:159]\n\t" \
  "ds_read_b64_tr_b16 v[228:229], v239 offset:53248\n\t" \
  "ds_read_b64_tr_b16 v[230:231], v239 offset:55296\n\t" \
  "s_waitcnt lgkmcnt(10)\n\t" \
  "v_mfma_f32_16x16x32_bf16 v[160:163], v[208:211], v[48:51], v[160:163]\n\t" \
  "v_mfma_f32_16x16x32_bf16 v[164:167], v[208:211], v[120:123], v[164:167]\n\t" \
  "ds_read_b64_tr_b16 v[208:209], v236 offset:57344\n\t" \
  "ds_read_b64_tr_b16 v[210:211], v236 offset:59392\n\t" \
  "s_waitcnt lgkmcnt(10)\n\t" \
  "v_mfma_f32_16x16x32_bf16 v[168:171], v[212:215], v[48:51], v[168:171]\n\t" \
  "v_mfma_f32_16x16x32_bf16 v[172:175], v[212:215], v[120:123], v[172:175]\n\t" \
  "buffer_store_dwordx4 v[196:199], %[vvo], s[60:63], 0 offen offset:1024 sc1\n\t" \
  "ds_read_b64_tr_b16 v[212:213], v237 offset:57344\n\t" \
  "ds_read_b64_tr_b16 v[214:215], v237 offset:59392\n\t" \
  "s_waitcnt vmcnt(19)\n\t" \
  "s_waitcnt lgkmcnt(10)\n\t" \
  "v_mfma_f32_16x16x32_bf16 v[144:147], v[216:219], v[52:55], v[144:147]\n\t" \
  "v_mfma_f32_16x16x32_bf16 v[148:151], v[216:219], v[124:127], v[148:151]\n\t" \
  "ds_read_b64_tr_b16 v[216:217], v238 offset:57344\n\t" \
  "ds_read_b64_tr_b16 v[218:219], v238 offset:59392\n\t" \
  "s_waitcnt lgkmcnt(10)\n\t" \
  "v_mfma_f32_16x16x32_bf16 v[152:155], v[220:223], v[52:55], v[152:155]\n\t" \
  "v_mfma_f32_16x16x32_bf16 v[156:159], v[220:223], v[124:127], v[156:159]\n\t" \
  "ds_read_b64_tr_b16 v[220:221], v239 offset:57344\n\t" \
  "ds_read_b64_tr_b16 v[222:223], v239 offset:59392\n\t" \
  "s_waitcnt lgkmcnt(10)\n\t" \
  "v_mfma_f32_16x16x32_bf16 v[160:163], v[224:227], v[52:55], v[160:163]\n\t" \
  "v_mfma_f32_16x16x32_bf16 v[164:167], v[224:227], v[124:127], v[164:167]\n\t" \
  "ds_read_b64_tr_b16 v[224:225], v236 offset:61440\n\t" \
  "ds_read_b64_tr_b16 v[226:227], v236 offset:63488\n\t" \
  "s_waitcnt lgkmcnt(10)\n\t" \
  "v_mfma_f32_16x16x32_bf16 v[168:171], v[228:231], v[52:55], v[168:171]\n\t" \
  "v_mfma_f32_16x16x32_bf16 v[172:175], v[228:231], v[124:127], v[172:175]\n\t" \
  "ds_read_b64_tr_b16 v[228:229], v237 offset:61440\n\t" \
  "ds_read_b64_tr_b16 v[230:231], v237 offset:63488\n\t" \
  "s_waitcnt vmcnt(17)\n\t" \
  "s_waitcnt lgkmcnt(10)\n\t" \
  "v_mfma_f32_16x16x32_bf16 v[144:147], v[208:211], v[56:59], v[144:147]\n\t" \
  "v_mfma_f32_16x16x32_bf16 v[148:151], v[208:211], v[128:131], v[148:151]\n\t" \
  "ds_read_b64_tr_b16 v[208:209], v238 offset:61440\n\t" \
  "ds_read_b64_tr_b16 v[210:211], v238 offset:63488\n\t" \
  "s_waitcnt lgkmcnt(10)\n\t" \
  "v_mfma_f32_16x16x32_bf16 v[152:155], v[212:215], v[56:59], v[152:155]\n\t" \
  "v_mfma_f32_16x16x32_bf16 v[156:159], v[212:215], v[128:131], v[156:159]\n\t" \
  "ds_read_b64_tr_b16 v[212:213], v239 offset:61440\n\t" \
  "ds_read_b64_tr_b16 v[214:215], v239 offset:63488\n\t" \
  "s_waitcnt lgkmcnt(10)\n\t" \
  "v_mfma_f32_16x16x32_bf16 v[160:163], v[216:219], v[56:59], v[160:163]\n\t" \
  "v_mfma_f32_16x16x32_bf16 v[164:167], v[216:219], v[128:131], v[164:167]\n\t" \
  "s_waitcnt vmcnt(10)\n\t" \
  "s_barrier\n\t" \
  "v_add_u32_e32 v236, 65536, v236\n\t" \
  "v_add_u32_e32 v237, 65536, v237\n\t" \
  "v_add_u32_e32 v238, 65536, v238\n\t" \
  "v_add_u32_e32 v239, 65536, v239\n\t" \
  "ds_read_b64_tr_b16 v[216:217], v236 offset:0\n\t" \
  "ds_read_b64_tr_b16 v[218:219], v236 offset:2048\n\t" \
  "s_waitcnt lgkmcnt(10)\n\t" \
  "v_mfma_f32_16x16x32_bf16 v[168:171], v[220:223], v[56:59], v[168:171]\n\t" \
  "v_mfma_f32_16x16x32_bf16 v[172:175], v[220:223], v[128:131], v[172:175]\n\t" \
  "buffer_store_dwordx4 v[204:207], %[vvo], s[60:63], 0 offen offset:1280 sc1\n\t" \
  "ds_read_b64_tr_b16 v[220:221], v237 offset:0\n\t" \
  "ds_read_b64_tr_b16 v[222:223], v237 offset:2048\n\t" \
  "s_waitcnt lgkmcnt(10)\n\t" \
  "v_mfma_f32_16x16x32_bf16 v[144:147], v[224:227], v[60:63], v[144:147]\n\t" \
  "v_mfma_f32_16x16x32_bf16 v[148:151], v[224:227], v[132:135], v[148:151]\n\t" \
  "ds_read_b64_tr_b16 v[224:225], v238 offset:0\n\t" \
  "ds_read_b64_tr_b16 v[226:227], v238 offset:2048\n\t" \
  "s_waitcnt lgkmcnt(10)\n\t" \
  "v_mfma_f32_16x16x32_bf16 v[152:155], v[228:231], v[60:63], v[152:155]\n\t" \
  "v_mfma_f32_16x16x32_bf16 v[156:159], v[228:231], v[132:135], v[156:159]\n\t" \
  "s_add_u32 s71, s80, 3670144\n\t" \
  "s_add_u32 m0, s76, 0\n\t" \
  "s_nop 0\n\t" \
  "buffer_load_dwordx4 %[wvo], s[44:47], s71 offen lds\n\t" \
  "ds_read_b64_tr_b16 v[228:229], v239 offset:0\n\t" \
  "ds_read_b64_tr_b16 v[230:231], v239 offset:2048\n\t" \
  "s_waitcnt lgkmcnt(10)\n\t" \
  "v_mfma_f32_16x16x32_bf16 v[160:163], v[208:211], v[60:63], v[160:163]\n\t" \
  "v_mfma_f32_16x16x32_bf16 v[164:167], v[208:211], v[132:135], v[164:167]\n\t" \
  "ds_read_b64_tr_b16 v[208:209], v236 offset:4096\n\t" \
  "ds_read_b64_tr_b16 v[210:211], v236 offset:6144\n\t" \
  "s_waitcnt lgkmcnt(10)\n\t" \
  "v_mfma_f32_16x16x32_bf16 v[168:171], v[212:215], v[60:63], v[168:171]\n\t" \
  "v_mfma_f32_16x16x32_bf16 v[172:175], v[212:215], v[132:135], v[172:175]\n\t" \
  "ds_read_b64_tr_b16 v[212:213], v237 offset:4096\n\t" \
  "ds_read_b64_tr_b16 v[214:215], v237 offset:6144\n\t" \
  "s_waitcnt lgkmcnt(10)\n\t" \
  "v_mfma_f32_16x16x32_bf16 v[144:147], v[216:219], v[64:67], v[144:147]\n\t" \
  "v_mfma_f32_16x16x32_bf16 v[148:151], v[216:219], v[136:139], v[148:151]\n\t" \
  "ds_read_b64_tr_b16 v[216:217], v238 offset:4096\n\t" \
  "ds_read_b64_tr_b16 v[218:219], v238 offset:6144\n\t" \
  "s_waitcnt lgkmcnt(10)\n\t" \
  "v_mfma_f32_16x16x32_bf16 v[152:155], v[220:223], v[64:67], v[152:155]\n\t" \
  "v_mfma_f32_16x16x32_bf16 v[156:159], v[220:223], v[136:139], v[156:159]\n\t" \
  "s_add_u32 s71, s80, 4194432\n\t" \
  "s_add_u32 m0, s76, 8192\n\t" \
  "s_nop 0\n\t" \
  "buffer_load_dwordx4 %[wvo], s[44:47], s71 offen lds\n\t" \
  "ds_read_b64_tr_b16 v[220:221], v239 offset:4096\n\t" \
  "ds_read_b64_tr_b16 v[222:223], v239 offset:6144\n\t" \
  "s_waitcnt lgkmcnt(10)\n\t" \
  "v_mfma_f32_16x16x32_bf16 v[160:163], v[224:227], v[64:67], v[160:163]\n\t" \
  "v_mfma_f32_16x16x32_bf16 v[164:167], v[224:227], v[136:139], v[164:167]\n\t" \
  "ds_read_b64_tr_b16 v[224:225], v236 offset:8192\n\t" \
  "ds_read_b64_tr_b16 v[226:227], v236 offset:10240\n\t" \
  "s_waitcnt lgkmcnt(10)\n\t" \
  "v_mfma_f32_16x16x32_bf16 v[168:171], v[228:231], v[64:67], v[168:171]\n\t" \
  "v_mfma_f32_16x16x32_bf16 v[172:175], v[228:231], v[136:139], v[172:175]\n\t" \
  "ds_read_b64_tr_b16 v[228:229], v237 offset:8192\n\t" \
  "ds_read_b64_tr_b16 v[230:231], v237 offset:10240\n\t" \
  "s_waitcnt lgkmcnt(10)\n\t" \
  "v_mfma_f32_16x16x32_bf16 v[144:147], v[208:211], v[68:71], v[144:147]\n\t" \
  "v_mfma_f32_16x16x32_bf16 v[148:151], v[208:211], v[140:143], v[148:151]\n\t" \
  "ds_read_b64_tr_b16 v[208:209], v238 offset:8192\n\t" \
  "ds_read_b64_tr_b16 v[210:211], v238 offset:10240\n\t" \
  "s_waitcnt lgkmcnt(10)\n\t" \
  "v_mfma_f32_16x16x32_bf16 v[152:155], v[212:215], v[68:71], v[152:155]\n\t" \
  "v_mfma_f32_16x16x32_bf16 v[156:159], v[212:215], v[140:143], v[156:159]\n\t" \
  "ds_read_b64_tr_b16 v[212:213], v239 offset:8192\n\t" \
  "ds_read_b64_tr_b16 v[214:215], v239 offset:10240\n\t" \
  "s_waitcnt lgkmcnt(10)\n\t" \
  "v_mfma_f32_16x16x32_bf16 v[160:163], v[216:219], v[68:71], v[160:163]\n\t" \
  "v_mfma_f32_16x16x32_bf16 v[164:167], v[216:219], v[140:143], v[164:167]\n\t" \
  "ds_read_b64_tr_b16 v[216:217], v236 offset:12288\n\t" \
  "ds_read_b64_tr_b16 v[218:219], v236 offset:14336\n\t" \
  "s_waitcnt lgkmcnt(10)\n\t" \
  "v_mfma_f32_16x16x32_bf16 v[168:171], v[220:223], v[68:71], v[168:171]\n\t" \
  "v_mfma_f32_16x16x32_bf16 v[172:175], v[220:223], v[140:143], v[172:175]\n\t" \
  "ds_read_b64_tr_b16 v[220:221], v237 offset:12288\n\t" \
  "ds_read_b64_tr_b16 v[222:223], v237 offset:14336\n\t" \
  "s_nop 7\n\t" \
  "v_mul_f32_e32 v144, s82, v144\n\t" \
  "v_mul_f32_e32 v145, s82, v145\n\t" \
  "v_mul_f32_e32 v146, s82, v146\n\t" \
  "v_mul_f32_e32 v147, s82, v147\n\t" \
  "v_mul_f32_e32 v148, s82, v148\n\t" \
  "v_mul_f32_e32 v149, s82, v149\n\t" \
  "v_mul_f32_e32 v150, s82, v150\n\t" \
  "v_mul_f32_e32 v151, s82, v151\n\t" \
  "v_mul_f32_e32 v152, s82, v152\n\t" \
  "v_mul_f32_e32 v153, s82, v153\n\t" \
  "v_mul_f32_e32 v154, s82, v154\n\t" \
  "v_mul_f32_e32 v155, s82, v155\n\t" \
  "v_mul_f32_e32 v156, s82, v156\n\t" \
  "v_mul_f32_e32 v157, s82, v157\n\t" \
  "v_mul_f32_e32 v158, s82, v158\n\t" \
  "v_mul_f32_e32 v159, s82, v159\n\t" \
  "v_mul_f32_e32 v160, s82, v160\n\t" \
  "v_mul_f32_e32 v161, s82, v161\n\t" \
  "v_mul_f32_e32 v162, s82, v162\n\t" \
  "v_mul_f32_e32 v163, s82, v163\n\t" \
  "v_mul_f32_e32 v164, s82, v164\n\t" \
  "v_mul_f32_e32 v165, s82, v165\n\t" \
  "v_mul_f32_e32 v166, s82, v166\n\t" \
  "v_mul_f32_e32 v167, s82, v167\n\t" \
  "v_mul_f32_e32 v168, s82, v168\n\t" \
  "v_mul_f32_e32 v169, s82, v169\n\t" \
  "v_mul_f32_e32 v170, s82, v170\n\t" \
  "v_mul_f32_e32 v171, s82, v171\n\t" \
  "v_mul_f32_e32 v172, s82, v172\n\t" \
  "v_mul_f32_e32 v173, s82, v173\n\t" \
  "v_mul_f32_e32 v174, s82, v174\n\t" \
  "v_mul_f32_e32 v175, s82, v175\n\t" \
  "v_cvt_pk_bf16_f32 v144, v144, v145\n\t" \
  "v_cvt_pk_bf16_f32 v145, v146, v147\n\t" \
  "v_cvt_pk_bf16_f32 v146, v152, v153\n\t" \
  "v_cvt_pk_bf16_f32 v147, v154, v155\n\t" \
  "v_cvt_pk_bf16_f32 v148, v148, v149\n\t" \
  "v_cvt_pk_bf16_f32 v149, v150, v151\n\t" \
  "v_cvt_pk_bf16_f32 v150, v156, v157\n\t" \
  "v_cvt_pk_bf16_f32 v151, v158, v159\n\t" \
  "v_cvt_pk_bf16_f32 v160, v160, v161\n\t" \
  "v_cvt_pk_bf16_f32 v161, v162, v163\n\t" \
  "v_cvt_pk_bf16_f32 v162, v168, v169\n\t" \
  "v_cvt_pk_bf16_f32 v163, v170, v171\n\t" \
  "v_cvt_pk_bf16_f32 v164, v164, v165\n\t" \
  "v_cvt_pk_bf16_f32 v165, v166, v167\n\t" \
  "v_cvt_pk_bf16_f32 v166, v172, v173\n\t" \
  "v_cvt_pk_bf16_f32 v167, v174, v175\n\t" \
  "s_nop 1\n\t" \
  "s_waitcnt lgkmcnt(10)\n\t" \
  "v_mfma_f32_16x16x32_bf16 v[176:179], v[224:227], v[0:3], 0\n\t" \
  "v_mfma_f32_16x16x32_bf16 v[180:183], v[224:227], v[72:75], 0\n\t" \
  "ds_read_b64_tr_b16 v[224:225], v238 offset:12288\n\t" \
  "ds_read_b64_tr_b16 v[226:227], v238 offset:14336\n\t" \
  "s_waitcnt lgkmcnt(10)\n\t" \
  "v_mfma_f32_16x16x32_bf16 v[184:187], v[228:231], v[0:3], 0\n\t" \
  "v_mfma_f32_16x16x32_bf16 v[188:191], v[228:231], v[72:75], 0\n\t" \
  "ds_read_b64_tr_b16 v[228:229], v239 offset:12288\n\t" \
  "ds_read_b64_tr_b16 v[230:231], v239 offset:14336\n\t" \
  "s_waitcnt lgkmcnt(10)\n\t" \
  "v_mfma_f32_16x16x32_bf16 v[192:195], v[208:211], v[0:3], 0\n\t" \
  "v_mfma_f32_16x16x32_bf16 v[196:199], v[208:211], v[72:75], 0\n\t" \
  "ds_read_b64_tr_b16 v[208:209], v236 offset:16384\n\t" \
  "ds_read_b64_tr_b16 v[210:211], v236 offset:18432\n\t" \
  "s_waitcnt lgkmcnt(10)\n\t" \
  "v_mfma_f32_16x16x32_bf16 v[200:203], v[212:215], v[0:3], 0\n\t" \
  "v_mfma_f32_16x16x32_bf16 v[204:207], v[212:215], v[72:75], 0\n\t" \
  "buffer_store_dwordx4 v[144:147], %[qvo], s[64:67], 0 offen offset:0 sc1\n\t" \
  "ds_read_b64_tr_b16 v[212:213], v237 offset:16384\n\t" \
  "ds_read_b64_tr_b16 v[214:215], v237 offset:18432\n\t" \
  "s_waitcnt lgkmcnt(10)\n\t" \
  "v_mfma_f32_16x16x32_bf16 v[176:179], v[216:219], v[4:7], v[176:179]\n\t" \
  "v_mfma_f32_16x16x32_bf16 v[180:183], v[216:219], v[76:79], v[180:183]\n\t" \
  "ds_read_b64_tr_b16 v[216:217], v238 offset:16384\n\t" \
  "ds_read_b64_tr_b16 v[218:219], v238 offset:18432\n\t" \
  "s_waitcnt lgkmcnt(10)\n\t" \
  "v_mfma_f32_16x16x32_bf16 v[184:187], v[220:223], v[4:7], v[184:187]\n\t" \
  "v_mfma_f32_16x16x32_bf16 v[188:191], v[220:223], v[76:79], v[188:191]\n\t" \
  "ds_read_b64_tr_b16 v[220:221], v239 offset:16384\n\t" \
  "ds_read_b64_tr_b16 v[222:223], v239 offset:18432\n\t" \
  "s_waitcnt lgkmcnt(10)\n\t" \
  "v_mfma_f32_16x16x32_bf16 v[192:195], v[224:227], v[4:7], v[192:195]\n\t" \
  "v_mfma_f32_16x16x32_bf16 v[196:199], v[224:227], v[76:79], v[196:199]\n\t" \
  "ds_read_b64_tr_b16 v[224:225], v236 offset:20480\n\t" \
  "ds_read_b64_tr_b16 v[226:227], v236 offset:22528\n\t" \
  "s_waitcnt lgkmcnt(10)\n\t" \
  "v_mfma_f32_16x16x32_bf16 v[200:203], v[228:231], v[4:7], v[200:203]\n\t" \
  "v_mfma_f32_16x16x32_bf16 v[204:207], v[228:231], v[76:79], v[204:207]\n\t" \
  "ds_read_b64_tr_b16 v[228:229], v237 offset:20480\n\t" \
  "ds_read_b64_tr_b16 v[230:231], v237 offset:22528\n\t" \
  "s_waitcnt lgkmcnt(10)\n\t" \
  "v_mfma_f32_16x16x32_bf16 v[176:179], v[208:211], v[8:11], v[176:179]\n\t" \
  "v_mfma_f32_16x16x32_bf16 v[180:183], v[208:211], v[80:83], v[180:183]\n\t" \
  "ds_read_b64_tr_b16 v[208:209], v238 offset:20480\n\t" \
  "ds_read_b64_tr_b16 v[210:211], v238 offset:22528\n\t" \
  "s_waitcnt lgkmcnt(10)\n\t" \
  "v_mfma_f32_16x16x32_bf16 v[184:187], v[212:215], v[8:11], v[184:187]\n\t" \
  "v_mfma_f32_16x16x32_bf16 v[188:191], v[212:215], v[80:83], v[188:191]\n\t" \
  "ds_read_b64_tr_b16 v[212:213], v239 offset:20480\n\t" \
  "ds_read_b64_tr_b16 v[214:215], v239 offset:22528\n\t" \
  "s_waitcnt lgkmcnt(10)\n\t" \
  "v_mfma_f32_16x16x32_bf16 v[192:195], v[216:219], v[8:11], v[192:195]\n\t" \
  "v_mfma_f32_16x16x32_bf16 v[196:199], v[216:219], v[80:83], v[196:199]\n\t" \
  "ds_read_b64_tr_b16 v[216:217], v236 offset:24576\n\t" \
  "ds_read_b64_tr_b16 v[218:219], v236 offset:26624\n\t" \
  "s_waitcnt lgkmcnt(10)\n\t" \
  "v_mfma_f32_16x16x32_bf16 v[200:203], v[220:223], v[8:11], v[200:203]\n\t" \
  "v_mfma_f32_16x16x32_bf16 v[204:207], v[220:223], v[80:83], v[204:207]\n\t" \
  "ds_read_b64_tr_b16 v[220:221], v237 offset:24576\n\t" \
  "ds_read_b64_tr_b16 v[222:223], v237 offset:26624\n\t" \
  "s_waitcnt lgkmcnt(10)\n\t" \
  "v_mfma_f32_16x16x32_bf16 v[176:179], v[224:227], v[12:15], v[176:179]\n\t" \
  "v_mfma_f32_16x16x32_bf16 v[180:183], v[224:227], v[84:87], v[180:183]\n\t" \
  "ds_read_b64_tr_b16 v[224:225], v238 offset:24576\n\t" \
  "ds_read_b64_tr_b16 v[226:227], v238 offset:26624\n\t" \
  "s_waitcnt lgkmcnt(10)\n\t" \
  "v_mfma_f32_16x16x32_bf16 v[184:187], v[228:231], v[12:15], v[184:187]\n\t" \
  "v_mfma_f32_16x16x32_bf16 v[188:191], v[228:231], v[84:87], v[188:191]\n\t" \
  "ds_read_b64_tr_b16 v[228:229], v239 offset:24576\n\t" \
  "ds_read_b64_tr_b16 v[230:231], v239 offset:26624\n\t" \
  "s_waitcnt lgkmcnt(10)\n\t" \
  "v_mfma_f32_16x16x32_bf16 v[192:195], v[208:211], v[12:15], v[192:195]\n\t" \
  "v_mfma_f32_16x16x32_bf16 v[196:199], v[208:211], v[84:87], v[196:199]\n\t" \
  "ds_read_b64_tr_b16 v[208:209], v236 offset:28672\n\t" \
  "ds_read_b64_tr_b16 v[210:211], v236 offset:30720\n\t" \
  "s_waitcnt lgkmcnt(10)\n\t" \
  "v_mfma_f32_16x16x32_bf16 v[200:203], v[212:215], v[12:15], v[200:203]\n\t" \
  "v_mfma_f32_16x16x32_bf16 v[204:207], v[212:215], v[84:87], v[204:207]\n\t" \
  "ds_read_b64_tr_b16 v[212:213], v237 offset:28672\n\t" \
  "ds_read_b64_tr_b16 v[214:215], v237 offset:30720\n\t" \
  "s_waitcnt lgkmcnt(10)\n\t" \
  "v_mfma_f32_16x16x32_bf16 v[176:179], v[216:219], v[16:19], v[176:179]\n\t" \
  "v_mfma_f32_16x16x32_bf16 v[180:183], v[216:219], v[88:91], v[180:183]\n\t" \
  "buffer_store_dwordx4 v[148:151], %[qvo], s[64:67], 0 offen offset:256 sc1\n\t" \
  "ds_read_b64_tr_b16 v[216:217], v238 offset:28672\n\t" \
  "ds_read_b64_tr_b16 v[218:219], v238 offset:30720\n\t" \
  "s_waitcnt lgkmcnt(10)\n\t" \
  "v_mfma_f32_16x16x32_bf16 v[184:187], v[220:223], v[16:19], v[184:187]\n\t" \
  "v_mfma_f32_16x16x32_bf16 v[188:191], v[220:223], v[88:91], v[188:191]\n\t" \
  "ds_read_b64_tr_b16 v[220:221], v239 offset:28672\n\t" \
  "ds_read_b64_tr_b16 v[222:223], v239 offset:30720\n\t" \
  "s_waitcnt lgkmcnt(10)\n\t" \
  "v_mfma_f32_16x16x32_bf16 v[192:195], v[224:227], v[16:19], v[192:195]\n\t" \
  "v_mfma_f32_16x16x32_bf16 v[196:199], v[224:227], v[88:91], v[196:199]\n\t" \
  "s_waitcnt vmcnt(7)\n\t" \
  "s_barrier\n\t" \
  "ds_read_b64_tr_b16 v[224:225], v236 offset:32768\n\t" \
  "ds_read_b64_tr_b16 v[226:227], v236 offset:34816\n\t" \
  "s_waitcnt lgkmcnt(10)\n\t" \
  "v_mfma_f32_16x16x32_bf16 v[200:203], v[228:231], v[16:19], v[200:203]\n\t" \
  "v_mfma_f32_16x16x32_bf16 v[204:207], v[228:231], v[88:91], v[204:207]\n\t" \
  "ds_read_b64_tr_b16 v[228:229], v237 offset:32768\n\t" \
  "ds_read_b64_tr_b16 v[230:231], v237 offset:34816\n\t" \
  "s_waitcnt lgkmcnt(10)\n\t" \
  "v_mfma_f32_16x16x32_bf16 v[176:179], v[208:211], v[20:23], v[176:179]\n\t" \
  "v_mfma_f32_16x16x32_bf16 v[180:183], v[208:211], v[92:95], v[180:183]\n\t" \
  "ds_read_b64_tr_b16 v[208:209], v238 offset:32768\n\t" \
  "ds_read_b64_tr_b16 v[210:211], v238 offset:34816\n\t" \
  "s_waitcnt lgkmcnt(10)\n\t" \
  "v_mfma_f32_16x16x32_bf16 v[184:187], v[212:215], v[20:23], v[184:187]\n\t" \
  "v_mfma_f32_16x16x32_bf16 v[188:191], v[212:215], v[92:95], v[188:191]\n\t" \
  "ds_read_b64_tr_b16 v[212:213], v239 offset:32768\n\t" \
  "ds_read_b64_tr_b16 v[214:215], v239 offset:34816\n\t" \
  "s_waitcnt lgkmcnt(10)\n\t" \
  "v_mfma_f32_16x16x32_bf16 v[192:195], v[216:219], v[20:23], v[192:195]\n\t" \
  "v_mfma_f32_16x16x32_bf16 v[196:199], v[216:219], v[92:95], v[196:199]\n\t" \
  "ds_read_b64_tr_b16 v[216:217], v236 offset:36864\n\t" \
  "ds_read_b64_tr_b16 v[218:219], v236 offset:38912\n\t" \
  "s_waitcnt lgkmcnt(10)\n\t" \
  "v_mfma_f32_16x16x32_bf16 v[200:203], v[220:223], v[20:23], v[200:203]\n\t" \
  "v_mfma_f32_16x16x32_bf16 v[204:207], v[220:223], v[92:95], v[204:207]\n\t" \
  "ds_read_b64_tr_b16 v[220:221], v237 offset:36864\n\t" \
  "ds_read_b64_tr_b16 v[222:223], v237 offset:38912\n\t" \
  "s_waitcnt lgkmcnt(10)\n\t" \
  "v_mfma_f32_16x16x32_bf16 v[176:179], v[224:227], v[24:27], v[176:179]\n\t" \
  "v_mfma_f32_16x16x32_bf16 v[180:183], v[224:227], v[96:99], v[180:183]\n\t" \
  "ds_read_b64_tr_b16 v[224:225], v238 offset:36864\n\t" \
  "ds_read_b64_tr_b16 v[226:227], v238 offset:38912\n\t" \
  "s_waitcnt lgkmcnt(10)\n\t" \
  "v_mfma_f32_16x16x32_bf16 v[184:187], v[228:231], v[24:27], v[184:187]\n\t" \
  "v_mfma_f32_16x16x32_bf16 v[188:191], v[228:231], v[96:99], v[188:191]\n\t" \
  "ds_read_b64_tr_b16 v[228:229], v239 offset:36864\n\t" \
  "ds_read_b64_tr_b16 v[230:231], v239 offset:38912\n\t" \
  "s_waitcnt lgkmcnt(10)\n\t" \
  "v_mfma_f32_16x16x32_bf16 v[192:195], v[208:211], v[24:27], v[192:195]\n\t" \
  "v_mfma_f32_16x16x32_bf16 v[196:199], v[208:211], v[96:99], v[196:199]\n\t" \
  "ds_read_b64_tr_b16 v[208:209], v236 offset:40960\n\t" \
  "ds_read_b64_tr_b16 v[210:211], v236 offset:43008\n\t" \
  "s_waitcnt lgkmcnt(10)\n\t" \
  "v_mfma_f32_16x16x32_bf16 v[200:203], v[212:215], v[24:27], v[200:203]\n\t" \
  "v_mfma_f32_16x16x32_bf16 v[204:207], v[212:215], v[96:99], v[204:207]\n\t" \
  "ds_read_b64_tr_b16 v[212:213], v237 offset:40960\n\t" \
  "ds_read_b64_tr_b16 v[214:215], v237 offset:43008\n\t" \
  "s_waitcnt lgkmcnt(10)\n\t" \
  "v_mfma_f32_16x16x32_bf16 v[176:179], v[216:219], v[28:31], v[176:179]\n\t" \
  "v_mfma_f32_16x16x32_bf16 v[180:183], v[216:219], v[100:103], v[180:183]\n\t" \
  "ds_read_b64_tr_b16 v[216:217], v238 offset:40960\n\t" \
  "ds_read_b64_tr_b16 v[218:219], v238 offset:43008\n\t" \
  "s_waitcnt lgkmcnt(10)\n\t" \
  "v_mfma_f32_16x16x32_bf16 v[184:187], v[220:223], v[28:31], v[184:187]\n\t" \
  "v_mfma_f32_16x16x32_bf16 v[188:191], v[220:223], v[100:103], v[188:191]\n\t" \
  "buffer_store_dwordx4 v[160:163], %[qvo], s[64:67], 0 offen offset:2048 sc1\n\t" \
  "ds_read_b64_tr_b16 v[220:221], v239 offset:40960\n\t" \
  "ds_read_b64_tr_b16 v[222:223], v239 offset:43008\n\t" \
  "s_waitcnt lgkmcnt(10)\n\t" \
  "v_mfma_f32_16x16x32_bf16 v[192:195], v[224:227], v[28:31], v[192:195]\n\t" \
  "v_mfma_f32_16x16x32_bf16 v[196:199], v[224:227], v[100:103], v[196:199]\n\t" \
  "ds_read_b64_tr_b16 v[224:225], v236 offset:45056\n\t" \
  "ds_read_b64_tr_b16 v[226:227], v236 offset:47104\n\t" \
  "s_waitcnt lgkmcnt(10)\n\t" \
  "v_mfma_f32_16x16x32_bf16 v[200:203], v[228:231], v[28:31], v[200:203]\n\t" \
  "v_mfma_f32_16x16x32_bf16 v[204:207], v[228:231], v[100:103], v[204:207]\n\t" \
  "ds_read_b64_tr_b16 v[228:229], v237 offset:45056\n\t" \
  "ds_read_b64_tr_b16 v[230:231], v237 offset:47104\n\t" \
  "s_waitcnt lgkmcnt(10)\n\t" \
  "v_mfma_f32_16x16x32_bf16 v[176:179], v[208:211], v[32:35], v[176:179]\n\t" \
  "v_mfma_f32_16x16x32_bf16 v[180:183], v[208:211], v[104:107], v[180:183]\n\t" \
  "ds_read_b64_tr_b16 v[208:209], v238 offset:45056\n\t" \
  "ds_read_b64_tr_b16 v[210:211], v238 offset:47104\n\t" \
  "s_waitcnt lgkmcnt(10)\n\t" \
  "v_mfma_f32_16x16x32_bf16 v[184:187], v[212:215], v[32:35], v[184:187]\n\t" \
  "v_mfma_f32_16x16x32_bf16 v[188:191], v[212:215], v[104:107], v[188:191]\n\t" \
  "ds_read_b64_tr_b16 v[212:213], v239 offset:45056\n\t" \
  "ds_read_b64_tr_b16 v[214:215], v239 offset:47104\n\t" \
  "s_waitcnt lgkmcnt(10)\n\t" \
  "v_mfma_f32_16x16x32_bf16 v[192:195], v[216:219], v[32:35], v[192:195]\n\t" \
  "v_mfma_f32_16x16x32_bf16 v[196:199], v[216:219], v[104:107], v[196:199]\n\t" \
  "ds_read_b64_tr_b16 v[216:217], v236 offset:49152\n\t" \
  "ds_read_b64_tr_b16 v[218:219], v236 offset:51200\n\t" \
  "s_waitcnt lgkmcnt(10)\n\t" \
  "v_mfma_f32_16x16x32_bf16 v[200:203], v[220:223], v[32:35], v[200:203]\n\t" \
  "v_mfma_f32_16x16x32_bf16 v[204:207], v[220:223], v[104:107], v[204:207]\n\t" \
  "ds_read_b64_tr_b16 v[220:221], v237 offset:49152\n\t" \
  "ds_read_b64_tr_b16 v[222:223], v237 offset:51200\n\t" \
  "s_waitcnt lgkmcnt(10)\n\t" \
  "v_mfma_f32_16x16x32_bf16 v[176:179], v[224:227], v[36:39], v[176:179]\n\t" \
  "v_mfma_f32_16x16x32_bf16 v[180:183], v[224:227], v[108:111], v[180:183]\n\t" \
  "ds_read_b64_tr_b16 v[224:225], v238 offset:49152\n\t" \
  "ds_read_b64_tr_b16 v[226:227], v238 offset:51200\n\t" \
  "s_waitcnt lgkmcnt(10)\n\t" \
  "v_mfma_f32_16x16x32_bf16 v[184:187], v[228:231], v[36:39], v[184:187]\n\t" \
  "v_mfma_f32_16x16x32_bf16 v[188:191], v[228:231], v[108:111], v[188:191]\n\t" \
  "ds_read_b64_tr_b16 v[228:229], v239 offset:49152\n\t" \
  "ds_read_b64_tr_b16 v[230:231], v239 offset:51200\n\t" \
  "s_waitcnt lgkmcnt(10)\n\t" \
  "v_mfma_f32_16x16x32_bf16 v[192:195], v[208:211], v[36:39], v[192:195]\n\t" \
  "v_mfma_f32_16x16x32_bf16 v[196:199], v[208:211], v[108:111], v[196:199]\n\t" \
  "ds_read_b64_tr_b16 v[208:209], v236 offset:53248\n\t" \
  "ds_read_b64_tr_b16 v[210:211], v236 offset:55296\n\t" \
  "s_waitcnt lgkmcnt(10)\n\t" \
  "v_mfma_f32_16x16x32_bf16 v[200:203], v[212:215], v[36:39], v[200:203]\n\t" \
  "v_mfma_f32_16x16x32_bf16 v[204:207], v[212:215], v[108:111], v[204:207]\n\t" \
  "ds_read_b64_tr_b16 v[212:213], v237 offset:53248\n\t" \
  "ds_read_b64_tr_b16 v[214:215], v237 offset:55296\n\t" \
  "s_waitcnt lgkmcnt(10)\n\t" \
  "v_mfma_f32_16x16x32_bf16 v[176:179], v[216:219], v[40:43], v[176:179]\n\t" \
  "v_mfma_f32_16x16x32_bf16 v[180:183], v[216:219], v[112:115], v[180:183]\n\t" \
  "ds_read_b64_tr_b16 v[216:217], v238 offset:53248\n\t" \
  "ds_read_b64_tr_b16 v[218:219], v238 offset:55296\n\t" \
  "s_waitcnt lgkmcnt(10)\n\t" \
  "v_mfma_f32_16x16x32_bf16 v[184:187], v[220:223], v[40:43], v[184:187]\n\t" \
  "v_mfma_f32_16x16x32_bf16 v[188:191], v[220:223], v[112:115], v[188:191]\n\t" \
  "ds_read_b64_tr_b16 v[220:221], v239 offset:53248\n\t" \
  "ds_read_b64_tr_b16 v[222:223], v239 offset:55296\n\t" \
  "s_waitcnt lgkmcnt(10)\n\t" \
  "v_mfma_f32_16x16x32_bf16 v[192:195], v[224:227], v[40:43], v[192:195]\n\t" \
  "v_mfma_f32_16x16x32_bf16 v[196:199], v[224:227], v[112:115], v[196:199]\n\t" \
  "buffer_store_dwordx4 v[164:167], %[qvo], s[64:67], 0 offen offset:2304 sc1\n\t" \
  "ds_read_b64_tr_b16 v[224:225], v236 offset:57344\n\t" \
  "ds_read_b64_tr_b16 v[226:227], v236 offset:59392\n\t" \
  "s_waitcnt lgkmcnt(10)\n\t" \
  "v_mfma_f32_16x16x32_bf16 v[200:203], v[228:231], v[40:43], v[200:203]\n\t" \
  "v_mfma_f32_16x16x32_bf16 v[204:207], v[228:231], v[112:115], v[204:207]\n\t" \
  "ds_read_b64_tr_b16 v[228:229], v237 offset:57344\n\t" \
  "ds_read_b64_tr_b16 v[230:231], v237 offset:59392\n\t" \
  "s_waitcnt lgkmcnt(10)\n\t" \
  "v_mfma_f32_16x16x32_bf16 v[176:179], v[208:211], v[44:47], v[176:179]\n\t" \
  "v_mfma_f32_16x16x32_bf16 v[180:183], v[208:211], v[116:119], v[180:183]\n\t" \
  "ds_read_b64_tr_b16 v[208:209], v238 offset:57344\n\t" \
  "ds_read_b64_tr_b16 v[210:211], v238 offset:59392\n\t" \
  "s_waitcnt lgkmcnt(10)\n\t" \
  "v_mfma_f32_16x16x32_bf16 v[184:187], v[212:215], v[44:47], v[184:187]\n\t" \
  "v_mfma_f32_16x16x32_bf16 v[188:191], v[212:215], v[116:119], v[188:191]\n\t" \
  "ds_read_b64_tr_b16 v[212:213], v239 offset:57344\n\t" \
  "ds_read_b64_tr_b16 v[214:215], v239 offset:59392\n\t" \
  "s_waitcnt lgkmcnt(10)\n\t" \
  "v_mfma_f32_16x16x32_bf16 v[192:195], v[216:219], v[44:47], v[192:195]\n\t" \
  "v_mfma_f32_16x16x32_bf16 v[196:199], v[216:219], v[116:119], v[196:199]\n\t" \
  "ds_read_b64_tr_b16 v[216:217], v236 offset:61440\n\t" \
  "ds_read_b64_tr_b16 v[218:219], v236 offset:63488\n\t" \
  "s_waitcnt lgkmcnt(10)\n\t" \
  "v_mfma_f32_16x16x32_bf16 v[200:203], v[220:223], v[44:47], v[200:203]\n\t" \
  "v_mfma_f32_16x16x32_bf16 v[204:207], v[220:223], v[116:119], v[204:207]\n\t" \
  "ds_read_b64_tr_b16 v[220:221], v237 offset:61440\n\t" \
  "ds_read_b64_tr_b16 v[222:223], v237 offset:63488\n\t" \
  "s_waitcnt lgkmcnt(10)\n\t" \
  "v_mfma_f32_16x16x32_bf16 v[176:179], v[224:227], v[48:51], v[176:179]\n\t" \
  "v_mfma_f32_16x16x32_bf16 v[180:183], v[224:227], v[120:123], v[180:183]\n\t" \
  "ds_read_b64_tr_b16 v[224:225], v238 offset:61440\n\t" \
  "ds_read_b64_tr_b16 v[226:227], v238 offset:63488\n\t" \
  "s_waitcnt lgkmcnt(10)\n\t" \
  "v_mfma_f32_16x16x32_bf16 v[184:187], v[228:231], v[48:51], v[184:187]\n\t" \
  "v_mfma_f32_16x16x32_bf16 v[188:191], v[228:231], v[120:123], v[188:191]\n\t" \
  "ds_read_b64_tr_b16 v[228:229], v239 offset:61440\n\t" \
  "ds_read_b64_tr_b16 v[230:231], v239 offset:63488\n\t" \
  "s_waitcnt lgkmcnt(10)\n\t" \
  "v_mfma_f32_16x16x32_bf16 v[192:195], v[208:211], v[48:51], v[192:195]\n\t" \
  "v_mfma_f32_16x16x32_bf16 v[196:199], v[208:211], v[120:123], v[196:199]\n\t" \
  "s_waitcnt vmcnt(4)\n\t" \
  "s_barrier\n\t" \
  "v_add_u32_e32 v236, 4294901760, v236\n\t" \
  "v_add_u32_e32 v237, 4294901760, v237\n\t" \
  "v_add_u32_e32 v238, 4294901760, v238\n\t" \
  "v_add_u32_e32 v239, 4294901760, v239\n\t" \
  "ds_read_b64_tr_b16 v[208:209], v236 offset:0\n\t" \
  "ds_read_b64_tr_b16 v[210:211], v236 offset:2048\n\t" \
  "s_waitcnt lgkmcnt(10)\n\t" \
  "v_mfma_f32_16x16x32_bf16 v[200:203], v[212:215], v[48:51], v[200:203]\n\t" \
  "v_mfma_f32_16x16x32_bf16 v[204:207], v[212:215], v[120:123], v[204:207]\n\t" \
  "ds_read_b64_tr_b16 v[212:213], v237 offset:0\n\t" \
  "ds_read_b64_tr_b16 v[214:215], v237 offset:2048\n\t" \
  "s_waitcnt lgkmcnt(10)\n\t" \
  "v_mfma_f32_16x16x32_bf16 v[176:179], v[216:219], v[52:55], v[176:179]\n\t" \
  "v_mfma_f32_16x16x32_bf16 v[180:183], v[216:219], v[124:127], v[180:183]\n\t" \
  "ds_read_b64_tr_b16 v[216:217], v238 offset:0\n\t" \
  "ds_read_b64_tr_b16 v[218:219], v238 offset:2048\n\t" \
  "s_waitcnt lgkmcnt(10)\n\t" \
  "v_mfma_f32_16x16x32_bf16 v[184:187], v[220:223], v[52:55], v[184:187]\n\t" \
  "v_mfma_f32_16x16x32_bf16 v[188:191], v[220:223], v[124:127], v[188:191]\n\t" \
  "ds_read_b64_tr_b16 v[220:221], v239 offset:0\n\t" \
  "ds_read_b64_tr_b16 v[222:223], v239 offset:2048\n\t" \
  "s_waitcnt lgkmcnt(10)\n\t" \
  "v_mfma_f32_16x16x32_bf16 v[192:195], v[224:227], v[52:55], v[192:195]\n\t" \
  "v_mfma_f32_16x16x32_bf16 v[196:199], v[224:227], v[124:127], v[196:199]\n\t" \
  "ds_read_b64_tr_b16 v[224:225], v236 offset:4096\n\t" \
  "ds_read_b64_tr_b16 v[226:227], v236 offset:6144\n\t" \
  "s_waitcnt lgkmcnt(10)\n\t" \
  "v_mfma_f32_16x16x32_bf16 v[200:203], v[228:231], v[52:55], v[200:203]\n\t" \
  "v_mfma_f32_16x16x32_bf16 v[204:207], v[228:231], v[124:127], v[204:207]\n\t" \
  "ds_read_b64_tr_b16 v[228:229], v237 offset:4096\n\t" \
  "ds_read_b64_tr_b16 v[230:231], v237 offset:6144\n\t" \
  "s_waitcnt lgkmcnt(10)\n\t" \
  "v_mfma_f32_16x16x32_bf16 v[176:179], v[208:211], v[56:59], v[176:179]\n\t" \
  "v_mfma_f32_16x16x32_bf16 v[180:183], v[208:211], v[128:131], v[180:183]\n\t" \
  "ds_read_b64_tr_b16 v[208:209], v238 offset:4096\n\t" \
  "ds_read_b64_tr_b16 v[210:211], v238 offset:6144\n\t" \
  "s_waitcnt lgkmcnt(10)\n\t" \
  "v_mfma_f32_16x16x32_bf16 v[184:187], v[212:215], v[56:59], v[184:187]\n\t" \
  "v_mfma_f32_16x16x32_bf16 v[188:191], v[212:215], v[128:131], v[188:191]\n\t" \
  "ds_read_b64_tr_b16 v[212:213], v239 offset:4096\n\t" \
  "ds_read_b64_tr_b16 v[214:215], v239 offset:6144\n\t" \
  "s_waitcnt lgkmcnt(10)\n\t" \
  "v_mfma_f32_16x16x32_bf16 v[192:195], v[216:219], v[56:59], v[192:195]\n\t" \
  "v_mfma_f32_16x16x32_bf16 v[196:199], v[216:219], v[128:131], v[196:199]\n\t" \
  "ds_read_b64_tr_b16 v[216:217], v236 offset:8192\n\t" \
  "ds_read_b64_tr_b16 v[218:219], v236 offset:10240\n\t" \
  "s_waitcnt lgkmcnt(10)\n\t" \
  "v_mfma_f32_16x16x32_bf16 v[200:203], v[220:223], v[56:59], v[200:203]\n\t" \
  "v_mfma_f32_16x16x32_bf16 v[204:207], v[220:223], v[128:131], v[204:207]\n\t" \
  "ds_read_b64_tr_b16 v[220:221], v237 offset:8192\n\t" \
  "ds_read_b64_tr_b16 v[222:223], v237 offset:10240\n\t" \
  "s_waitcnt lgkmcnt(10)\n\t" \
  "v_mfma_f32_16x16x32_bf16 v[176:179], v[224:227], v[60:63], v[176:179]\n\t" \
  "v_mfma_f32_16x16x32_bf16 v[180:183], v[224:227], v[132:135], v[180:183]\n\t" \
  "ds_read_b64_tr_b16 v[224:225], v238 offset:8192\n\t" \
  "ds_read_b64_tr_b16 v[226:227], v238 offset:10240\n\t" \
  "s_waitcnt lgkmcnt(10)\n\t" \
  "v_mfma_f32_16x16x32_bf16 v[184:187], v[228:231], v[60:63], v[184:187]\n\t" \
  "v_mfma_f32_16x16x32_bf16 v[188:191], v[228:231], v[132:135], v[188:191]\n\t" \
  "ds_read_b64_tr_b16 v[228:229], v239 offset:8192\n\t" \
  "ds_read_b64_tr_b16 v[230:231], v239 offset:10240\n\t" \
  "s_waitcnt lgkmcnt(10)\n\t" \
  "v_mfma_f32_16x16x32_bf16 v[192:195], v[208:211], v[60:63], v[192:195]\n\t" \
  "v_mfma_f32_16x16x32_bf16 v[196:199], v[208:211], v[132:135], v[196:199]\n\t" \
  "ds_read_b64_tr_b16 v[208:209], v236 offset:12288\n\t" \
  "ds_read_b64_tr_b16 v[210:211], v236 offset:14336\n\t" \
  "s_waitcnt lgkmcnt(10)\n\t" \
  "v_mfma_f32_16x16x32_bf16 v[200:203], v[212:215], v[60:63], v[200:203]\n\t" \
  "v_mfma_f32_16x16x32_bf16 v[204:207], v[212:215], v[132:135], v[204:207]\n\t" \
  "ds_read_b64_tr_b16 v[212:213], v237 offset:12288\n\t" \
  "ds_read_b64_tr_b16 v[214:215], v237 offset:14336\n\t" \
  "s_waitcnt lgkmcnt(10)\n\t" \
  "v_mfma_f32_16x16x32_bf16 v[176:179], v[216:219], v[64:67], v[176:179]\n\t" \
  "v_mfma_f32_16x16x32_bf16 v[180:183], v[216:219], v[136:139], v[180:183]\n\t" \
  "ds_read_b64_tr_b16 v[216:217], v238 offset:12288\n\t" \
  "ds_read_b64_tr_b16 v[218:219], v238 offset:14336\n\t" \
  "s_waitcnt lgkmcnt(10)\n\t" \
  "v_mfma_f32_16x16x32_bf16 v[184:187], v[220:223], v[64:67], v[184:187]\n\t" \
  "v_mfma_f32_16x16x32_bf16 v[188:191], v[220:223], v[136:139], v[188:191]\n\t" \
  "ds_read_b64_tr_b16 v[220:221], v239 offset:12288\n\t" \
  "ds_read_b64_tr_b16 v[222:223], v239 offset:14336\n\t" \
  "s_waitcnt lgkmcnt(10)\n\t" \
  "v_mfma_f32_16x16x32_bf16 v[192:195], v[224:227], v[64:67], v[192:195]\n\t" \
  "v_mfma_f32_16x16x32_bf16 v[196:199], v[224:227], v[136:139], v[196:199]\n\t" \
  "s_waitcnt lgkmcnt(8)\n\t" \
  "v_mfma_f32_16x16x32_bf16 v[200:203], v[228:231], v[64:67], v[200:203]\n\t" \
  "v_mfma_f32_16x16x32_bf16 v[204:207], v[228:231], v[136:139], v[204:207]\n\t" \
  "s_waitcnt lgkmcnt(6)\n\t" \
  "v_mfma_f32_16x16x32_bf16 v[176:179], v[208:211], v[68:71], v[176:179]\n\t" \
  "v_mfma_f32_16x16x32_bf16 v[180:183], v[208:211], v[140:143], v[180:183]\n\t" \
  "s_waitcnt lgkmcnt(4)\n\t" \
  "v_mfma_f32_16x16x32_bf16 v[184:187], v[212:215], v[68:71], v[184:187]\n\t" \
  "v_mfma_f32_16x16x32_bf16 v[188:191], v[212:215], v[140:143], v[188:191]\n\t" \
  "s_waitcnt lgkmcnt(2)\n\t" \
  "v_mfma_f32_16x16x32_bf16 v[192:195], v[216:219], v[68:71], v[192:195]\n\t" \
  "v_mfma_f32_16x16x32_bf16 v[196:199], v[216:219], v[140:143], v[196:199]\n\t" \
  "s_waitcnt lgkmcnt(0)\n\t" \
  "v_mfma_f32_16x16x32_bf16 v[200:203], v[220:223], v[68:71], v[200:203]\n\t" \
  "v_mfma_f32_16x16x32_bf16 v[204:207], v[220:223], v[140:143], v[204:207]\n\t" \
  "s_nop 7\n\t" \
  "v_mul_f32_e32 v176, s82, v176\n\t" \
  "v_mul_f32_e32 v177, s82, v177\n\t" \
  "v_mul_f32_e32 v178, s82, v178\n\t" \
  "v_mul_f32_e32 v179, s82, v179\n\t" \
  "v_mul_f32_e32 v180, s82, v180\n\t" \
  "v_mul_f32_e32 v181, s82, v181\n\t" \
  "v_mul_f32_e32 v182, s82, v182\n\t" \
  "v_mul_f32_e32 v183, s82, v183\n\t" \
  "v_mul_f32_e32 v184, s82, v184\n\t" \
  "v_mul_f32_e32 v185, s82, v185\n\t" \
  "v_mul_f32_e32 v186, s82, v186\n\t" \
  "v_mul_f32_e32 v187, s82, v187\n\t" \
  "v_mul_f32_e32 v188, s82, v188\n\t" \
  "v_mul_f32_e32 v189, s82, v189\n\t" \
  "v_mul_f32_e32 v190, s82, v190\n\t" \
  "v_mul_f32_e32 v191, s82, v191\n\t" \
  "v_mul_f32_e32 v192, s82, v192\n\t" \
  "v_mul_f32_e32 v193, s82, v193\n\t" \
  "v_mul_f32_e32 v194, s82, v194\n\t" \
  "v_mul_f32_e32 v195, s82, v195\n\t" \
  "v_mul_f32_e32 v196, s82, v196\n\t" \
  "v_mul_f32_e32 v197, s82, v197\n\t" \
  "v_mul_f32_e32 v198, s82, v198\n\t" \
  "v_mul_f32_e32 v199, s82, v199\n\t" \
  "v_mul_f32_e32 v200, s82, v200\n\t" \
  "v_mul_f32_e32 v201, s82, v201\n\t" \
  "v_mul_f32_e32 v202, s82, v202\n\t" \
  "v_mul_f32_e32 v203, s82, v203\n\t" \
  "v_mul_f32_e32 v204, s82, v204\n\t" \
  "v_mul_f32_e32 v205, s82, v205\n\t" \
  "v_mul_f32_e32 v206, s82, v206\n\t" \
  "v_mul_f32_e32 v207, s82, v207\n\t" \
  "v_cvt_pk_bf16_f32 v176, v176, v177\n\t" \
  "v_cvt_pk_bf16_f32 v177, v178, v179\n\t" \
  "v_cvt_pk_bf16_f32 v178, v184, v185\n\t" \
  "v_cvt_pk_bf16_f32 v179, v186, v187\n\t" \
  "v_cvt_pk_bf16_f32 v180, v180, v181\n\t" \
  "v_cvt_pk_bf16_f32 v181, v182, v183\n\t" \
  "v_cvt_pk_bf16_f32 v182, v188, v189\n\t" \
  "v_cvt_pk_bf16_f32 v183, v190, v191\n\t" \
  "v_cvt_pk_bf16_f32 v192, v192, v193\n\t" \
  "v_cvt_pk_bf16_f32 v193, v194, v195\n\t" \
  "v_cvt_pk_bf16_f32 v194, v200, v201\n\t" \
  "v_cvt_pk_bf16_f32 v195, v202, v203\n\t" \
  "v_cvt_pk_bf16_f32 v196, v196, v197\n\t" \
  "v_cvt_pk_bf16_f32 v197, v198, v199\n\t" \
  "v_cvt_pk_bf16_f32 v198, v204, v205\n\t" \
  "v_cvt_pk_bf16_f32 v199, v206, v207\n\t" \
  "s_nop 1\n\t" \
  "buffer_store_dwordx4 v[176:179], %[qvo], s[64:67], s81 offen offset:0 sc1\n\t" \
  "buffer_store_dwordx4 v[180:183], %[qvo], s[64:67], s81 offen offset:256 sc1\n\t" \
  "buffer_store_dwordx4 v[192:195], %[qvo], s[64:67], s81 offen offset:2048 sc1\n\t" \
  "buffer_store_dwordx4 v[196:199], %[qvo], s[64:67], s81 offen offset:2304 sc1\n\t" \
  ""
#define GEMM_QKV_CLOB "memory","vcc","scc","v0","v1","v2","v3","v4","v5","v6","v7","v8","v9","v10","v11","v12","v13","v14","v15","v16","v17","v18","v19","v20","v21","v22","v23","v24","v25","v26","v27","v28","v29","v30","v31","v32","v33","v34","v35","v36","v37","v38","v39","v40","v41","v42","v43","v44","v45","v46","v47","v48","v49","v50","v51","v52","v53","v54","v55","v56","v57","v58","v59","v60","v61","v62","v63","v64","v65","v66","v67","v68","v69","v70","v71","v72","v73","v74","v75","v76","v77","v78","v79","v80","v81","v82","v83","v84","v85","v86","v87","v88","v89","v90","v91","v92","v93","v94","v95","v96","v97","v98","v99","v100","v101","v102","v103","v104","v105","v106","v107","v108","v109","v110","v111","v112","v113","v114","v115","v116","v117","v118","v119","v120","v121","v122","v123","v124","v125","v126","v127","v128","v129","v130","v131","v132","v133","v134","v135","v136","v137","v138","v139","v140","v141","v142","v143","v144","v145","v146","v147","v148","v149","v150","v151","v152","v153","v154","v155","v156","v157","v158","v159","v160","v161","v162","v163","v164","v165","v166","v167","v168","v169","v170","v171","v172","v173","v174","v175","v176","v177","v178","v179","v180","v181","v182","v183","v184","v185","v186","v187","v188","v189","v190","v191","v192","v193","v194","v195","v196","v197","v198","v199","v200","v201","v202","v203","v204","v205","v206","v207","v208","v209","v210","v211","v212","v213","v214","v215","v216","v217","v218","v219","v220","v221","v222","v223","v224","v225","v226","v227","v228","v229","v230","v231","v232","v233","v234","v235","v236","v237","v238","v239","v240","v241","v242","v243","v244","v245","v246","v247","s36","s37","s38","s39","s40","s41","s42","s43","s44","s45","s46","s47","s48","s49","s50","s51","s52","s53","s54","s55","s56","s57","s58","s59","s60","s61","s62","s63","s64","s65","s66","s67","s68","s69","s70","s71","s72","s73","s74","s75","s76","s77","s78","s79","s80","s81","s82","s83"
__device__ __forceinline__ void st16p(void* p, u32x4 v){ *(u32x4*)p=v; }
extern "C" __global__ void k_qkv(); extern "C" __global__ void attn_kernel(); extern "C" __global__ void k_oproj(); extern "C" __global__ void k_ffn1(); extern "C" __global__ void k_ffn2();
extern "C" __global__ __launch_bounds__(320) void k_prep(const char* __restrict__ x, const char* __restrict__ ckv, const char* __restrict__ wq, const char* __restrict__ wuk, const char* __restrict__ wuv,
                                              const char* wo, const char* w1, const char* w2, char* wsb, char* outp){
  __shared__ float red[2][4][16];
  const int lane=threadIdx.x&63, wid=threadIdx.x>>6, row=lane&15, kg=lane>>4;
  const int b=blockIdx.x;
  if(wid==4){
    if(b<128) return;
    const int bb=b-128, xg=bb&7, ix=bb>>3;
    const char* ad[15];
#pragma unroll
    for(int t=0;t<13;++t){
      int i=ix*13+t; i=i<200?i:199;
      const int r=i*8+(lane>>3);
      const char* base= r<512? wuk+(size_t)r*8192 : (r<1024? wuv+(size_t)(r-512)*8192 : wq+(size_t)(r-1024)*8192);
      ad[t]=base+xg*1024+(lane&7)*128;
    }
    {
      uint64_t a; int len; const int k=ix&7, half=ix>>3;
      if(k==0||k>=5){ CODE_ADDR(k_qkv,a); len=CODE_QKV; } else if(k==1){ CODE_ADDR(attn_kernel,a); len=CODE_ATTN; } else if(k==2){ CODE_ADDR(k_oproj,a); len=CODE_OPROJ; }
      else if(k==3){ CODE_ADDR(k_ffn1,a); len=CODE_FFN1; } else { CODE_ADDR(k_ffn2,a); len=CODE_FFN2; }
#pragma unroll
      for(int t=0;t<2;++t){ int off=(half*2+t)*8192+lane*128; off=off<len?off:0; ad[13+t]=(const char*)a+off; }
    }
    unsigned r[15];
#pragma unroll
    for(int t=0;t<15;++t) r[t]=prefetch_line(ad[t]);
    if(b==128 && lane==0){ g_tab.wuk=wuk; g_tab.wuv=wuv; g_tab.wq=wq; g_tab.wo=wo; g_tab.w1=w1; g_tab.w2=w2; g_tab.ws=wsb; g_tab.out=outp; }
    if(b==129 && lane<NCNT) *(unsigned*)(wsb+WS_CNT+lane*128)=0u;
    asm volatile("s_waitcnt vmcnt(0)" : "+v"(r[0]),"+v"(r[1]),"+v"(r[2]),"+v"(r[3]),"+v"(r[4]),"+v"(r[5]),"+v"(r[6]),"+v"(r[7]),
                 "+v"(r[8]),"+v"(r[9]),"+v"(r[10]),"+v"(r[11]),"+v"(r[12]),"+v"(r[13]),"+v"(r[14]) :: "memory");
    return;
  }
  if(b<128){
    const int rb=b, ns=wid<2?5:4;
    const char* src=x+(size_t)(rb*16+row)*1152+kg*8+wid*64;
    u32x2 lo[5],hi[5];
#pragma unroll
    for(int c=0;c<5;++c){
      const int cc=(c<4||wid<2)?c:0;
      lo[c]=*(const u32x2*)(src+cc*256); hi[c]=*(const u32x2*)(src+cc*256+32);
    }
    if(ns<5){ asm volatile("" : "+v"(lo[4]), "+v"(hi[4])); lo[4]=(u32x2){0u,0u}; hi[4]=(u32x2){0u,0u}; }
    float h[40];
#pragma unroll
    for(int c=0;c<5;++c){ h[8*c]=bflo(lo[c][0]); h[8*c+1]=bfhi(lo[c][0]); h[8*c+2]=bflo(lo[c][1]); h[8*c+3]=bfhi(lo[c][1]);
                          h[8*c+4]=bflo(hi[c][0]); h[8*c+5]=bfhi(hi[c][0]); h[8*c+6]=bflo(hi[c][1]); h[8*c+7]=bfhi(hi[c][1]); }
    float s=0.f;
#pragma unroll
    for(int i=0;i<40;++i) s+=h[i];
    s+=__shfl_xor(s,16); s+=__shfl_xor(s,32);
    if(lane<16) red[0][wid][lane]=s;
    lds_barrier();
    const float mean=((red[0][0][row]+red[0][1][row])+(red[0][2][row]+red[0][3][row]))*(1.0f/576.0f);
    float q=0.f;
#pragma unroll
    for(int c=0;c<5;++c) if(c<ns){
#pragma unroll
      for(int i=0;i<8;++i){ const float d=h[8*c+i]-mean; q+=d*d; } }
    q+=__shfl_xor(q,16); q+=__shfl_xor(q,32);
    if(lane<16) red[1][wid][lane]=q;
    lds_barrier();
    const float var=(red[1][0][row]+red[1][1][row])+(red[1][2][row]+red[1][3][row]);
    const float rs=1.0f/sqrtf(var*(1.0f/576.0f)+1e-5f);
#pragma unroll
    for(int c=0;c<5;++c) if(c<ns){
      const size_t off=((size_t)rb*18+wid+4*c)*1024+lane*16;
      st16p(wsb+WS_LN1+off,(u32x4){cvtpk((h[8*c]-mean)*rs,(h[8*c+1]-mean)*rs),cvtpk((h[8*c+2]-mean)*rs,(h[8*c+3]-mean)*rs),cvtpk((h[8*c+4]-mean)*rs,(h[8*c+5]-mean)*rs),cvtpk((h[8*c+6]-mean)*rs,(h[8*c+7]-mean)*rs)});
      st16p(wsb+WS_XAF+off,(u32x4){lo[c][0],lo[c][1],hi[c][0],hi[c][1]});
    }
  } else {
    const int rb=b-128, s0=wid*4;
    const char* src=ckv+(size_t)(rb*16+row)*1024+kg*8+s0*64;
    u32x2 lo[4],hi[4];
#pragma unroll
    for(int c=0;c<4;++c){ lo[c]=*(const u32x2*)(src+c*64); hi[c]=*(const u32x2*)(src+c*64+32); }
    char* d=wsb+WS_CKV+((size_t)rb*16+s0)*1024+lane*16;
#pragma unroll
    for(int c=0;c<4;++c) st16p(d+c*1024,(u32x4){lo[c][0],lo[c][1],hi[c][0],hi[c][1]});
  }
}
extern "C" __global__ __launch_bounds__(512,2) void k_qkv(){
  extern __shared__ __attribute__((aligned(16))) char lds_[];
  const PtrTab tab=load_tab(); char* const wsb=tab.ws;
  const int tid=threadIdx.x, lane=tid&63, wid=__builtin_amdgcn_readfirstlane(tid>>6);
  const int b=blockIdx.x, x=b&7, idx=b>>3, head=x*4+(idx>>3), rt=idx&7;
  const unsigned lds0=(unsigned)(size_t)(LDS3 char*)lds_;
  const unsigned row=(wid&3)*8+(lane>>3), pos=lane&7;
  const unsigned wvo=row*8192+((((pos>>1)^((row>>1)&3))<<1)+(pos&1))*16;
  const unsigned avo=lane*16;
  const unsigned g=lane>>4, i=lane&15, q=i>>2;
  const unsigned trb=lds0+(g*4+q)*128+(lane&3)*8, trx=2*(g&1)+(q>>1);
  const unsigned kvo=(g>>1)*1024+((g&1)*32+16*(i>>3)+(i&7))*16;
  const unsigned qvo=(g>>1)*1024+((g&1)*32+i)*16;
  const unsigned vvo=(g>>1)*4096+((g&1)*32+i)*16;
  const char* wk=tab.wuk+head*256; const char* wv=tab.wuv+head*256; const char* wq=tab.wq+head*256;
  const char* ck=wsb+WS_CKV+(size_t)(rt*16+2*wid)*16*1024; const char* l1=wsb+WS_LN1+(size_t)(rt*16+2*wid)*18*1024;
  const size_t go=(size_t)(head*64+rt*8+wid)*8192;
  char* ko=wsb+WS_K+go; char* vo=wsb+WS_V+go; char* qo=wsb+WS_Q+go;
  asm volatile(GEMM_QKV_TEXT ::
     [wvo]"v"(wvo),[avo]"v"(avo),[trb]"v"(trb),[trx]"v"(trx),[kvo]"v"(kvo),[qvo]"v"(qvo),[vvo]"v"(vvo),
     [wid]"s"(wid),[lds0]"s"(lds0),[wk]"s"(wk),[wv]"s"(wv),[wq]"s"(wq),[ckv]"s"(ck),[ln1]"s"(l1),[ko]"s"(ko),[vo]"s"(vo),[qo]"s"(qo) : GEMM_QKV_CLOB);
}
#define GEMMA_BODY(TEXT,...) \
  const int tid=threadIdx.x, lane=tid&63; \
  const unsigned lds0=(unsigned)(size_t)(LDS3 char*)lds_; \
  const unsigned pi0=wid*64+lane, pi8=512+lane; \
  const unsigned wvo0=(pi0/18)*wpitch+(pi0%18)*16, wvo8=(pi8/18)*wpitch+(pi8%18)*16; \
  const unsigned avo=lane*16; \
  const unsigned tro=lds0+((lane>>4)*4+((lane&15)>>2))*288+(lane&3)*8; \
  asm volatile(TEXT :: [wvo0]"v"(wvo0),[wvo8]"v"(wvo8),[avo]"v"(avo),[tro]"v"(tro), \
    [wid]"s"(wid),[lds0]"s"(lds0),[wpitch]"s"(wpitch),[ars]"s"(ars),[ors]"s"(ors),[par]"s"(par),[wptr]"s"(wptr),[aptr]"s"(aptr),[optr]"s"(optr) : __VA_ARGS__);
__device__ __forceinline__ u32x4 ldp(__amdgpu_buffer_rsrc_t r, unsigned off){ return __builtin_bit_cast(u32x4,__builtin_amdgcn_raw_buffer_load_b128(r,off,0,16)); }

extern "C" __global__ __launch_bounds__(512,2) void k_oproj(){
  extern __shared__ __attribute__((aligned(16))) char lds_[];
  const int wid=__builtin_amdgcn_readfirstlane(threadIdx.x>>6);
  {
    const PtrTab tab=load_tab(); char* const wsb=tab.ws;
    const int b=blockIdx.x, x=b&7, idx=b>>3, rt=idx>>2, ct=idx&3;
    const char* aptr=wsb+WS_CTX+((size_t)rt*16*128+16*x)*1024; const unsigned ars=128*1024;
    const char* wptr=tab.wo+(size_t)512*x*1152+ct*288; const unsigned wpitch=1152;
    char* optr=wsb+WS_P+(((size_t)x*128+rt*16)*18+(9*ct>>1))*1024; const unsigned ors=18*1024; const int par=ct&1;
    GEMMA_BODY(GEMM_P16_TEXT,GEMM_P16_CLOB)
  }
  const PtrTab tab=load_tab(); char* const wsb=tab.ws;
  const int b=blockIdx.x, rt=b>>5, lane=lane_id();
  const int q=(b&7)*4+((b>>3)&3), rb=rt*16+(q>>1), row=lane&15;
  const bool act=(row>>3)==(q&1);
  const int ns=wid<2?3:2;
  const __amdgpu_buffer_rsrc_t rp=__builtin_amdgcn_make_buffer_rsrc((void*)(wsb+WS_P),(short)0,(int)(8*AFB),0x00020000);
  arrive_and_wait((unsigned*)(wsb+WS_CNT+rt*128),32u,wid,lane);
  const __amdgpu_buffer_rsrc_t rx=__builtin_amdgcn_make_buffer_rsrc((void*)(wsb+WS_XAF),(short)0,(int)AFB,0x00020000);
  u32x4 pv[3][8], xv[3];
#pragma unroll
  for(int c=0;c<3;++c){
    const unsigned off=(act && c<ns)? (unsigned)((rb*18+wid+8*c)*1024+lane*16) : 0x80000000u;
#pragma unroll
    for(int p=0;p<8;++p) pv[c][p]=ldp(rp,off+p*(unsigned)AFB);
    xv[c]=__builtin_bit_cast(u32x4,__builtin_amdgcn_raw_buffer_load_b128(rx,off,0,0));
  }
  float h[24]; u32x4 hw[3];
#pragma unroll
  for(int c=0;c<3;++c){
#pragma unroll
    for(int j=0;j<4;++j){
      float a=0.f,bb=0.f;
#pragma unroll
      for(int p=0;p<8;++p){ a+=bflo(pv[c][p][j]); bb+=bfhi(pv[c][p][j]); }
      hw[c][j]=cvtpk(a+bflo(xv[c][j]),bb+bfhi(xv[c][j]));
      h[c*8+2*j]=bflo(hw[c][j]); h[c*8+2*j+1]=bfhi(hw[c][j]);
    }
  }
  float* red=(float*)lds_;
  float s=0.f;
#pragma unroll
  for(int i=0;i<24;++i) s+=h[i];
  s+=__shfl_xor(s,16); s+=__shfl_xor(s,32);
  if(lane<16) red[wid*16+lane]=s;
  lds_barrier();
  float mean=0.f;
#pragma unroll
  for(int w=0;w<8;++w) mean+=red[w*16+row];
  mean*=(1.0f/576.0f);
  float qq=0.f;
#pragma unroll
  for(int c=0;c<3;++c) if(c<ns){
#pragma unroll
    for(int i=0;i<8;++i){ const float d=h[c*8+i]-mean; qq+=d*d; } }
  qq+=__shfl_xor(qq,16); qq+=__shfl_xor(qq,32);
  if(lane<16) red[128+wid*16+lane]=qq;
  lds_barrier();
  float var=0.f;
#pragma unroll
  for(int w=0;w<8;++w) var+=red[128+w*16+row];
  const float rs=1.0f/sqrtf(var*(1.0f/576.0f)+1e-5f);
#pragma unroll
  for(int c=0;c<3;++c) if(act && c<ns){
    u32x4 w;
#pragma unroll
    for(int j=0;j<4;++j) w[j]=cvtpk((h[c*8+2*j]-mean)*rs,(h[c*8+2*j+1]-mean)*rs);
    st16(wsb+WS_LN2+(size_t)((rb*18+wid+8*c)*1024+lane*16),w);
    st16(wsb+WS_H+(size_t)((rb*18+wid+8*c)*1024+lane*16),hw[c]);
  }
}
extern "C" __global__ __launch_bounds__(512,2) void k_ffn1(){
  extern __shared__ __attribute__((aligned(16))) char lds_[];
  const int wid=__builtin_amdgcn_readfirstlane(threadIdx.x>>6);
  const PtrTab tab=load_tab(); char* const wsb=tab.ws;
  const int b=blockIdx.x, x=b&7, idx=b>>3, rt=idx>>2, ct=x*4+(idx&3);
  const char* aptr=wsb+WS_LN2+(size_t)rt*16*18*1024; const unsigned ars=18*1024;
  const char* wptr=tab.w1+ct*288; const unsigned wpitch=9216;
  char* optr=wsb+WS_H1+((size_t)rt*16*144+(9*ct>>1))*1024; const unsigned ors=144*1024; const int par=ct&1;
  GEMMA_BODY(GEMM_FFN1_TEXT,GEMM_FFN1_CLOB)
}
extern "C" __global__ __launch_bounds__(512,2) void k_ffn2(){
  extern __shared__ __attribute__((aligned(16))) char lds_[];
  const int wid=__builtin_amdgcn_readfirstlane(threadIdx.x>>6);
  {
    const PtrTab tab=load_tab(); char* const wsb=tab.ws;
    const int b=blockIdx.x, x=b&7, idx=b>>3, rt=idx>>2, ct=idx&3;
    const char* aptr=wsb+WS_H1+((size_t)rt*16*144+18*x)*1024; const unsigned ars=144*1024;
    const char* wptr=tab.w2+(size_t)576*x*1152+ct*288; const unsigned wpitch=1152;
    char* optr=wsb+WS_P2+(((size_t)x*128+rt*16)*18+(9*ct>>1))*1024; const unsigned ors=18*1024; const int par=ct&1;
    GEMMA_BODY(GEMM_P18_TEXT,GEMM_P18_CLOB)
  }
  const PtrTab tab=load_tab(); char* const wsb=tab.ws; char* const out=tab.out;
  const int b=blockIdx.x, x=b&7, rt=b>>5, ct=(b>>3)&3, lane=lane_id();
  arrive_and_wait((unsigned*)(wsb+WS_CNT+(8+rt*4+ct)*128),8u,wid,lane);
  const __amdgpu_buffer_rsrc_t rp=__builtin_amdgcn_make_buffer_rsrc((void*)(wsb+WS_P2),(short)0,(int)(8*AFB),0x00020000);
  const int par=ct&1, c0=(9*ct)>>1, row=lane&15, kg=lane>>4;
  const __amdgpu_buffer_rsrc_t rh=__builtin_amdgcn_make_buffer_rsrc((void*)(wsb+WS_H),(short)0,(int)AFB,0x00020000);
  u32x4 pv[2][8], hv[2];
#pragma unroll
  for(int t=0;t<2;++t){
    const int u=wid+8*t, rb=rt*16+2*x+(u>=5?1:0), i=u>=5?u-5:u;
    const unsigned off=(u<10)? (unsigned)((rb*18+c0+i)*1024+lane*16) : 0x80000000u;
#pragma unroll
    for(int p=0;p<8;++p) pv[t][p]=ldp(rp,off+p*(unsigned)AFB);
    hv[t]=__builtin_bit_cast(u32x4,__builtin_amdgcn_raw_buffer_load_b128(rh,off,0,0));
  }
#pragma unroll
  for(int t=0;t<2;++t){
    const int u=wid+8*t;
    if(u<10){
      const int rb=rt*16+2*x+(u>=5?1:0), i=u>=5?u-5:u, s=c0+i;
      u32x4 w;
#pragma unroll
      for(int j=0;j<4;++j){
        float a=0.f,bb=0.f;
#pragma unroll
        for(int p=0;p<8;++p){ a+=bflo(pv[t][p][j]); bb+=bfhi(pv[t][p][j]); }
        w[j]=cvtpk(a+bflo(hv[t][j]),bb+bfhi(hv[t][j]));
      }
      char* d=out+(size_t)(rb*16+row)*1152+s*64+kg*8;
      if(!(par==1 && i==0)) *(u32x2*)d=(u32x2){w[0],w[1]};
      if(!(par==0 && i==4)) *(u32x2*)(d+32)=(u32x2){w[2],w[3]};
    }
  }
}
constexpr int AT_LDS_BYTES=131072;
__device__ __forceinline__ void attn_slow(const char* __restrict__ Qf, const char* __restrict__ Kf, const char* __restrict__ Vf, char* __restrict__ Cf, int head, int qblk, int r){
  const int lane=lane_id(), qcol=lane&31, hh=lane>>5;
  const char* qp=Qf+((size_t)(head*64+qblk*4+r)*8)*1024+lane*16;
  const char* kp=Kf+(size_t)head*524288+lane*16; const char* vp=Vf+(size_t)head*524288+lane*16;
  f32x16 o0={},o1={},o2={},o3={}; float m=-1e30f,l=0.f;
  const int N=4*qblk+r+1;
  for(int n=0;n<N;++n){
    f32x16 S={};
#pragma unroll
    for(int s=0;s<8;++s) S=__builtin_amdgcn_mfma_f32_32x32x16_bf16(*(const bf16x8*)(kp+(size_t)n*8192+s*1024),*(const bf16x8*)(qp+s*1024),S,0,0,0);
    if(n==N-1){
#pragma unroll
      for(int i=0;i<16;++i){ const int c=16*((i>>2)&1)+8*(i>>3)+(i&3); if(c>qcol-4*hh) S[i]=-__builtin_inff(); }
    }
    float t=S[0];
#pragma unroll
    for(int i=1;i<16;++i) t=fmaxf(t,S[i]);
    t=fmaxf(t,__shfl_xor(t,32));
    const float mn=fmaxf(m,t), al=__builtin_amdgcn_exp2f(m-mn); m=mn;
    float ps=0.f;
#pragma unroll
    for(int i=0;i<16;++i){ S[i]=__builtin_amdgcn_exp2f(S[i]-mn); ps+=S[i]; }
    l=l*al+ps;
    o0*=al; o1*=al; o2*=al; o3*=al;
#pragma unroll
    for(int t2=0;t2<2;++t2){
      const u32x4 pw={cvtpk(S[8*t2],S[8*t2+1]),cvtpk(S[8*t2+2],S[8*t2+3]),cvtpk(S[8*t2+4],S[8*t2+5]),cvtpk(S[8*t2+6],S[8*t2+7])};
      const bf16x8 pf=__builtin_bit_cast(bf16x8,pw);
      const char* v=vp+(size_t)n*8192+t2*4096;
      o0=__builtin_amdgcn_mfma_f32_32x32x16_bf16(*(const bf16x8*)(v),pf,o0,0,0,0);
      o1=__builtin_amdgcn_mfma_f32_32x32x16_bf16(*(const bf16x8*)(v+1024),pf,o1,0,0,0);
      o2=__builtin_amdgcn_mfma_f32_32x32x16_bf16(*(const bf16x8*)(v+2048),pf,o2,0,0,0);
      o3=__builtin_amdgcn_mfma_f32_32x32x16_bf16(*(const bf16x8*)(v+3072),pf,o3,0,0,0);
    }
  }
  l+=__shfl_xor(l,32);
  const float inv=1.0f/l;
  char* cp=Cf+((size_t)(qblk*8+2*r+(qcol>>4))*128+head*4)*1024+(hh*16+(qcol&15))*16;
#define ATTN_SLOW_ST(O,DB) { \
    *(u32x4*)(cp+DB*1024)    =(u32x4){cvtpk(O[0]*inv,O[1]*inv),cvtpk(O[2]*inv,O[3]*inv),cvtpk(O[8]*inv,O[9]*inv),cvtpk(O[10]*inv,O[11]*inv)}; \
    *(u32x4*)(cp+DB*1024+512)=(u32x4){cvtpk(O[4]*inv,O[5]*inv),cvtpk(O[6]*inv,O[7]*inv),cvtpk(O[12]*inv,O[13]*inv),cvtpk(O[14]*inv,O[15]*inv)}; }
  ATTN_SLOW_ST(o0,0) ATTN_SLOW_ST(o1,1) ATTN_SLOW_ST(o2,2) ATTN_SLOW_ST(o3,3)
#undef ATTN_SLOW_ST
}
extern "C" __global__ __launch_bounds__(512,2) void attn_kernel(){
  extern __shared__ __attribute__((aligned(16))) char lds_[];
  const int wid=__builtin_amdgcn_readfirstlane(threadIdx.x>>6);
  unsigned flag;
  {
    const PtrTab tab=load_tab(); char* const wsb=tab.ws; const char* Qf=wsb+WS_Q; const char* Kf=wsb+WS_K; const char* Vf=wsb+WS_V; char* Cf=wsb+WS_CTX;
    const int b=blockIdx.x, head=(b&7)*4+(b>>6), pair=(b>>3)&7;
    const unsigned lds0=(unsigned)(size_t)(LDS3 char*)lds_;
    const int x=b&7, lane=threadIdx.x&63, u=(b>>3)*4+(wid&3);
    const char* ad[2];
#pragma unroll
    for(int t=0;t<2;++t){
      int L=(u*2+t)*64+lane; L=L<14976?L:14975;
      const int M=L-9792, r=M/9, c=M-r*9;
      ad[t]= L<4608? tab.wo+(size_t)x*589824+(size_t)L*128 : (L<9792? tab.w2+(size_t)x*663552+(size_t)(L-4608)*128 : tab.w1+(size_t)r*9216+x*1152+c*128);
    }
    uint64_t ca; int clen; const int k=u%6;
    if(k<2){ CODE_ADDR(k_oproj,ca); clen=CODE_OPROJ; } else if(k<4){ CODE_ADDR(k_ffn1,ca); clen=CODE_FFN1; } else { CODE_ADDR(k_ffn2,ca); clen=CODE_FFN2; }
    int co=(k&1)*8192+lane*128; co=co<clen?co:0;
    const char* pf2=(const char*)ca+co;
    asm volatile(ATTN_ASM_TEXT : [flag]"=s"(flag) : [qf]"s"(Qf),[kf]"s"(Kf),[vf]"s"(Vf),[cf]"s"(Cf),[head]"s"(head),[pair]"s"(pair),[wid]"s"(wid),[lds0]"s"(lds0),
                 [pf0]"v"(ad[0]),[pf1]"v"(ad[1]),[pf2]"v"(pf2) : ATTN_ASM_CLOBBERS);
  }
  if(flag){
    asm volatile("s_waitcnt vmcnt(0)" ::: "memory");
    const int b=blockIdx.x;
    const PtrTab tab=load_tab(); char* const wsb=tab.ws; const int pair=(b>>3)&7;
    attn_slow(wsb+WS_Q,wsb+WS_K,wsb+WS_V,wsb+WS_CTX,(b&7)*4+(b>>6),(wid>>2)?pair:15-pair,wid&3);
  }
}
extern "C" void kernel_launch(void* const* d_in, const int* in_sizes, int n_in, void* d_out, int out_size, void* d_ws, size_t ws_size, hipStream_t stream){
  (void)in_sizes; (void)n_in; (void)out_size; (void)ws_size;
  (void)hipFuncSetAttribute((const void*)k_qkv,hipFuncAttributeMaxDynamicSharedMemorySize,131072);
  (void)hipFuncSetAttribute((const void*)attn_kernel,hipFuncAttributeMaxDynamicSharedMemorySize,131072);
  (void)hipFuncSetAttribute((const void*)k_ffn1,hipFuncAttributeMaxDynamicSharedMemorySize,156672);
  (void)hipFuncSetAttribute((const void*)k_ffn2,hipFuncAttributeMaxDynamicSharedMemorySize,156672);
  (void)hipFuncSetAttribute((const void*)k_oproj,hipFuncAttributeMaxDynamicSharedMemorySize,147456);
  k_prep<<<256,320,0,stream>>>((const char*)d_in[0],(const char*)d_in[1],(const char*)d_in[2],(const char*)d_in[3],(const char*)d_in[4],
                               (const char*)d_in[5],(const char*)d_in[7],(const char*)d_in[8],(char*)d_ws,(char*)d_out);
  k_qkv<<<256,512,131072,stream>>>();
  attn_kernel<<<256,512,131072,stream>>>();
  k_oproj<<<256,512,147456,stream>>>();
  k_ffn1<<<256,512,156672,stream>>>();
  k_ffn2<<<256,512,156672,stream>>>();
}
```
